```python
import torch

def _softmax(x, axis=-1):
    return torch.softmax(x, dim=axis)
def _gelu(x):
    return torch.nn.functional.gelu(x, approximate="tanh")
def _silu(x):
    return torch.nn.functional.silu(x)
def _sigmoid(x):
    return torch.sigmoid(x)
def _layernorm(x, axis=-1, eps=1e-5):
    m = x.mean(dim=axis, keepdim=True)
    v = x.var(dim=axis, keepdim=True, unbiased=False)
    return (x - m) / torch.sqrt(v + eps)
def _scatter_add(tgt, idx, src, axis=0):
    return tgt.clone().index_add_(axis, idx, src)
def _segment_sum(src, seg, n):
    out = torch.zeros((n,) + src.shape[1:], dtype=src.dtype)
    return out.index_add_(0, seg, src)
def _pad(x, pw):
    flat = tuple(v for pair in reversed(pw) for v in pair)
    return torch.nn.functional.pad(x, flat)

kernel_name = 'interleave_layers__linear_kernel_hedgehog__geglu_ffn__prompt'

def setup_inputs():

    _w = (lambda a: (lambda t: t.to(torch.bfloat16) if t.is_floating_point() else t)(torch.as_tensor(a)))
    def _A_setup():
        B, S, D, H = 1, 2048, 512, 32
        Dh = D // H
        return {
            "x": _w((lambda *s: torch.randn(*s, dtype=torch.bfloat16) * 0.05)(B, S, D)),
            "wqkv": _w((lambda *s: torch.randn(*s, dtype=torch.bfloat16) * 0.2)(D, 3 * D)),
            "w_phi": _w((lambda *s: torch.randn(*s, dtype=torch.bfloat16) * 0.2)(H, Dh, Dh) * (Dh ** -0.5)),
            "wo": _w((lambda *s: torch.randn(*s, dtype=torch.bfloat16) * 0.05)(D, D)),
        }
    _A_in = _A_setup()

    def _B_setup():
        B, S, D, F = 1, 2048, 512, 1376
        return {
            "x": _w((lambda *s: torch.randn(*s, dtype=torch.bfloat16) * 0.05)(B, S, D)),
            "w_gate": _w((lambda *s: torch.randn(*s, dtype=torch.bfloat16) * 0.05)(D, F)),
            "w_up": _w((lambda *s: torch.randn(*s, dtype=torch.bfloat16) * 0.05)(D, F)),
            "w_down": _w((lambda *s: torch.randn(*s, dtype=torch.bfloat16) * 0.05)(F, D)),
        }
    _B_in = _B_setup()

    return {
        "A_x": _A_in["x"],
        "A_wqkv": _A_in["wqkv"],
        "A_w_phi": _A_in["w_phi"],
        "A_wo": _A_in["wo"],
        "B_x": _B_in["x"],
        "B_w_gate": _B_in["w_gate"],
        "B_w_up": _B_in["w_up"],
        "B_w_down": _B_in["w_down"],
    }

def reference(A_x, A_wqkv, A_w_phi, A_wo, B_x, B_w_gate, B_w_up, B_w_down):
    def _A_ref(x, wqkv, w_phi, wo):
        B, S, D = x.shape
        H = 32; Dh = D // H
        qkv = (lambda x, *d: x.permute(*d))((x @ wqkv).reshape(B, S, 3, H, Dh), 2, 0, 3, 1, 4)
        q, k, v = qkv[0], qkv[1], qkv[2]
        pq = torch.einsum("bhsd,hdf->bhsf", q, w_phi)
        pk = torch.einsum("bhsd,hdf->bhsf", k, w_phi)
        phi_q = torch.concatenate([_softmax(pq, axis=-1), _softmax(-pq, axis=-1)], axis=-1)
        phi_k = torch.concatenate([_softmax(pk, axis=-1), _softmax(-pk, axis=-1)], axis=-1)
        kv = torch.einsum("bhsf,bhsd->bhfd", phi_k, v)
        z  = torch.einsum("bhsf->bhf", phi_k)
        num = torch.einsum("bhqf,bhfd->bhqd", phi_q, kv)
        den = torch.einsum("bhqf,bhf->bhq", phi_q, z)[..., None] + 1e-6
        o = num / den
        return (lambda x, *d: x.permute(*d))(o, 0, 2, 1, 3).reshape(B, S, D) @ wo

    def _B_ref(x, w_gate, w_up, w_down):
        return (_gelu(x @ w_gate) * (x @ w_up)) @ w_down

    x = A_x
    x = x + _A_ref(_layernorm(x, axis=-1), A_wqkv, A_w_phi, A_wo)
    x = x + _B_ref(_layernorm(x, axis=-1), B_w_gate, B_w_up, B_w_down)
    x = x + _A_ref(_layernorm(x, axis=-1), A_wqkv, A_w_phi, A_wo)
    x = x + _B_ref(_layernorm(x, axis=-1), B_w_gate, B_w_up, B_w_down)
    return x
```

```cpp
#include <hip/hip_runtime.h>
#include <hip/hip_bf16.h>
#include <stdint.h>

typedef unsigned short u16;
constexpr int S = 2048, D = 512, H = 32, DH = 16, F = 1376;

__device__ __forceinline__ float bf2f(u16 b) { return __builtin_bit_cast(float, (unsigned)b << 16); }
__device__ __forceinline__ u16 f2bf(float f) { __bf16 h = (__bf16)f; return __builtin_bit_cast(u16, h); }
__device__ __forceinline__ float rbf(float f) { return bf2f(f2bf(f)); }

__global__ void k_ln(const u16* x, u16* y) {
  int row = blockIdx.x, t = threadIdx.x;
  float v[8]; float s = 0;
  for (int i = 0; i < 8; ++i) { v[i] = bf2f(x[row * D + t * 8 + i]); s += v[i]; }
  for (int o = 32; o; o >>= 1) s += __shfl_xor(s, o);
  float m = s / D; float q = 0;
  for (int i = 0; i < 8; ++i) { float d = v[i] - m; q += d * d; }
  for (int o = 32; o; o >>= 1) q += __shfl_xor(q, o);
  float r = rsqrtf(q / D + 1e-5f);
  for (int i = 0; i < 8; ++i) y[row * D + t * 8 + i] = f2bf((v[i] - m) * r);
}

__global__ void k_gemm(const u16* A, const u16* B, const u16* res, u16* C, int N, int K) {
  __shared__ float As[16][33], Bs[32][17];
  int tx = threadIdx.x & 15, ty = threadIdx.x >> 4;
  int r0 = blockIdx.y * 16, c0 = blockIdx.x * 16;
  float acc = 0;
  for (int k0 = 0; k0 < K; k0 += 32) {
    As[ty][tx] = bf2f(A[(r0 + ty) * K + k0 + tx]);
    As[ty][tx + 16] = bf2f(A[(r0 + ty) * K + k0 + tx + 16]);
    Bs[ty][tx] = bf2f(B[(k0 + ty) * N + c0 + tx]);
    Bs[ty + 16][tx] = bf2f(B[(k0 + ty + 16) * N + c0 + tx]);
    __syncthreads();
    for (int k = 0; k < 32; ++k) acc += As[ty][k] * Bs[k][tx];
    __syncthreads();
  }
  float o = rbf(acc);
  if (res) o += bf2f(res[(r0 + ty) * N + c0 + tx]);
  C[(r0 + ty) * N + c0 + tx] = f2bf(o);
}

__global__ void k_phi(const u16* qkv, const u16* wphi, u16* phi) {
  int idx = blockIdx.x * blockDim.x + threadIdx.x;
  int h = idx % H, s = (idx / H) % S, w = idx / (H * S);
  float q[16], p[16];
  for (int d = 0; d < 16; ++d) q[d] = bf2f(qkv[s * 3 * D + w * D + h * 16 + d]);
  float mx = -1e30f, mn = 1e30f;
  for (int f = 0; f < 16; ++f) { float a = 0; for (int d = 0; d < 16; ++d) a += q[d] * bf2f(wphi[h * 256 + d * 16 + f]); p[f] = rbf(a); mx = fmaxf(mx, p[f]); mn = fminf(mn, p[f]); }
  float s1 = 0, s2 = 0;
  for (int f = 0; f < 16; ++f) { s1 += __expf(p[f] - mx); s2 += __expf(mn - p[f]); }
  u16* o = phi + ((size_t)(w * S + s) * H + h) * 32;
  for (int f = 0; f < 16; ++f) { o[f] = f2bf(__expf(p[f] - mx) / s1); o[16 + f] = f2bf(__expf(mn - p[f]) / s2); }
}

__global__ void k_kv(const u16* qkv, const u16* phi, float* kv) {
  int h = blockIdx.x, t = threadIdx.x;
  if (t >= 32 * 17) return;
  int f = t / 17, d = t % 17;
  float a = 0;
  for (int s = 0; s < S; ++s) {
    float pk = bf2f(phi[((size_t)(S + s) * H + h) * 32 + f]);
    float v = d < 16 ? bf2f(qkv[s * 3 * D + 2 * D + h * 16 + d]) : 1.f;
    a += pk * v;
  }
  kv[h * 544 + t] = rbf(a);
}

__global__ void k_o(const u16* phi, const float* kv, u16* o) {
  int idx = blockIdx.x * blockDim.x + threadIdx.x;
  int d = idx % 16, h = (idx / 16) % H, s = idx / D;
  float num = 0, den = 0;
  for (int f = 0; f < 32; ++f) { float p = bf2f(phi[((size_t)s * H + h) * 32 + f]); num += p * kv[h * 544 + f * 17 + d]; den += p * kv[h * 544 + f * 17 + 16]; }
  o[idx] = f2bf(rbf(num) / rbf(rbf(den) + 1e-6f));
}

__global__ void k_geglu(const u16* g, const u16* u, u16* hh) {
  int idx = blockIdx.x * blockDim.x + threadIdx.x;
  float x = bf2f(g[idx]);
  float t = tanhf(0.7978845608028654f * (x + 0.044715f * x * x * x));
  float ge = rbf(0.5f * x * (1.f + t));
  hh[idx] = f2bf(ge * bf2f(u[idx]));
}

extern "C" void kernel_launch(void* const* d_in, const int* in_sizes, int n_in, void* d_out, int out_size, void* d_ws, size_t ws_size, hipStream_t stream) {
  const u16* x0 = (const u16*)d_in[0];
  const u16* wqkv = (const u16*)d_in[1];
  const u16* wphi = (const u16*)d_in[2];
  const u16* wo = (const u16*)d_in[3];
  const u16* wg = (const u16*)d_in[5];
  const u16* wu = (const u16*)d_in[6];
  const u16* wd = (const u16*)d_in[7];
  char* ws = (char*)d_ws;
  u16* xn = (u16*)(ws);
  u16* qkv = (u16*)(ws + (4 << 20));
  u16* phi = (u16*)(ws + (12 << 20));
  float* kv = (float*)(ws + (24 << 20));
  u16* o = (u16*)(ws + (25 << 20));
  u16* x1 = (u16*)(ws + (28 << 20));
  u16* g = (u16*)(ws + (32 << 20));
  u16* u = (u16*)(ws + (40 << 20));
  u16* hh = (u16*)(ws + (48 << 20));
  u16* x2 = (u16*)(ws + (56 << 20));
  const u16* xin = x0;
  for (int l = 0; l < 2; ++l) {
    u16* xo = l == 0 ? x2 : (u16*)d_out;
    k_ln<<<S, 64, 0, stream>>>(xin, xn);
    k_gemm<<<dim3(3 * D / 16, S / 16), 256, 0, stream>>>(xn, wqkv, nullptr, qkv, 3 * D, D);
    k_phi<<<2 * S * H / 256, 256, 0, stream>>>(qkv, wphi, phi);
    k_kv<<<H, 576, 0, stream>>>(qkv, phi, kv);
    k_o<<<S * D / 256, 256, 0, stream>>>(phi, kv, o);
    k_gemm<<<dim3(D / 16, S / 16), 256, 0, stream>>>(o, wo, xin, x1, D, D);
    k_ln<<<S, 64, 0, stream>>>(x1, xn);
    k_gemm<<<dim3(F / 16, S / 16), 256, 0, stream>>>(xn, wg, nullptr, g, F, D);
    k_gemm<<<dim3(F / 16, S / 16), 256, 0, stream>>>(xn, wu, nullptr, u, F, D);
    k_geglu<<<S * F / 256, 256, 0, stream>>>(g, u, hh);
    k_gemm<<<dim3(D / 16, S / 16), 256, 0, stream>>>(hh, wd, x1, xo, D, F);
    xin = x2;
  }
}
```

```cpp
#include <hip/hip_runtime.h>
#include <hip/hip_bf16.h>

__global__ __launch_bounds__(256, 1) __attribute__((aligned(4096))) void k_main(const void* x, const void* wqkv, const void* wphi, const void* wo, const void* wg,
                                                 const void* wu, const void* wd, void* out, void* ws) {
  __shared__ __attribute__((aligned(16))) char lds[163840];
  unsigned tid = threadIdx.x;
  asm volatile(
    "s_load_dwordx16 s[36:51], %1, 0\n"
    "s_load_dwordx2 s[52:53], %1, 64\n"
    "s_getpc_b64 s[92:93]\n"
    "Lpc:\n"
    "s_mov_b32 s4, %2\n"
    "v_lshrrev_b32 v1, 6, v0\n"
    "v_and_b32 v9, 63, v0\n"
    "v_readfirstlane_b32 s58, v1\n"
    "v_lshlrev_b32 v0, 4, v9\n"
    "s_and_b32 s93, s93, 0xffff\n"
    "s_mov_b32 s94, Lend-Lpc\n"
    "s_mov_b32 s95, 0x20000\n"
    "v_lshlrev_b32 v251, 7, v9\n"
    "v_cmp_gt_u32 vcc, 4, v9\n"
    "s_nop 1\n"
    "v_cndmask_b32 v245, 2.0, v251, vcc\n"
    "s_lshr_b32 s62, s4, 3\n"
    "s_lshl2_add_u32 s62, s62, s58\n"
    "s_lshl_b32 s74, s62, 9\n"
    "buffer_load_dword a32, v245, s[92:95], s74 offen\n"
    "s_lshr_b32 s62, s4, 6\n"
    "s_bfe_u32 s63, s4, 0x20001\n"
    "s_lshl2_add_u32 s59, s63, s62\n"
    "s_bfe_u32 s73, s4, 0x30003\n"
    "s_and_b32 s62, s4, 1\n"
    "s_lshl3_add_u32 s60, s62, s73\n"
    "s_lshl_b32 s62, s59, 2\n"
    "s_and_b32 s63, s4, 1\n"
    "s_lshl1_add_u32 s62, s63, s62\n"
    "s_lshr_b32 s63, s58, 1\n"
    "s_add_u32 s72, s62, s63\n"
    "s_lshl_b32 s66, s58, 13\n"
    "s_lshl_b32 s7, s58, 10\n"
    "s_lshl_b32 s62, s72, 15\n"
    "s_and_b32 s63, s58, 1\n"
    "s_lshl_b32 s63, s63, 14\n"
    "s_add_u32 s84, s62, s63\n"
    "s_lshl_b32 s62, s73, 7\n"
    "s_add_u32 s91, s84, s62\n"
    "s_and_b32 s62, s58, 1\n"
    "s_lshl1_add_u32 s89, s59, s62\n"
    "s_mul_i32 s62, s89, 0xc000\n"
    "s_lshl_b32 s63, s60, 6\n"
    "s_add_u32 s85, s62, s63\n"
    "s_add_u32 s62, s85, 0x800\n"
    "s_cmp_lt_u32 s58, 2\n"
    "s_cselect_b32 s86, s62, 2.0\n"
    "s_add_u32 s62, s85, 0x400\n"
    "s_lshr_b32 s63, s58, 1\n"
    "s_lshl_b32 s63, s63, 10\n"
    "s_sub_u32 s85, s62, s63\n"
    "s_lshl_b32 s74, s60, 10\n"
    "v_lshrrev_b32 v241, 4, v9\n"
    "v_lshrrev_b32 v240, 2, v9\n"
    "v_and_b32 v242, 3, v9\n"
    "v_lshrrev_b32 v243, 5, v9\n"
    "v_and_b32 v251, 0x70, v0\n"
    "v_lshrrev_b32 v252, 3, v9\n"
    "v_lshl_add_u32 v13, v252, 10, v251\n"
    "v_lshlrev_b32 v251, 4, v241\n"
    "v_xor_b32 v100, v13, v251\n"
    "v_xor_b32 v101, 64, v100\n"
    "v_mul_u32_u24 v251, 0xc00, v240\n"
    "v_lshlrev_b32 v252, 1, v243\n"
    "v_xor_b32 v252, v242, v252\n"
    "v_lshl_add_u32 v102, v252, 4, v251\n"
    "s_mul_i32 s62, s89, 0xac00\n"
    "s_mul_i32 s63, s60, 10\n"
    "s_min_u32 s64, s60, 12\n"
    "s_add_u32 s64, s63, s64\n"
    "s_lshl4_add_u32 s78, s64, s62\n"
    "s_lshr_b32 s62, s58, 1\n"
    "s_and_b32 s63, s59, 7\n"
    "s_lshl1_add_u32 s88, s63, s62\n"
    "s_mul_i32 s62, s88, 10\n"
    "s_min_u32 s63, s88, 12\n"
    "s_add_u32 s63, s62, s63\n"
    "s_lshr_b32 s62, s59, 3\n"
    "s_lshl1_add_u32 s63, s62, s63\n"
    "s_and_b32 s62, s58, 1\n"
    "s_add_u32 s62, s63, s62\n"
    "s_lshl_b32 s62, s62, 13\n"
    "s_lshl_b32 s63, s73, 7\n"
    "s_add_u32 s87, s62, s63\n"
    "s_and_b32 s62, s59, 7\n"
    "s_lshl2_add_u32 s88, s62, s58\n"
    "s_mul_i32 s62, s88, 10\n"
    "s_min_u32 s63, s88, 12\n"
    "s_add_u32 s63, s62, s63\n"
    "s_add_u32 s63, s63, 10\n"
    "s_lshl_b32 s63, s63, 13\n"
    "s_lshl_b32 s62, s73, 7\n"
    "s_add_u32 s88, s63, s62\n"
    "s_add_u32 s62, s87, 0x10000\n"
    "s_cmp_lt_u32 s59, 8\n"
    "s_cselect_b32 s88, s62, s88\n"
    "s_lshl4_add_u32 s62, s59, s60\n"
    "s_lshl_b32 s62, s62, 7\n"
    "s_add_u32 s67, s62, 0x1000\n"
    "s_add_u32 s62, s67, 0x90000\n"
    "v_lshl_add_u32 v251, v9, 15, s62\n"
    "v_cmp_gt_u32 vcc, 2, v9\n"
    "s_nop 1\n"
    "v_cndmask_b32 v251, 2.0, v251, vcc\n"
    "s_lshl_b32 s62, s4, 7\n"
    "v_and_b32 v252, 7, v9\n"
    "v_lshl_add_u32 v252, v252, 2, s58\n"
    "v_lshl_add_u32 v252, v252, 21, s62\n"
    "v_cmp_gt_u32 vcc, 32, v9\n"
    "s_nop 1\n"
    "v_cndmask_b32 v252, 2.0, v252, vcc\n"
    "v_cmp_gt_u32 vcc, 32, v9\n"
    "s_nop 1\n"
    "v_cndmask_b32 v11, v252, v251, vcc\n"
    "s_lshl2_add_u32 s62, s4, s58\n"
    "s_lshl_b32 s62, s62, 11\n"
    "v_lshl_add_u32 v251, v9, 7, s62\n"
    "v_cmp_gt_u32 vcc, 16, v9\n"
    "s_nop 1\n"
    "v_cndmask_b32 v247, 2.0, v251, vcc\n"
    "s_lshl2_add_u32 s62, s4, s58\n"
    "s_mul_i32 s62, s62, 0x600\n"
    "v_lshl_add_u32 v251, v9, 7, s62\n"
    "v_cmp_gt_u32 vcc, 12, v9\n"
    "s_nop 1\n"
    "v_cndmask_b32 v248, 2.0, v251, vcc\n"
    "s_and_b32 s62, s58, 1\n"
    "s_lshl1_add_u32 s62, s4, s62\n"
    "s_mul_i32 s62, s62, 0xb00\n"
    "v_lshl_add_u32 v251, v9, 7, s62\n"
    "v_cmp_gt_u32 vcc, 22, v9\n"
    "s_nop 1\n"
    "v_cndmask_b32 v238, 2.0, v251, vcc\n"
    "s_lshl2_add_u32 s62, s4, s58\n"
    "s_mul_i32 s62, s62, 0x580\n"
    "v_lshl_add_u32 v251, v9, 7, s62\n"
    "v_cmp_gt_u32 vcc, 11, v9\n"
    "s_nop 1\n"
    "v_cndmask_b32 v239, 2.0, v251, vcc\n"
    "s_lshl2_add_u32 s62, s4, s58\n"
    "s_lshl_b32 s62, s62, 9\n"
    "v_add_u32 v7, s62, v245\n"
    "v_add_u32 v255, 0, v9\n"
    "v_mul_u32_u24 v251, 0xaaab, v255\n"
    "v_lshrrev_b32 v251, 19, v251\n"
    "v_mul_u32_u24 v251, 0xa00, v251\n"
    "v_lshl_add_u32 v109, v255, 4, v251\n"
    "v_add_u32 v255, 64, v9\n"
    "v_mul_u32_u24 v251, 0xaaab, v255\n"
    "v_lshrrev_b32_e64 v251, 19, v251\n"
    "v_mul_u32_u24 v251, 0xa00, v251\n"
    "v_lshl_add_u32 v110, v255, 4, v251\n"
    "v_add_u32 v255, 0x80, v9\n"
    "v_mul_u32_u24 v251, 0xaaab, v255\n"
    "v_lshrrev_b32 v251, 19, v251\n"
    "v_mul_u32_u24 v251, 0xa00, v251\n"
    "v_lshl_add_u32 v12, v255, 4, v251\n"
    "s_waitcnt lgkmcnt(0)\n"
    "s_mov_b32 s12, s36\n"
    "s_and_b32 s13, s37, 0xffff\n"
    "s_mov_b32 s14, 2097152\n"
    "s_mov_b32 s15, 0x20000\n"
    "s_mov_b32 s16, s38\n"
    "s_and_b32 s17, s39, 0xffff\n"
    "s_mov_b32 s18, 1572864\n"
    "s_mov_b32 s19, 0x20000\n"
    "s_add_u32 m0, s66, 131072\n"
    "s_add_u32 s69, s91, 0\n"
    "buffer_load_dwordx4 v100, s[12:15], s69 offen offset:0 lds\n"
    "s_add_u32 s69, s91, 7168\n"
    "buffer_load_dwordx4 v101, s[12:15], s69 offen offset:1024 lds\n"
    "s_add_u32 m0, s7, 65536\n"
    "s_add_u32 s69, s85, 0\n"
    "buffer_load_dwordx4 v102, s[16:19], s69 offen offset:0 lds\n"
    "s_add_u32 m0, s7, 69632\n"
    "s_add_u32 s69, s86, 0\n"
    "buffer_load_dwordx4 v102, s[16:19], s69 offen offset:0 lds\n"
    "s_mov_b32 s92, s40\n"
    "s_and_b32 s93, s41, 0xffff\n"
    "s_mov_b32 s94, 16384\n"
    "s_mov_b32 s95, 0x20000\n"
    "s_add_u32 m0, s66, 133120\n"
    "s_add_u32 s69, s74, 0\n"
    "buffer_load_dwordx4 v0, s[92:95], s69 offen offset:0 lds\n"
    "s_load_dwordx2 s[34:35], s[52:53], 0\n"
    "s_mov_b32 s8, s52\n"
    "s_and_b32 s9, s53, 0xffff\n"
    "s_mov_b32 s10, 35131392\n"
    "s_mov_b32 s11, 0x20000\n"
    "s_mov_b32 s20, s42\n"
    "s_and_b32 s21, s43, 0xffff\n"
    "s_mov_b32 s22, 524288\n"
    "s_mov_b32 s23, 0x20000\n"
    "s_mov_b32 s28, s48\n"
    "s_and_b32 s29, s49, 0xffff\n"
    "s_mov_b32 s30, 1409024\n"
    "s_mov_b32 s31, 0x20000\n"
    "s_cmp_lt_u32 s58, 2\n"
    "s_cselect_b32 s44, s44, s46\n"
    "s_cselect_b32 s45, s45, s47\n"
    "s_mov_b32 s24, s44\n"
    "s_and_b32 s25, s45, 0xffff\n"
    "s_mov_b32 s26, 1409024\n"
    "s_mov_b32 s27, 0x20000\n"
    "s_mov_b32 s12, s50\n"
    "s_and_b32 s13, s51, 0xffff\n"
    "s_mov_b32 s37, s78\n"
    "s_mov_b32 s38, s87\n"
    "s_mov_b32 s93, s88\n"
    "v_mov_b32 v8, v7\n"
    "buffer_load_dword a38, v8, s[20:23], 0 offen\n"
    "buffer_load_dword a35, v11, s[8:11], 0 offen sc1\n"
    "s_lshl2_add_u32 s71, s59, s58\n"
    "v_and_b32 v244, 15, v9\n"
    "v_and_b32 v246, 31, v9\n"
    "v_and_b32 v249, 3, v240\n"
    "v_and_b32 v250, 1, v241\n"
    "v_lshrrev_b32 v251, 1, v244\n"
    "v_xor_b32 v251, v251, v243\n"
    "v_lshlrev_b32 v251, 4, v251\n"
    "v_lshl_add_u32 v251, v244, 7, v251\n"
    "v_lshl_add_u32 v251, v250, 3, v251\n"
    "s_add_u32 s62, s66, 0x20000\n"
    "v_add_u32 v116, s62, v251\n"
    "v_xor_b32 v117, 32, v116\n"
    "v_xor_b32 v118, 64, v116\n"
    "v_xor_b32 v119, 0x60, v116\n"
    "v_lshlrev_b32 v251, 6, v240\n"
    "v_lshl_add_u32 v251, v242, 3, v251\n"
    "v_lshl_add_u32 v251, v243, 5, v251\n"
    "s_and_b32 s62, s58, 1\n"
    "s_lshl_b32 s62, s62, 5\n"
    "v_xor_b32 v251, s62, v251\n"
    "v_add_u32 v107, 0x10000, v251\n"
    "v_lshlrev_b32 v251, 6, v244\n"
    "v_lshl_add_u32 v251, v241, 3, v251\n"
    "v_lshrrev_b32 v252, 3, v244\n"
    "v_lshl_add_u32 v251, v252, 5, v251\n"
    "s_and_b32 s62, s58, 1\n"
    "s_lshl_b32 s62, s62, 5\n"
    "v_xor_b32 v251, s62, v251\n"
    "s_lshr_b32 s62, s58, 1\n"
    "s_lshl_b32 s62, s62, 11\n"
    "s_add_u32 s62, s62, 0x10000\n"
    "v_add_u32 v108, s62, v251\n"
    "s_and_b32 s62, s58, 1\n"
    "s_lshl_b32 s62, s62, 9\n"
    "s_add_u32 s62, s66, s62\n"
    "s_add_u32 s62, s62, 0x20800\n"
    "v_lshl_add_u32 v113, v9, 3, s62\n"
    "s_mul_i32 s82, s72, 0x8800\n"
    "s_lshl_b32 s62, s73, 12\n"
    "s_add_u32 s62, s82, s62\n"
    "s_and_b32 s63, s58, 1\n"
    "s_lshl_b32 s63, s63, 10\n"
    "s_add_u32 s81, s62, s63\n"
    "s_lshl_b32 s62, s73, 7\n"
    "s_add_u32 s62, s82, s62\n"
    "s_and_b32 s63, s58, 1\n"
    "s_lshl_b32 s63, s63, 10\n"
    "s_add_u32 s63, s62, s63\n"
    "s_add_u32 s82, s63, 0x8000\n"
    "s_lshl2_add_u32 s62, s60, s58\n"
    "s_lshl_b32 s90, s62, 13\n"
    "s_mul_i32 s62, s60, 0x18000\n"
    "s_lshl_b32 s63, s59, 11\n"
    "s_add_u32 s63, s62, s63\n"
    "s_and_b32 s62, s58, 1\n"
    "s_lshl_b32 s62, s62, 10\n"
    "s_add_u32 s62, s63, s62\n"
    "s_add_u32 s44, s62, 0x241000\n"
    "s_add_u32 s62, s44, 0x8000\n"
    "s_cmp_lt_u32 s58, 2\n"
    "s_cselect_b32 s45, s62, 2.0\n"
    "s_lshr_b32 s62, s58, 1\n"
    "s_lshl_b32 s62, s62, 16\n"
    "s_add_u32 s44, s44, s62\n"
    "s_lshr_b32 s62, s90, 2\n"
    "s_lshl_b32 s63, s59, 7\n"
    "s_add_u32 s63, s62, s63\n"
    "s_add_u32 s39, s63, 0x221000\n"
    "s_mul_i32 s62, s58, 0x28000\n"
    "s_add_u32 s36, s62, s67\n"
    "v_and_b32 v251, 0x70, v0\n"
    "v_lshrrev_b32 v252, 3, v9\n"
    "v_lshl_add_u32 v251, v252, 15, v251\n"
    "v_cmp_gt_u32 vcc, 40, v9\n"
    "s_nop 1\n"
    "v_cndmask_b32 v251, 2.0, v251, vcc\n"
    "v_lshrrev_b32 v252, 3, v9\n"
    "s_mul_i32 s62, s58, 5\n"
    "v_add_u32 v252, s62, v252\n"
    "v_cmp_gt_u32 vcc, 18, v252\n"
    "s_nop 1\n"
    "v_cndmask_b32 v10, 2.0, v251, vcc\n"
    "v_lshlrev_b32 v251, 3, v244\n"
    "v_lshl_add_u32 v6, v250, 15, v251\n"
    "s_lshl_b32 s62, s59, 4\n"
    "s_lshl2_add_u32 s62, s58, s62\n"
    "s_lshl_b32 s62, s62, 7\n"
    "v_add_u32 v3, s62, v245\n"
    "s_lshl_b32 s62, s58, 6\n"
    "s_add_u32 s62, s62, s60\n"
    "s_lshl_b32 s62, s62, 7\n"
    "v_lshl_add_u32 v251, v9, 11, s62\n"
    "v_cmp_gt_u32 vcc, 4, v9\n"
    "s_nop 1\n"
    "v_cndmask_b32 v14, 2.0, v251, vcc\n"
    "v_accvgpr_write_b32 a250, v14\n"
    "s_cmp_eq_u32 s58, 0\n"
    "s_cselect_b32 s94, s67, 2.0\n"
    "s_cmp_eq_u32 s58, 0\n"
    "s_cselect_b32 s95, -1, 0\n"
    "v_mov_b32 v251, 0x3f803f80\n"
    "s_mov_b32 s61, 60000\n"
    "s_mov_b32 s54, 0xffff0000\n"
    "s_mov_b32 s55, 0xffff0000\n"
    "v_accvgpr_write_b32 a255, 1\n"
    "v_accvgpr_write_b32 a240, v251\n"
    "v_accvgpr_write_b32 a40, 0\n"
    "v_accvgpr_write_b32 a236, -1\n"
    "v_accvgpr_write_b32 a241, v251\n"
    "v_accvgpr_write_b32 a41, 0\n"
    "v_accvgpr_write_b32 a237, -1\n"
    "v_accvgpr_write_b32 a242, v251\n"
    "v_accvgpr_write_b32 a42, 0\n"
    "v_accvgpr_write_b32 a238, -1\n"
    "v_accvgpr_write_b32 a243, v251\n"
    "v_accvgpr_write_b32 a43, 0\n"
    "v_accvgpr_write_b32 a239, -1\n"
    "v_add_u32 v1, 0x10000, v0\n"
    "v_add_u32 v2, 0x20000, v0\n"
    "s_lshl_b32 s5, s58, 12\n"
    "s_mul_i32 s62, s60, 0x18000\n"
    "s_add_u32 s62, s62, s5\n"
    "s_add_u32 s6, s62, 0x241000\n"
    "s_add_u32 s75, s6, s5\n"
    "s_mul_i32 s80, s71, 0x8800\n"
    "s_lshl_b32 s62, s58, 16\n"
    "s_and_b32 s63, s60, 7\n"
    "s_lshl_b32 s63, s63, 7\n"
    "s_add_u32 s76, s62, s63\n"
    "s_mul_i32 s62, s60, 0x30000\n"
    "s_add_u32 s62, s62, s66\n"
    "s_add_u32 s77, s62, 0x3c1000\n"
    "s_lshl2_add_u32 s62, s60, s58\n"
    "s_mul_i32 s62, s62, 0xb000\n"
    "s_add_u32 s79, s62, 0x6c1000\n"
    "s_lshl_b32 s62, s71, 15\n"
    "s_lshl_b32 s63, s60, 10\n"
    "s_add_u32 s83, s62, s63\n"
    "s_mul_i32 s85, s71, 0x16000\n"
    "s_lshr_b32 s62, s60, 2\n"
    "s_mul_i32 s62, s62, 0x5800\n"
    "s_add_u32 s62, s85, s62\n"
    "s_and_b32 s63, s60, 3\n"
    "s_lshl_b32 s63, s63, 8\n"
    "s_add_u32 s63, s62, s63\n"
    "s_add_u32 s63, s63, 0x5000\n"
    "s_cmp_lt_u32 s60, 12\n"
    "s_cselect_b32 s86, s63, 2.0\n"
    "s_bfe_u32 s62, s60, 0x20001\n"
    "s_mul_i32 s62, s62, 0x5800\n"
    "s_add_u32 s62, s85, s62\n"
    "s_lshr_b32 s63, s60, 3\n"
    "s_lshl_b32 s63, s63, 11\n"
    "s_add_u32 s63, s62, s63\n"
    "s_and_b32 s62, s60, 1\n"
    "s_lshl_b32 s62, s62, 9\n"
    "s_add_u32 s85, s63, s62\n"
    "s_sub_u32 s62, s77, s5\n"
    "s_add_u32 s62, s62, 0x28000\n"
    "s_cmp_lt_u32 s60, 12\n"
    "s_cselect_b32 s78, s62, 2.0\n"
    "s_mul_i32 s62, s72, 0x16000\n"
    "s_mul_i32 s63, s58, 0x5800\n"
    "s_add_u32 s87, s62, s63\n"
    "s_xor_b32 s62, s72, 1\n"
    "s_mul_i32 s62, s62, 0x16000\n"
    "s_mul_i32 s63, s58, 0x5800\n"
    "s_add_u32 s88, s62, s63\n"
    "s_cmp_lt_u32 s58, 3\n"
    "s_cselect_b32 s89, 0, 2.0\n"
    "s_and_b32 s62, s60, 2\n"
    "s_lshl_b32 s92, s62, 9\n"
    "s_mul_i32 s62, s60, 0x30000\n"
    "s_and_b32 s63, s58, 1\n"
    "s_mul_i32 s63, s63, 0x18000\n"
    "s_add_u32 s63, s62, s63\n"
    "s_lshl_b32 s62, s59, 11\n"
    "s_add_u32 s62, s63, s62\n"
    "s_lshr_b32 s63, s58, 1\n"
    "s_lshl_b32 s63, s63, 10\n"
    "s_add_u32 s63, s62, s63\n"
    "s_add_u32 s40, s63, 0x3c1000\n"
    "s_lshl_b32 s62, s59, 10\n"
    "s_sub_u32 s62, s40, s62\n"
    "s_lshr_b32 s63, s58, 1\n"
    "s_mul_i32 s63, s63, 12\n"
    "s_add_u32 s63, s60, s63\n"
    "s_cmp_lt_u32 s63, 12\n"
    "s_cselect_b32 s41, s62, 2.0\n"
    "s_and_b32 s62, s58, 1\n"
    "s_cmp_eq_u32 s62, 0\n"
    "s_cselect_b32 s62, s40, s41\n"
    "s_add_u32 s41, s62, 0x10000\n"
    "s_and_b32 s62, s59, 3\n"
    "s_lshl2_add_u32 s62, s60, s62\n"
    "s_mul_i32 s62, s62, 0xb000\n"
    "s_lshr_b32 s63, s59, 2\n"
    "s_lshl_b32 s63, s63, 12\n"
    "s_add_u32 s63, s62, s63\n"
    "s_add_u32 s63, s63, s7\n"
    "s_add_u32 s42, s63, 0x6c1000\n"
    "s_cmp_lt_u32 s59, 11\n"
    "s_cselect_b32 s43, s42, 2.0\n"
    "v_lshlrev_b32 v124, 3, v242\n"
    "v_mul_u32_u24 v251, 0xc0, v240\n"
    "v_add_u32 v251, v251, v124\n"
    "s_and_b32 s62, s58, 1\n"
    "s_mul_i32 s62, s62, 0x60\n"
    "s_lshr_b32 s63, s58, 1\n"
    "s_lshl_b32 s63, s63, 14\n"
    "s_add_u32 s63, s62, s63\n"
    "s_add_u32 s63, s63, 0x18000\n"
    "v_add_u32 v120, s63, v251\n"
    "v_lshrrev_b32 v251, 1, v242\n"
    "s_and_b32 s62, s58, 1\n"
    "s_mul_i32 s62, s62, 0x3ff0\n"
    "v_mul_u32_u24 v251, s62, v251\n"
    "buffer_store_dwordx4 a[40:43], v10, s[8:11], s36 offen sc1\n"
    "s_waitcnt vmcnt(6)\n"
    "ds_read_b64 v[80:81], v116\n"
    "ds_read_b64 v[82:83], v117\n"
    "ds_read_b64 v[84:85], v118\n"
    "ds_read_b64 v[86:87], v119\n"
    "s_add_u32 s73, s81, 9965568\n"
    "s_waitcnt lgkmcnt(0)\n"
    "buffer_store_dwordx4 v[80:83], v0, s[8:11], s73 offen sc1\n"
    "buffer_store_dwordx4 v[84:87], v0, s[8:11], s73 offen offset:2048 sc1\n"
    "s_waitcnt vmcnt(5)\n"
    "s_barrier\n"
    "ds_read_b64_tr_b16 v[64:65], v113\n"
    "ds_read_b64 v[66:67], v108 offset:0\n"
    "ds_read_b64 v[68:69], v108 offset:1024\n"
    "ds_read_b64_tr_b16 v[20:21], v107 offset:4096\n"
    "ds_read_b64_tr_b16 v[22:23], v107 offset:5120\n"
    "v_mov_b32 v88, 0x3f803f80\n"
    "v_mov_b32 v92, 0\n"
    "v_mov_b32 v93, 0\n"
    "v_dot2c_f32_bf16 v92, v88, v80\n"
    "v_dot2c_f32_bf16 v93, v80, v80\n"
    "v_dot2c_f32_bf16 v92, v88, v81\n"
    "v_dot2c_f32_bf16 v93, v81, v81\n"
    "v_dot2c_f32_bf16 v92, v88, v82\n"
    "v_dot2c_f32_bf16 v93, v82, v82\n"
    "v_dot2c_f32_bf16 v92, v88, v83\n"
    "v_dot2c_f32_bf16 v93, v83, v83\n"
    "v_dot2c_f32_bf16 v92, v88, v84\n"
    "v_dot2c_f32_bf16 v93, v84, v84\n"
    "v_dot2c_f32_bf16 v92, v88, v85\n"
    "v_dot2c_f32_bf16 v93, v85, v85\n"
    "v_dot2c_f32_bf16 v92, v88, v86\n"
    "v_dot2c_f32_bf16 v93, v86, v86\n"
    "v_dot2c_f32_bf16 v92, v88, v87\n"
    "v_dot2c_f32_bf16 v93, v87, v87\n"
    "v_mul_u32_u24 v7, 17, v0\n"
    "s_waitcnt lgkmcnt(3)\n"
    "v_mfma_f32_16x16x16_bf16 v[72:75], v[66:67], v[64:65], 0\n"
    "s_waitcnt lgkmcnt(2)\n"
    "v_mfma_f32_16x16x16_bf16 v[76:79], v[68:69], v[64:65], 0\n"
    "s_waitcnt lgkmcnt(0)\n"
    "v_mfma_f32_16x16x32_bf16 v[36:39], a[240:243], v[20:23], 0\n"
    "buffer_store_dwordx4 v[20:23], v0, s[8:11], s45 offen sc1\n"
    "v_cmp_gt_u32 vcc, 0x100, v0\n"
    "v_permlane32_swap_b32 v92, v93\n"
    "v_add_f32 v92, v92, v93\n"
    "v_mov_b32 v93, v92\n"
    "v_bfe_u32 v7, v7, 7, 4\n"
    "v_lshlrev_b32 v7, 3, v7\n"
    "v_permlane16_swap_b32 v92, v93\n"
    "v_add_f32 v92, v92, v93\n"
    "v_mov_b32 v93, v92\n"
    "v_cndmask_b32 v7, 2.0, v7, vcc\n"
    "s_add_u32 s73, s82, 9965568\n"
    "v_permlane32_swap_b32 v92, v93\n"
    "buffer_store_dwordx2 v[92:93], v7, s[8:11], s73 offen sc1\n"
    "v_cvt_pk_bf16_f32 v16, v72, v73\n"
    "v_cvt_pk_bf16_f32 v17, v74, v75\n"
    "v_cvt_pk_bf16_f32 v18, v76, v77\n"
    "v_cvt_pk_bf16_f32 v19, v78, v79\n"
    "buffer_store_dwordx4 v[16:19], v0, s[8:11], s44 offen sc1\n"
    "s_nop 0\n"
    "v_mfma_f32_16x16x32_bf16 v[32:35], a[240:243], v[16:19], 0\n"
    "s_nop 7\n"
    "v_mov_b32 v33, v36\n"
    "buffer_store_dwordx2 v[32:33], v7, s[8:11], s39 offen sc1\n"
    "s_add_u32 m0, s66, 98304\n"
    "s_add_u32 s69, s37, 0\n"
    "buffer_load_dwordx4 v109, s[24:27], s69 offen offset:0 lds\n"
    "s_add_u32 m0, s66, 99328\n"
    "s_add_u32 s69, s37, 0\n"
    "buffer_load_dwordx4 v110, s[24:27], s69 offen offset:0 lds\n"
    "s_add_u32 m0, s66, 100352\n"
    "s_add_u32 s69, s37, 0\n"
    "buffer_load_dwordx4 v12, s[24:27], s69 offen offset:0 lds\n"
    "v_add_u32 v121, v251, v120\n"
    "s_lshl_b32 s62, s58, 5\n"
    "s_add_u32 s62, s62, 0x18000\n"
    "v_add_u32 v124, s62, v124\n"
    "v_and_b32 v251, 7, v240\n"
    "v_lshlrev_b32 v251, 7, v251\n"
    "v_lshl_add_u32 v251, v243, 14, v251\n"
    "v_add_u32 v122, v251, v124\n"
    "v_lshlrev_b32 v251, 7, v249\n"
    "v_lshl_add_u32 v251, v241, 13, v251\n"
    "v_add_u32 v124, v251, v124\n"
    "s_cmp_lt_u32 s59, 8\n"
    "s_cselect_b64 vcc, -1, 0\n"
    "v_cndmask_b32 v123, v124, v122, vcc\n"
    "s_cmp_lt_u32 s59, 8\n"
    "s_cselect_b32 s62, 16, 1\n"
    "s_lshl_b32 s62, s62, 9\n"
    "v_add_u32 v124, s62, v123\n"
    "v_xor_b32 v251, v249, v243\n"
    "v_lshlrev_b32 v251, 3, v251\n"
    "v_lshl_add_u32 v251, v246, 5, v251\n"
    "v_add_u32 v251, s7, v251\n"
    "v_accvgpr_write_b32 a244, v251\n"
    "v_xor_b32 v251, v249, v241\n"
    "v_lshlrev_b32 v251, 3, v251\n"
    "v_lshl_add_u32 v251, v244, 5, v251\n"
    "v_add_u32 v251, s7, v251\n"
    "v_accvgpr_write_b32 a245, v251\n"
    "v_xor_b32 v251, v242, v241\n"
    "v_lshlrev_b32 v251, 3, v251\n"
    "v_lshl_add_u32 v255, v240, 5, v251\n"
    "s_lshl_b32 s62, s58, 12\n"
    "v_add_u32 v251, s62, v255\n"
    "v_accvgpr_write_b32 a246, v251\n"
    "s_lshr_b32 s62, s58, 1\n"
    "s_lshl_b32 s62, s62, 12\n"
    "s_add_u32 s62, s62, 0x4000\n"
    "v_add_u32 v251, s62, v255\n"
    "v_accvgpr_write_b32 a247, v251\n"
    "v_lshl_add_u32 v255, v246, 1, v243\n"
    "v_and_b32 v251, 15, v255\n"
    "v_bfe_u32 v252, v255, 4, 1\n"
    "v_lshl_add_u32 v251, v252, 5, v251\n"
    "v_lshlrev_b32 v251, 4, v251\n"
    "v_lshrrev_b32 v252, 5, v255\n"
    "v_lshl_add_u32 v251, v252, 3, v251\n"
    "s_add_u32 s62, s7, 0x6000\n"
    "v_add_u32 v251, s62, v251\n"
    "v_accvgpr_write_b32 a248, v251\n"
    "v_and_b32 v251, 1, v9\n"
    "v_lshlrev_b32 v251, 5, v251\n"
    "v_lshl_add_u32 v251, v249, 2, v251\n"
    "v_lshlrev_b32 v251, 4, v251\n"
    "v_bfe_u32 v252, v9, 1, 1\n"
    "v_lshlrev_b32 v252, 2, v252\n"
    "v_lshl_add_u32 v252, v250, 1, v252\n"
    "v_add_u32 v252, v252, v243\n"
    "v_lshl_add_u32 v251, v252, 1, v251\n"
    "s_add_u32 s62, s7, 0x6100\n"
    "v_add_u32 v251, s62, v251\n"
    "v_accvgpr_write_b32 a249, v251\n"
    "v_lshlrev_b32 v251, 4, v244\n"
    "v_lshl_add_u32 v251, v243, 8, v251\n"
    "v_lshl_add_u32 v251, v250, 14, v251\n"
    "v_accvgpr_write_b32 a251, v251\n"
    "v_lshrrev_b32 v251, 3, v9\n"
    "v_lshlrev_b32 v251, 10, v251\n"
    "v_lshlrev_b32 v252, 1, v241\n"
    "v_and_b32 v253, 7, v9\n"
    "v_xor_b32 v253, v252, v253\n"
    "v_lshl_add_u32 v15, v253, 4, v251\n"
    "v_accvgpr_write_b32 a252, v15\n"
    "v_lshl_add_u32 v255, v250, 2, v249\n"
    "v_lshl_add_u32 v251, v243, 4, v255\n"
    "v_lshlrev_b32 v251, 7, v251\n"
    "v_lshrrev_b32 v252, 1, v255\n"
    "v_lshl_add_u32 v251, v252, 5, v251\n"
    "v_lshl_add_u32 v251, v242, 3, v251\n"
    "v_add_u32 v251, 0x18000, v251\n"
    "v_accvgpr_write_b32 a253, v251\n"
    "v_lshlrev_b32 v48, 16, v80\n"
    "v_and_b32 v49, 0xffff0000, v80\n"
    "v_lshlrev_b32 v50, 16, v81\n"
    "v_and_b32 v51, 0xffff0000, v81\n"
    "v_lshlrev_b32 v52, 16, v82\n"
    "v_and_b32 v53, 0xffff0000, v82\n"
    "v_lshlrev_b32 v54, 16, v83\n"
    "v_and_b32 v55, 0xffff0000, v83\n"
    "v_lshlrev_b32 v56, 16, v84\n"
    "v_and_b32 v57, 0xffff0000, v84\n"
    "v_lshlrev_b32 v58, 16, v85\n"
    "v_and_b32 v59, 0xffff0000, v85\n"
    "v_lshlrev_b32 v60, 16, v86\n"
    "v_and_b32 v61, 0xffff0000, v86\n"
    "v_lshlrev_b32 v62, 16, v87\n"
    "v_and_b32 v63, 0xffff0000, v87\n"
    "v_accvgpr_write_b32 a220, v48\n"
    "v_accvgpr_write_b32 a221, v49\n"
    "v_accvgpr_write_b32 a222, v50\n"
    "v_accvgpr_write_b32 a223, v51\n"
    "v_accvgpr_write_b32 a224, v52\n"
    "v_accvgpr_write_b32 a225, v53\n"
    "v_accvgpr_write_b32 a226, v54\n"
    "v_accvgpr_write_b32 a227, v55\n"
    "v_accvgpr_write_b32 a228, v56\n"
    "v_accvgpr_write_b32 a229, v57\n"
    "v_accvgpr_write_b32 a230, v58\n"
    "v_accvgpr_write_b32 a231, v59\n"
    "v_accvgpr_write_b32 a232, v60\n"
    "v_accvgpr_write_b32 a233, v61\n"
    "v_accvgpr_write_b32 a234, v62\n"
    "v_accvgpr_write_b32 a235, v63\n"
    "s_add_u32 s34, s34, 1\n"
    "v_mov_b32 v4, s34\n"
    "v_mov_b32 v5, s35\n"
    "s_mov_b64 exec, 0\n"
    "s_cmp_eq_u32 s58, 3\n"
    "s_cbranch_scc1 Lpad1\n"
    "s_cmp_eq_u32 s58, 2\n"
    "s_cbranch_scc1 Lpad2\n"
    "s_cmp_eq_u32 s58, 1\n"
    "s_cbranch_scc1 Lpad3\n"
    "s_branch Lpad4\n"
    "Lwarm0:\n"
    "s_mov_b64 exec, -1\n"
    "s_waitcnt vmcnt(3)\n"
    "s_barrier\n"
    "s_lshl3_add_u32 s74, s59, 593920\n"
    "buffer_store_dwordx2 v[4:5], v14, s[8:11], s74 offen sc1\n"
    "s_lshl3_add_u32 s74, s60, 626688\n"
    "buffer_store_dwordx2 v[4:5], v3, s[8:11], s74 offen sc1\n"
    "s_add_u32 m0, s66, 101376\n"
    "s_add_u32 s69, s38, 0\n"
    "buffer_load_dwordx4 v13, s[28:31], s69 offen offset:0 lds\n"
    "s_add_u32 s69, s38, 31744\n"
    "buffer_load_dwordx4 v13, s[28:31], s69 offen offset:1024 lds\n"
    "s_add_u32 m0, s66, 103424\n"
    "s_add_u32 s69, s93, 0\n"
    "buffer_load_dwordx4 v13, s[28:31], s69 offen offset:0 lds\n"
    "s_nop 0\n"
    "s_sleep 14\n"
    "s_add_u32 s68, s94, 589824\n"
    "buffer_load_dwordx2 v[8:9], v6, s[8:11], s68 offen sc1\n"
    "s_add_u32 m0, s66, 131072\n"
    "s_add_u32 s69, s76, 262144\n"
    "buffer_load_dwordx4 v15, s[20:23], s69 offen offset:0 lds\n"
    "s_add_u32 s69, s76, 269312\n"
    "buffer_load_dwordx4 v15, s[20:23], s69 offen offset:1024 lds\n"
    "s_add_u32 s69, s76, 276480\n"
    "buffer_load_dwordx4 v15, s[20:23], s69 offen offset:2048 lds\n"
    "s_add_u32 s69, s76, 283648\n"
    "buffer_load_dwordx4 v15, s[20:23], s69 offen offset:3072 lds\n"
    "s_waitcnt vmcnt(4)\n"
    "v_cmp_ne_u64 vcc, s[34:35], v[8:9]\n"
    "s_nop 0\n"
    "s_and_b32 s29, vcc_lo, s95\n"
    "s_cbranch_scc1 Lslow1\n"
    "Lback1:\n"
    "s_and_b32 s95, s95, 16\n"
    "v_cmp_eq_u32 vcc, 0, v0\n"
    "s_nop 1\n"
    "v_cndmask_b32 v6, 2.0, v0, vcc\n"
    "s_barrier\n"
    "s_lshr_b32 s39, s90, 2\n"
    "s_add_u32 s74, s39, 2232320\n"
    "buffer_load_dwordx4 v[48:51], v0, s[8:11], s74 offen offset:0 sc1\n"
    "buffer_load_dwordx4 v[52:55], v0, s[8:11], s74 offen offset:1024 sc1\n"
    "s_add_u32 m0, s5, 0\n"
    "s_nop 0\n"
    "s_add_u32 s69, s6, 0\n"
    "buffer_load_dwordx4 v0, s[8:11], s69 offen offset:0 sc1 lds\n"
    "buffer_load_dwordx4 v0, s[8:11], s69 offen offset:1024 sc1 lds\n"
    "buffer_load_dwordx4 v0, s[8:11], s69 offen offset:2048 sc1 lds\n"
    "buffer_load_dwordx4 v0, s[8:11], s69 offen offset:3072 sc1 lds\n"
    "s_add_u32 m0, s5, 32768\n"
    "s_add_u32 s69, s6, 32768\n"
    "buffer_load_dwordx4 v0, s[8:11], s69 offen offset:0 sc1 lds\n"
    "buffer_load_dwordx4 v0, s[8:11], s69 offen offset:1024 sc1 lds\n"
    "buffer_load_dwordx4 v0, s[8:11], s69 offen offset:2048 sc1 lds\n"
    "buffer_load_dwordx4 v0, s[8:11], s69 offen offset:3072 sc1 lds\n"
    "s_add_u32 s71, s80, 9965568\n"
    "buffer_load_dwordx4 v[128:131], v0, s[8:11], s71 offen offset:0 sc1\n"
    "buffer_load_dwordx4 v[132:135], v0, s[8:11], s71 offen offset:1024 sc1\n"
    "buffer_load_dwordx4 v[136:139], v0, s[8:11], s71 offen offset:2048 sc1\n"
    "buffer_load_dwordx4 v[140:143], v0, s[8:11], s71 offen offset:3072 sc1\n"
    "s_add_u32 s71, s80, 9969664\n"
    "buffer_load_dwordx4 v[144:147], v0, s[8:11], s71 offen offset:0 sc1\n"
    "buffer_load_dwordx4 v[148:151], v0, s[8:11], s71 offen offset:1024 sc1\n"
    "s_nop 0\n"
    "s_mov_b32 s28, 0\n"
    "ds_read_b64_tr_b16 a[4:5], v120 offset:0\n"
    "ds_read_b64_tr_b16 a[6:7], v120 offset:8192\n"
    "ds_read_b64_tr_b16 a[8:9], v120 offset:32\n"
    "ds_read_b64_tr_b16 a[10:11], v120 offset:8224\n"
    "ds_read_b64_tr_b16 a[12:13], v121 offset:64\n"
    "ds_read_b64_tr_b16 a[14:15], v121 offset:8256\n"
    "ds_read_b64_tr_b16 a[16:17], v122 offset:3072\n"
    "ds_read_b64_tr_b16 a[18:19], v122 offset:11264\n"
    "ds_read_b64_tr_b16 a[20:21], v122 offset:4096\n"
    "ds_read_b64_tr_b16 a[22:23], v122 offset:12288\n"
    "ds_read_b64_tr_b16 a[24:25], v123 offset:5120\n"
    "ds_read_b64_tr_b16 a[26:27], v124 offset:5120\n"
    "s_waitcnt lgkmcnt(10)\n"
    "v_mfma_f32_16x16x32_bf16 v[64:67], a[240:243], a[4:7], 0\n"
    "s_waitcnt lgkmcnt(8)\n"
    "v_mfma_f32_16x16x32_bf16 v[68:71], a[240:243], a[8:11], 0\n"
    "s_waitcnt lgkmcnt(6)\n"
    "v_mfma_f32_16x16x32_bf16 v[72:75], a[240:243], a[12:15], 0\n"
    "buffer_load_dwordx4 v[152:155], v0, s[8:11], s71 offen offset:2048 sc1\n"
    "s_nop 2\n"
    "v_accvgpr_write_b32 a28, v64\n"
    "s_nop 0\n"
    "v_accvgpr_write_b32 a29, v68\n"
    "s_nop 0\n"
    "v_accvgpr_write_b32 a30, v72\n"
    "s_waitcnt lgkmcnt(0) vmcnt(15)\n"
    "v_add_f32 v48, v48, v52\n"
    "v_add_f32 v49, v49, v53\n"
    "v_add_f32 v50, v50, v54\n"
    "buffer_load_dwordx4 v[156:159], v0, s[8:11], s71 offen offset:3072 sc1\n"
    "Lpad1:\n"
    "s_cbranch_execz Lpad5\n"
    "v_add_f32 v51, v51, v55\n"
    "v_add_f32_dpp v56, v48, v48 row_ror:8 row_mask:0xf bank_mask:3\n"
    "v_add_f32_dpp v57, v49, v49 row_ror:8 row_mask:0xf bank_mask:3\n"
    "s_nop 0\n"
    "v_add_f32_dpp v56, v50, v50 row_ror:8 row_mask:0xf bank_mask:12\n"
    "v_add_f32_dpp v57, v51, v51 row_ror:8 row_mask:0xf bank_mask:12\n"
    "v_cvt_pk_bf16_f32 v58, v56, v56\n"
    "s_add_u32 m0, s5, 16384\n"
    "s_add_u32 s69, s6, 16384\n"
    "buffer_load_dwordx4 v0, s[8:11], s69 offen offset:0 sc1 lds\n"
    "v_lshlrev_b32 v58, 16, v58\n"
    "v_sub_f32 v56, v56, v58\n"
    "v_cvt_pk_bf16_f32 v48, v58, v56\n"
    "v_and_b32 v49, 0xffff0000, v48\n"
    "v_sub_f32 v56, v56, v49\n"
    "v_cvt_pk_bf16_f32 v49, v56, v58\n"
    "buffer_load_dwordx4 v0, s[8:11], s69 offen offset:1024 sc1 lds\n"
    "v_cvt_pk_bf16_f32 v58, v57, v57\n"
    "v_lshlrev_b32 v58, 16, v58\n"
    "v_sub_f32 v57, v57, v58\n"
    "v_cvt_pk_bf16_f32 v50, v58, v57\n"
    "v_and_b32 v51, 0xffff0000, v50\n"
    "v_sub_f32 v57, v57, v51\n"
    "v_cvt_pk_bf16_f32 v51, v57, v58\n"
    "v_lshrrev_b32 v58, 1, v0\n"
    "v_add_u32 v12, 0x10000, v58\n"
    "v_add_u32 v13, s7, v12\n"
    "ds_write_b64 v13, v[48:49] offset:0\n"
    "ds_write_b64 v13, v[50:51] offset:512\n"
    "s_waitcnt vmcnt(10) lgkmcnt(0)\n"
    "s_nop 0\n"
    "s_barrier\n"
    "Lredo4:\n"
    "ds_read_b128 v[80:83], v0 offset:0\n"
    "ds_read_b128 v[84:87], v0 offset:1024\n"
    "ds_read_b128 v[88:91], v0 offset:32768\n"
    "ds_read_b128 v[92:95], v0 offset:33792\n"
    "ds_read_b128 v[96:99], v0 offset:2048\n"
    "ds_read_b128 v[64:67], v0 offset:3072\n"
    "ds_read_b128 v[68:71], v0 offset:34816\n"
    "ds_read_b128 v[72:75], v0 offset:35840\n"
    "ds_read_b64 a[176:177], v12 offset:0\n"
    "ds_read_b64 a[178:179], v12 offset:1024\n"
    "ds_read_b64 a[180:181], v12 offset:512\n"
    "ds_read_b64 a[182:183], v12 offset:1536\n"
    "ds_read_b64 a[184:185], v12 offset:2048\n"
    "ds_read_b64 a[186:187], v12 offset:3072\n"
    "buffer_load_dwordx4 v0, s[8:11], s69 offen offset:2048 sc1 lds\n"
    "buffer_load_dwordx4 v0, s[8:11], s69 offen offset:3072 sc1 lds\n"
    "s_waitcnt vmcnt(10) lgkmcnt(10)\n"
    "v_mfma_f32_16x16x32_bf16 v[16:19], v[80:83], v[128:131], 0\n"
    "ds_read_b128 v[76:79], v0 offset:4096\n"
    "s_add_u32 m0, s5, 49152\n"
    "v_mfma_f32_16x16x32_bf16 v[20:23], v[80:83], v[132:135], 0\n"
    "s_add_u32 s69, s6, 49152\n"
    "v_mfma_f32_16x16x32_bf16 v[28:31], v[84:87], v[132:135], 0\n"
    "ds_read_b128 v[80:83], v0 offset:5120\n"
    "buffer_load_dwordx4 v0, s[8:11], s69 offen offset:0 sc1 lds\n"
    "v_mfma_f32_16x16x32_bf16 v[24:27], v[84:87], v[128:131], 0\n"
    "buffer_load_dwordx4 v0, s[8:11], s69 offen offset:1024 sc1 lds\n"
    "v_mfma_f32_16x16x32_bf16 v[32:35], v[88:91], v[128:131], 0\n"
    "ds_read_b128 v[84:87], v0 offset:36864\n"
    "v_mfma_f32_16x16x32_bf16 v[40:43], v[88:91], v[132:135], 0\n"
    "buffer_load_dwordx4 v0, s[8:11], s69 offen offset:2048 sc1 lds\n"
    "v_mfma_f32_16x16x32_bf16 v[44:47], v[92:95], v[132:135], 0\n"
    "ds_read_b128 v[88:91], v0 offset:37888\n"
    "v_mfma_f32_16x16x32_bf16 v[36:39], v[92:95], v[128:131], 0\n"
    "s_waitcnt vmcnt(11) lgkmcnt(10)\n"
    "v_mfma_f32_16x16x32_bf16 v[16:19], v[96:99], v[136:139], v[16:19]\n"
    "ds_read_b128 v[92:95], v0 offset:6144\n"
    "v_mfma_f32_16x16x32_bf16 v[20:23], v[96:99], v[140:143], v[20:23]\n"
    "buffer_load_dwordx4 v0, s[8:11], s69 offen offset:3072 sc1 lds\n"
    "v_mfma_f32_16x16x32_bf16 v[28:31], v[64:67], v[140:143], v[28:31]\n"
    "ds_read_b128 v[96:99], v0 offset:7168\n"
    "s_add_u32 s71, s80, 9973760\n"
    "v_mfma_f32_16x16x32_bf16 v[24:27], v[64:67], v[136:139], v[24:27]\n"
    "buffer_load_dwordx4 v[160:163], v0, s[8:11], s71 offen offset:0 sc1\n"
    "v_mfma_f32_16x16x32_bf16 v[32:35], v[68:71], v[136:139], v[32:35]\n"
    "ds_read_b128 v[64:67], v0 offset:38912\n"
    "v_mfma_f32_16x16x32_bf16 v[40:43], v[68:71], v[140:143], v[40:43]\n"
    "buffer_load_dwordx4 v[164:167], v0, s[8:11], s71 offen offset:1024 sc1\n"
    "v_mfma_f32_16x16x32_bf16 v[44:47], v[72:75], v[140:143], v[44:47]\n"
    "ds_read_b128 v[68:71], v0 offset:39936\n"
    "v_mfma_f32_16x16x32_bf16 v[36:39], v[72:75], v[136:139], v[36:39]\n"
    "s_waitcnt vmcnt(12) lgkmcnt(4)\n"
    "v_mfma_f32_16x16x32_bf16 v[16:19], v[76:79], v[144:147], v[16:19]\n"
    "ds_read_b128 v[72:75], v0 offset:8192\n"
    "v_mfma_f32_16x16x32_bf16 v[20:23], v[76:79], v[148:151], v[20:23]\n"
    "buffer_load_dwordx4 v[168:171], v0, s[8:11], s71 offen offset:2048 sc1\n"
    "v_mfma_f32_16x16x32_bf16 v[28:31], v[80:83], v[148:151], v[28:31]\n"
    "ds_read_b128 v[76:79], v0 offset:9216\n"
    "v_mfma_f32_16x16x32_bf16 v[24:27], v[80:83], v[144:147], v[24:27]\n"
    "buffer_load_dwordx4 v[172:175], v0, s[8:11], s71 offen offset:3072 sc1\n"
    "v_mfma_f32_16x16x32_bf16 v[32:35], v[84:87], v[144:147], v[32:35]\n"
    "ds_read_b128 v[80:83], v0 offset:40960\n"
    "s_add_u32 s71, s80, 9977856\n"
    "v_mfma_f32_16x16x32_bf16 v[40:43], v[84:87], v[148:151], v[40:43]\n"
    "buffer_load_dwordx4 v[176:179], v0, s[8:11], s71 offen offset:0 sc1\n"
    "v_mfma_f32_16x16x32_bf16 v[44:47], v[88:91], v[148:151], v[44:47]\n"
    "ds_read_b128 v[84:87], v0 offset:41984\n"
    "v_mfma_f32_16x16x32_bf16 v[36:39], v[88:91], v[144:147], v[36:39]\n"
    "s_waitcnt vmcnt(13) lgkmcnt(4)\n"
    "v_mfma_f32_16x16x32_bf16 v[16:19], v[92:95], v[152:155], v[16:19]\n"
    "ds_read_b128 v[88:91], v0 offset:10240\n"
    "v_mfma_f32_16x16x32_bf16 v[20:23], v[92:95], v[156:159], v[20:23]\n"
    "buffer_load_dwordx4 v[180:183], v0, s[8:11], s71 offen offset:1024 sc1\n"
    "v_mfma_f32_16x16x32_bf16 v[28:31], v[96:99], v[156:159], v[28:31]\n"
    "ds_read_b128 v[92:95], v0 offset:11264\n"
    "v_mfma_f32_16x16x32_bf16 v[24:27], v[96:99], v[152:155], v[24:27]\n"
    "buffer_load_dwordx4 v[184:187], v0, s[8:11], s71 offen offset:2048 sc1\n"
    "v_mfma_f32_16x16x32_bf16 v[32:35], v[64:67], v[152:155], v[32:35]\n"
    "ds_read_b128 v[96:99], v0 offset:43008\n"
    "v_mfma_f32_16x16x32_bf16 v[40:43], v[64:67], v[156:159], v[40:43]\n"
    "buffer_load_dwordx4 v[188:191], v0, s[8:11], s71 offen offset:3072 sc1\n"
    "v_mfma_f32_16x16x32_bf16 v[44:47], v[68:71], v[156:159], v[44:47]\n"
    "ds_read_b128 v[64:67], v0 offset:44032\n"
    "s_add_u32 s72, s80, 9998336\n"
    "v_mfma_f32_16x16x32_bf16 v[36:39], v[68:71], v[152:155], v[36:39]\n"
    "s_waitcnt vmcnt(6) lgkmcnt(4)\n"
    "v_mfma_f32_16x16x32_bf16 v[16:19], v[72:75], v[160:163], v[16:19]\n"
    "ds_read_b128 v[68:71], v0 offset:12288\n"
    "v_mfma_f32_16x16x32_bf16 v[20:23], v[72:75], v[164:167], v[20:23]\n"
    "buffer_load_dwordx4 v[100:103], v0, s[8:11], s72 offen offset:0 sc1\n"
    "v_mfma_f32_16x16x32_bf16 v[28:31], v[76:79], v[164:167], v[28:31]\n"
    "ds_read_b128 v[72:75], v0 offset:13312\n"
    "s_add_u32 s71, s80, 9981952\n"
    "v_mfma_f32_16x16x32_bf16 v[24:27], v[76:79], v[160:163], v[24:27]\n"
    "buffer_load_dwordx4 v[192:195], v0, s[8:11], s71 offen offset:0 sc1\n"
    "v_mfma_f32_16x16x32_bf16 v[32:35], v[80:83], v[160:163], v[32:35]\n"
    "ds_read_b128 v[76:79], v0 offset:45056\n"
    "v_mfma_f32_16x16x32_bf16 v[40:43], v[80:83], v[164:167], v[40:43]\n"
    "buffer_load_dwordx4 v[196:199], v0, s[8:11], s71 offen offset:1024 sc1\n"
    "v_mfma_f32_16x16x32_bf16 v[44:47], v[84:87], v[164:167], v[44:47]\n"
    "ds_read_b128 v[80:83], v0 offset:46080\n"
    "v_mfma_f32_16x16x32_bf16 v[36:39], v[84:87], v[160:163], v[36:39]\n"
    "s_waitcnt vmcnt(7) lgkmcnt(4)\n"
    "v_mfma_f32_16x16x32_bf16 v[16:19], v[88:91], v[168:171], v[16:19]\n"
    "ds_read_b128 v[84:87], v0 offset:14336\n"
    "v_mfma_f32_16x16x32_bf16 v[20:23], v[88:91], v[172:175], v[20:23]\n"
    "buffer_load_dwordx4 v[104:107], v0, s[8:11], s72 offen offset:1024 sc1\n"
    "v_mfma_f32_16x16x32_bf16 v[28:31], v[92:95], v[172:175], v[28:31]\n"
    "ds_read_b128 v[88:91], v0 offset:15360\n"
    "v_mfma_f32_16x16x32_bf16 v[24:27], v[92:95], v[168:171], v[24:27]\n"
    "buffer_load_dwordx4 v[200:203], v0, s[8:11], s71 offen offset:2048 sc1\n"
    "v_mfma_f32_16x16x32_bf16 v[32:35], v[96:99], v[168:171], v[32:35]\n"
    "ds_read_b128 v[92:95], v0 offset:47104\n"
    "v_mfma_f32_16x16x32_bf16 v[40:43], v[96:99], v[172:175], v[40:43]\n"
    "buffer_load_dwordx4 v[204:207], v0, s[8:11], s71 offen offset:3072 sc1\n"
    "v_mfma_f32_16x16x32_bf16 v[44:47], v[64:67], v[172:175], v[44:47]\n"
    "ds_read_b128 v[96:99], v0 offset:48128\n"
    "s_add_u32 s71, s80, 9986048\n"
    "v_mfma_f32_16x16x32_bf16 v[36:39], v[64:67], v[168:171], v[36:39]\n"
    "s_barrier\n"
    "s_waitcnt vmcnt(8) lgkmcnt(4)\n"
    "v_mfma_f32_16x16x32_bf16 v[16:19], v[68:71], v[176:179], v[16:19]\n"
    "ds_read_b128 v[64:67], v0 offset:16384\n"
    "v_mfma_f32_16x16x32_bf16 v[20:23], v[68:71], v[180:183], v[20:23]\n"
    "buffer_load_dwordx4 v[208:211], v0, s[8:11], s71 offen offset:0 sc1\n"
    "v_mfma_f32_16x16x32_bf16 v[28:31], v[72:75], v[180:183], v[28:31]\n"
    "ds_read_b128 v[68:71], v0 offset:17408\n"
    "v_mfma_f32_16x16x32_bf16 v[24:27], v[72:75], v[176:179], v[24:27]\n"
    "buffer_load_dwordx4 v[212:215], v0, s[8:11], s71 offen offset:1024 sc1\n"
    "v_mfma_f32_16x16x32_bf16 v[32:35], v[76:79], v[176:179], v[32:35]\n"
    "ds_read_b128 v[72:75], v0 offset:49152\n"
    "v_mfma_f32_16x16x32_bf16 v[40:43], v[76:79], v[180:183], v[40:43]\n"
    "v_mfma_f32_16x16x32_bf16 v[44:47], v[80:83], v[180:183], v[44:47]\n"
    "ds_read_b128 v[76:79], v0 offset:50176\n"
    "v_mfma_f32_16x16x32_bf16 v[36:39], v[80:83], v[176:179], v[36:39]\n"
    "s_waitcnt vmcnt(8) lgkmcnt(4)\n"
    "v_mfma_f32_16x16x32_bf16 v[16:19], v[84:87], v[184:187], v[16:19]\n"
    "ds_read_b128 v[80:83], v0 offset:18432\n"
    "buffer_load_dwordx4 v[216:219], v0, s[8:11], s71 offen offset:2048 sc1\n"
    "v_mfma_f32_16x16x32_bf16 v[20:23], v[84:87], v[188:191], v[20:23]\n"
    "buffer_load_dwordx4 v[220:223], v0, s[8:11], s71 offen offset:3072 sc1\n"
    "v_mfma_f32_16x16x32_bf16 v[28:31], v[88:91], v[188:191], v[28:31]\n"
    "ds_read_b128 v[84:87], v0 offset:19456\n"
    "s_add_u32 s71, s80, 9990144\n"
    "v_mfma_f32_16x16x32_bf16 v[24:27], v[88:91], v[184:187], v[24:27]\n"
    "buffer_load_dwordx4 v[224:227], v0, s[8:11], s71 offen offset:0 sc1\n"
    "v_mfma_f32_16x16x32_bf16 v[32:35], v[92:95], v[184:187], v[32:35]\n"
    "ds_read_b128 v[88:91], v0 offset:51200\n"
    "v_mfma_f32_16x16x32_bf16 v[40:43], v[92:95], v[188:191], v[40:43]\n"
    "v_mfma_f32_16x16x32_bf16 v[44:47], v[96:99], v[188:191], v[44:47]\n"
    "ds_read_b128 v[92:95], v0 offset:52224\n"
    "v_mfma_f32_16x16x32_bf16 v[36:39], v[96:99], v[184:187], v[36:39]\n"
    "s_waitcnt vmcnt(8) lgkmcnt(4)\n"
    "v_mfma_f32_16x16x32_bf16 v[16:19], v[64:67], v[192:195], v[16:19]\n"
    "ds_read_b128 v[96:99], v0 offset:20480\n"
    "buffer_load_dwordx4 v[228:231], v0, s[8:11], s71 offen offset:1024 sc1\n"
    "v_mfma_f32_16x16x32_bf16 v[20:23], v[64:67], v[196:199], v[20:23]\n"
    "buffer_load_dwordx4 v[232:235], v0, s[8:11], s71 offen offset:2048 sc1\n"
    "v_mfma_f32_16x16x32_bf16 v[28:31], v[68:71], v[196:199], v[28:31]\n"
    "ds_read_b128 v[64:67], v0 offset:21504\n"
    "v_mfma_f32_16x16x32_bf16 v[24:27], v[68:71], v[192:195], v[24:27]\n"
    "buffer_load_dwordx4 v[236:239], v0, s[8:11], s71 offen offset:3072 sc1\n"
    "v_mfma_f32_16x16x32_bf16 v[32:35], v[72:75], v[192:195], v[32:35]\n"
    "ds_read_b128 v[68:71], v0 offset:53248\n"
    "s_add_u32 s71, s80, 9994240\n"
    "v_mfma_f32_16x16x32_bf16 v[40:43], v[72:75], v[196:199], v[40:43]\n"
    "v_mfma_f32_16x16x32_bf16 v[44:47], v[76:79], v[196:199], v[44:47]\n"
    "ds_read_b128 v[72:75], v0 offset:54272\n"
    "v_mfma_f32_16x16x32_bf16 v[36:39], v[76:79], v[192:195], v[36:39]\n"
    "s_waitcnt vmcnt(8) lgkmcnt(4)\n"
    "v_mfma_f32_16x16x32_bf16 v[16:19], v[80:83], v[200:203], v[16:19]\n"
    "ds_read_b128 v[76:79], v0 offset:22528\n"
    "buffer_load_dwordx4 v[240:243], v0, s[8:11], s71 offen offset:0 sc1\n"
    "v_add_f32_dpp v112, v100, v100 row_ror:8 row_mask:0xf bank_mask:3\n"
    "v_add_f32_dpp v113, v101, v101 row_ror:8 row_mask:0xf bank_mask:3\n"
    "v_mfma_f32_16x16x32_bf16 v[20:23], v[80:83], v[204:207], v[20:23]\n"
    "buffer_load_dwordx4 v[244:247], v0, s[8:11], s71 offen offset:1024 sc1\n"
    "v_mfma_f32_16x16x32_bf16 v[28:31], v[84:87], v[204:207], v[28:31]\n"
    "ds_read_b128 v[80:83], v0 offset:23552\n"
    "v_add_f32_dpp v114, v104, v104 row_ror:8 row_mask:0xf bank_mask:3\n"
    "v_mfma_f32_16x16x32_bf16 v[24:27], v[84:87], v[200:203], v[24:27]\n"
    "buffer_load_dwordx4 v[248:251], v0, s[8:11], s71 offen offset:2048 sc1\n"
    "v_add_f32_dpp v115, v105, v105 row_ror:8 row_mask:0xf bank_mask:3\n"
    "v_mfma_f32_16x16x32_bf16 v[32:35], v[88:91], v[200:203], v[32:35]\n"
    "ds_read_b128 v[84:87], v0 offset:55296\n"
    "v_add_f32_dpp v112, v102, v102 row_ror:8 row_mask:0xf bank_mask:12\n"
    "v_mfma_f32_16x16x32_bf16 v[40:43], v[88:91], v[204:207], v[40:43]\n"
    "v_add_f32_dpp v113, v103, v103 row_ror:8 row_mask:0xf bank_mask:12\n"
    "v_add_f32_dpp v114, v106, v106 row_ror:8 row_mask:0xf bank_mask:12\n"
    "v_mfma_f32_16x16x32_bf16 v[44:47], v[92:95], v[204:207], v[44:47]\n"
    "ds_read_b128 v[88:91], v0 offset:56320\n"
    "v_add_f32_dpp v115, v107, v107 row_ror:8 row_mask:0xf bank_mask:12\n"
    "v_mfma_f32_16x16x32_bf16 v[36:39], v[92:95], v[200:203], v[36:39]\n"
    "v_permlane32_swap_b32 v112, v113\n"
    "s_waitcnt vmcnt(9) lgkmcnt(4)\n"
    "v_mfma_f32_16x16x32_bf16 v[16:19], v[96:99], v[208:211], v[16:19]\n"
    "ds_read_b128 v[92:95], v0 offset:24576\n"
    "buffer_load_dwordx4 v[252:255], v0, s[8:11], s71 offen offset:3072 sc1\n"
    "v_mfma_f32_16x16x32_bf16 v[20:23], v[96:99], v[212:215], v[20:23]\n"
    "s_add_u32 m0, s66, 65536\n"
    "s_add_u32 s69, s75, 65536\n"
    "v_mfma_f32_16x16x32_bf16 v[28:31], v[64:67], v[212:215], v[28:31]\n"
    "ds_read_b128 v[96:99], v0 offset:25600\n"
    "v_permlane32_swap_b32 v114, v115\n"
    "v_mfma_f32_16x16x32_bf16 v[24:27], v[64:67], v[208:211], v[24:27]\n"
    "buffer_load_dwordx4 v0, s[8:11], s69 offen offset:0 sc1 lds\n"
    "v_mfma_f32_16x16x32_bf16 v[32:35], v[68:71], v[208:211], v[32:35]\n"
    "ds_read_b128 v[64:67], v0 offset:57344\n"
    "v_add_f32 v112, v112, v113\n"
    "v_mfma_f32_16x16x32_bf16 v[40:43], v[68:71], v[212:215], v[40:43]\n"
    "buffer_load_dwordx4 v0, s[8:11], s69 offen offset:1024 sc1 lds\n"
    "v_mfma_f32_16x16x32_bf16 v[44:47], v[72:75], v[212:215], v[44:47]\n"
    "ds_read_b128 v[68:71], v0 offset:58368\n"
    "v_add_f32 v114, v114, v115\n"
    "v_mfma_f32_16x16x32_bf16 v[36:39], v[72:75], v[208:211], v[36:39]\n"
    "s_nop 0\n"
    "v_permlane16_swap_b32 v112, v114\n"
    "s_waitcnt vmcnt(10) lgkmcnt(4)\n"
    "v_mfma_f32_16x16x32_bf16 v[16:19], v[76:79], v[216:219], v[16:19]\n"
    "ds_read_b128 v[72:75], v0 offset:26624\n"
    "buffer_load_dwordx4 v0, s[8:11], s69 offen offset:2048 sc1 lds\n"
    "v_mfma_f32_16x16x32_bf16 v[20:23], v[76:79], v[220:223], v[20:23]\n"
    "buffer_load_dwordx4 v0, s[8:11], s69 offen offset:3072 sc1 lds\n"
    "v_mfma_f32_16x16x32_bf16 v[28:31], v[80:83], v[220:223], v[28:31]\n"
    "ds_read_b128 v[76:79], v0 offset:27648\n"
    "s_add_u32 m0, s66, 69632\n"
    "v_add_f32 v112, v112, v114\n"
    "v_mfma_f32_16x16x32_bf16 v[24:27], v[80:83], v[216:219], v[24:27]\n"
    "s_add_u32 s69, s75, 69632\n"
    "v_mul_f32 v108, 0xbb000000, v112\n"
    "v_mfma_f32_16x16x32_bf16 v[32:35], v[84:87], v[216:219], v[32:35]\n"
    "ds_read_b128 v[80:83], v0 offset:59392\n"
    "v_mul_f32 v116, 0xbb000000, v112\n"
    "v_mfma_f32_16x16x32_bf16 v[40:43], v[84:87], v[220:223], v[40:43]\n"
    "buffer_load_dwordx4 v0, s[8:11], s69 offen offset:0 sc1 lds\n"
    "v_mfma_f32_16x16x32_bf16 v[44:47], v[88:91], v[220:223], v[44:47]\n"
    "ds_read_b128 v[84:87], v0 offset:60416\n"
    "v_permlane32_swap_b32 v108, v116\n"
    "v_mfma_f32_16x16x32_bf16 v[36:39], v[88:91], v[216:219], v[36:39]\n"
    "buffer_load_dwordx4 v0, s[8:11], s69 offen offset:1024 sc1 lds\n"
    "s_waitcnt vmcnt(12) lgkmcnt(4)\n"
    "v_mfma_f32_16x16x32_bf16 v[16:19], v[92:95], v[224:227], v[16:19]\n"
    "ds_read_b128 v[88:91], v0 offset:28672\n"
    "v_fma_f32 v109, -v108, v108, -v116\n"
    "v_mfma_f32_16x16x32_bf16 v[20:23], v[92:95], v[228:231], v[20:23]\n"
    "buffer_load_dwordx4 v0, s[8:11], s69 offen offset:2048 sc1 lds\n"
    "v_mfma_f32_16x16x32_bf16 v[28:31], v[96:99], v[228:231], v[28:31]\n"
    "ds_read_b128 v[92:95], v0 offset:29696\n"
    "v_cvt_pk_bf16_f32 v124, v108, v108\n"
    "v_max_f32 v116, 0, v109\n"
    "v_mfma_f32_16x16x32_bf16 v[24:27], v[96:99], v[224:227], v[24:27]\n"
    "buffer_load_dwordx4 v0, s[8:11], s69 offen offset:3072 sc1 lds\n"
    "v_mfma_f32_16x16x32_bf16 v[32:35], v[64:67], v[224:227], v[32:35]\n"
    "ds_read_b128 v[96:99], v0 offset:61440\n"
    "v_lshrrev_b32 v10, 1, v0\n"
    "v_mfma_f32_16x16x32_bf16 v[40:43], v[64:67], v[228:231], v[40:43]\n"
    "v_and_b32 v11, 0xe0, v0\n"
    "v_cmp_eq_u32 vcc, 0, v11\n"
    "v_lshrrev_b32 v11, 4, v0\n"
    "v_and_b32 v11, 0x31, v11\n"
    "v_mul_u32_u24 v11, 65, v11\n"
    "v_and_b32 v11, 0x70, v11\n"
    "v_cndmask_b32 v11, 2.0, v11, vcc\n"
    "v_lshlrev_b32 v112, 16, v124\n"
    "v_mfma_f32_16x16x32_bf16 v[44:47], v[68:71], v[228:231], v[44:47]\n"
    "ds_read_b128 v[64:67], v0 offset:62464\n"
    "s_lshl_b32 s62, s59, 9\n"
    "s_add_u32 s62, s62, s90\n"
    "s_lshr_b32 s63, s62, 2\n"
    "s_add_u32 s62, s62, 659456\n"
    "s_add_u32 s63, s63, 1708032\n"
    "v_mfma_f32_16x16x32_bf16 v[36:39], v[68:71], v[224:227], v[36:39]\n"
    "buffer_store_dwordx2 a[236:237], v10, s[8:11], s62 offen sc1\n"
    "s_waitcnt vmcnt(13) lgkmcnt(4)\n"
    "v_mfma_f32_16x16x32_bf16 v[16:19], v[72:75], v[232:235], v[16:19]\n"
    "ds_read_b128 v[68:71], v0 offset:30720\n"
    "v_add_f32 v116, 0x3727c5ac, v116\n"
    "v_mfma_f32_16x16x32_bf16 v[20:23], v[72:75], v[236:239], v[20:23]\n"
    "buffer_store_dwordx4 a[236:239], v11, s[8:11], s63 offen sc1\n"
    "v_sub_f32 v112, v108, v112\n"
    "v_mfma_f32_16x16x32_bf16 v[28:31], v[76:79], v[236:239], v[28:31]\n"
    "ds_read_b128 v[72:75], v0 offset:31744\n"
    "v_fmac_f32 v116, 0, v109\n"
    "v_mfma_f32_16x16x32_bf16 v[24:27], v[76:79], v[232:235], v[24:27]\n"
    "buffer_store_dwordx4 a[4:7], v0, s[8:11], s40 offen sc1\n"
    "v_mfma_f32_16x16x32_bf16 v[32:35], v[80:83], v[232:235], v[32:35]\n"
    "ds_read_b128 v[76:79], v0 offset:63488\n"
    "s_add_u32 s74, s40, 32768\n"
    "v_mfma_f32_16x16x32_bf16 v[40:43], v[80:83], v[236:239], v[40:43]\n"
    "buffer_store_dwordx4 a[8:11], v0, s[8:11], s74 offen sc1\n"
    "v_cvt_pk_bf16_f32 v125, v108, v112\n"
    "v_mfma_f32_16x16x32_bf16 v[44:47], v[84:87], v[236:239], v[44:47]\n"
    "ds_read_b128 v[80:83], v0 offset:64512\n"
    "v_mov_b32 v126, v124\n"
    "v_mfma_f32_16x16x32_bf16 v[36:39], v[84:87], v[232:235], v[36:39]\n"
    "s_waitcnt vmcnt(14) lgkmcnt(4)\n"
    "v_mfma_f32_16x16x32_bf16 v[16:19], v[88:91], v[240:243], v[16:19]\n"
    "buffer_store_dwordx4 a[12:15], v0, s[8:11], s41 offen sc1\n"
    "v_mfma_f32_16x16x32_bf16 v[20:23], v[88:91], v[244:247], v[20:23]\n"
    "v_cmp_gt_u32 vcc, 0x100, v0\n"
    "s_nop 1\n"
    "v_cndmask_b32 v7, 2.0, v0, vcc\n"
    "v_rsq_f32 v118, v116\n"
    "v_mfma_f32_16x16x32_bf16 v[28:31], v[92:95], v[244:247], v[28:31]\n"
    "s_lshl_b32 s74, s59, 9\n"
    "s_add_u32 s74, s74, s90\n"
    "s_lshr_b32 s74, s74, 1\n"
    "s_add_u32 s74, s74, 1970176\n"
    "v_mfma_f32_16x16x32_bf16 v[24:27], v[92:95], v[240:243], v[24:27]\n"
    "buffer_store_dwordx4 a[28:31], v7, s[8:11], s74 offen sc1\n"
    "v_mfma_f32_16x16x32_bf16 v[32:35], v[96:99], v[240:243], v[32:35]\n"
    "s_add_u32 s74, s42, 0\n"
    "v_mov_b32 v127, v125\n"
    "v_mfma_f32_16x16x32_bf16 v[40:43], v[96:99], v[244:247], v[40:43]\n"
    "buffer_store_dwordx4 a[16:19], v0, s[8:11], s74 offen sc1\n"
    "v_mov_b32 v117, v116\n"
    "v_mfma_f32_16x16x32_bf16 v[44:47], v[64:67], v[244:247], v[44:47]\n"
    "v_permlane16_swap_b32 v124, v126\n"
    "v_mfma_f32_16x16x32_bf16 v[36:39], v[64:67], v[240:243], v[36:39]\n"
    "v_permlane16_swap_b32 v125, v127\n"
    "s_nop 1\n"
    "v_mfma_f32_16x16x16_bf16 v[16:19], a[176:177], v[124:125], v[16:19]\n"
    "v_mfma_f32_16x16x16_bf16 v[20:23], a[176:177], v[126:127], v[20:23]\n"
    "v_mfma_f32_16x16x16_bf16 v[24:27], a[178:179], v[124:125], v[24:27]\n"
    "v_mfma_f32_16x16x16_bf16 v[28:31], a[178:179], v[126:127], v[28:31]\n"
    "v_mfma_f32_16x16x16_bf16 v[32:35], a[180:181], v[124:125], v[32:35]\n"
    "v_mfma_f32_16x16x16_bf16 v[40:43], a[180:181], v[126:127], v[40:43]\n"
    "v_mfma_f32_16x16x16_bf16 v[36:39], a[182:183], v[124:125], v[36:39]\n"
    "v_mfma_f32_16x16x16_bf16 v[44:47], a[182:183], v[126:127], v[44:47]\n"
    "s_waitcnt vmcnt(15) lgkmcnt(0)\n"
    "v_mfma_f32_16x16x32_bf16 v[16:19], v[68:71], v[248:251], v[16:19]\n"
    "v_mul_f32 v120, 0x3fb8aa3b, v118\n"
    "v_mov_b32 v119, v118\n"
    "v_mfma_f32_16x16x32_bf16 v[20:23], v[68:71], v[252:255], v[20:23]\n"
    "s_nop 0\n"
    "v_permlane16_swap_b32 v118, v119\n"
    "v_mfma_f32_16x16x32_bf16 v[28:31], v[72:75], v[252:255], v[28:31]\n"
    "v_mfma_f32_16x16x32_bf16 v[24:27], v[72:75], v[248:251], v[24:27]\n"
    "v_mfma_f32_16x16x32_bf16 v[32:35], v[76:79], v[248:251], v[32:35]\n"
    "v_mfma_f32_16x16x32_bf16 v[40:43], v[76:79], v[252:255], v[40:43]\n"
    "v_mfma_f32_16x16x32_bf16 v[44:47], v[80:83], v[252:255], v[44:47]\n"
    "v_mfma_f32_16x16x32_bf16 v[36:39], v[80:83], v[248:251], v[36:39]\n"
    "v_permlane16_swap_b32 v16, v20\n"
    "v_permlane16_swap_b32 v17, v21\n"
    "v_permlane16_swap_b32 v18, v22\n"
    "v_permlane16_swap_b32 v19, v23\n"
    "s_barrier\n"
    "v_permlane16_swap_b32 v24, v28\n"
    "v_permlane16_swap_b32 v25, v29\n"
    "s_add_u32 s74, s42, 16384\n"
    "buffer_store_dwordx4 a[20:23], v0, s[8:11], s74 offen sc1\n"
    "v_permlane16_swap_b32 v26, v30\n"
    "v_permlane16_swap_b32 v27, v31\n"
    "v_accvgpr_read_b32 v10, a244\n"
    "v_accvgpr_read_b32 v14, a245\n"
    "v_xor_b32_e64 v11, 16, v10\n"
    "v_pk_mul_f32 v[32:33], v[32:33], v[118:119] op_sel:[0,0] op_sel_hi:[1,0]\n"
    "v_pk_mul_f32 v[34:35], v[34:35], v[118:119] op_sel:[0,0] op_sel_hi:[1,0]\n"
    "v_pk_mul_f32 v[36:37], v[36:37], v[118:119] op_sel:[0,0] op_sel_hi:[1,0]\n"
    "v_pk_mul_f32 v[38:39], v[38:39], v[118:119] op_sel:[0,0] op_sel_hi:[1,0]\n"
    "v_pk_mul_f32 v[40:41], v[40:41], v[118:119] op_sel:[0,1] op_sel_hi:[1,1]\n"
    "v_pk_mul_f32 v[42:43], v[42:43], v[118:119] op_sel:[0,1] op_sel_hi:[1,1]\n"
    "v_pk_mul_f32 v[44:45], v[44:45], v[118:119] op_sel:[0,1] op_sel_hi:[1,1]\n"
    "v_pk_mul_f32 v[46:47], v[46:47], v[118:119] op_sel:[0,1] op_sel_hi:[1,1]\n"
    "v_max3_f32 v80, v16, v17, v18\n"
    "v_max3_f32 v81, v24, v25, v26\n"
    "v_min3_f32 v82, v16, v17, v18\n"
    "v_min3_f32 v83, v24, v25, v26\n"
    "v_max3_f32 v80, v80, v19, v20\n"
    "s_add_u32 s74, s43, 32768\n"
    "buffer_store_dwordx4 a[24:27], v0, s[8:11], s74 offen sc1\n"
    "v_max3_f32 v81, v81, v27, v28\n"
    "v_min3_f32 v82, v82, v19, v20\n"
    "v_min3_f32 v83, v83, v27, v28\n"
    "v_max3_f32 v80, v80, v21, v22\n"
    "v_max3_f32 v81, v81, v29, v30\n"
    "v_min3_f32 v82, v82, v21, v22\n"
    "v_min3_f32 v83, v83, v29, v30\n"
    "v_max_f32 v80, v80, v23\n"
    "v_max_f32 v81, v81, v31\n"
    "v_min_f32 v82, v82, v23\n"
    "v_min_f32 v83, v83, v31\n"
    "v_permlane32_swap_b32 v80, v81\n"
    "s_nop 0\n"
    "v_permlane32_swap_b32 v82, v83\n"
    "v_max_f32 v80, v80, v81\n"
    "v_min_f32 v82, v82, v83\n"
    "v_mul_f32 v80, v80, v120\n"
    "v_mul_f32 v82, v82, v120\n"
    "v_mov_b32 v81, v80\n"
    "s_add_u32 m0, s66, 135168\n"
    "s_add_u32 s69, s76, 294912\n"
    "buffer_load_dwordx4 v15, s[20:23], s69 offen offset:0 lds\n"
    "v_mov_b32 v83, v82\n"
    "v_permlane32_swap_b32 v80, v81\n"
    "s_nop 0\n"
    "v_permlane32_swap_b32 v82, v83\n"
    "v_pk_fma_f32 v[64:65], v[16:17], v[120:121], v[82:83] op_sel:[0,0,0] op_sel_hi:[1,0,0] neg_lo:[1,0,0] neg_hi:[1,0,0]\n"
    "v_pk_fma_f32 v[66:67], v[18:19], v[120:121], v[82:83] op_sel:[0,0,0] op_sel_hi:[1,0,0] neg_lo:[1,0,0] neg_hi:[1,0,0]\n"
    "v_pk_fma_f32 v[68:69], v[20:21], v[120:121], v[82:83] op_sel:[0,0,0] op_sel_hi:[1,0,0] neg_lo:[1,0,0] neg_hi:[1,0,0]\n"
    "v_pk_fma_f32 v[70:71], v[22:23], v[120:121], v[82:83] op_sel:[0,0,0] op_sel_hi:[1,0,0] neg_lo:[1,0,0] neg_hi:[1,0,0]\n"
    "v_exp_f32 v64, v64\n"
    "v_pk_fma_f32 v[16:17], v[16:17], v[120:121], v[80:81] op_sel:[0,0,0] op_sel_hi:[1,0,0] neg_lo:[0,0,1] neg_hi:[0,0,1]\n"
    "v_exp_f32 v65, v65\n"
    "v_pk_fma_f32 v[18:19], v[18:19], v[120:121], v[80:81] op_sel:[0,0,0] op_sel_hi:[1,0,0] neg_lo:[0,0,1] neg_hi:[0,0,1]\n"
    "v_exp_f32 v66, v66\n"
    "v_pk_fma_f32 v[20:21], v[20:21], v[120:121], v[80:81] op_sel:[0,0,0] op_sel_hi:[1,0,0] neg_lo:[0,0,1] neg_hi:[0,0,1]\n"
    "v_exp_f32 v67, v67\n"
    "v_pk_fma_f32 v[22:23], v[22:23], v[120:121], v[80:81] op_sel:[0,0,0] op_sel_hi:[1,0,0] neg_lo:[0,0,1] neg_hi:[0,0,1]\n"
    "v_exp_f32 v68, v68\n"
    "v_cvt_pk_bf16_f32 v88, v32, v33\n"
    "v_exp_f32 v69, v69\n"
    "s_add_u32 s69, s76, 302080\n"
    "buffer_load_dwordx4 v15, s[20:23], s69 offen offset:1024 lds\n"
    "v_cvt_pk_bf16_f32 v89, v34, v35\n"
    "v_exp_f32 v70, v70\n"
    "ds_write_b64 v14, v[88:89] offset:16384\n"
    "v_exp_f32 v71, v71\n"
    "v_cvt_pk_bf16_f32 v90, v36, v37\n"
    "v_cvt_pk_bf16_f32 v91, v38, v39\n"
    "ds_write_b64 v14, v[90:91] offset:20480\n"
    "v_exp_f32 v16, v16\n"
    "v_pk_fma_f32 v[72:73], v[24:25], v[120:121], v[82:83] op_sel:[0,0,1] op_sel_hi:[1,0,1] neg_lo:[1,0,0] neg_hi:[1,0,0]\n"
    "v_exp_f32 v17, v17\n"
    "v_pk_fma_f32 v[74:75], v[26:27], v[120:121], v[82:83] op_sel:[0,0,1] op_sel_hi:[1,0,1] neg_lo:[1,0,0] neg_hi:[1,0,0]\n"
    "v_exp_f32 v18, v18\n"
    "v_pk_fma_f32 v[76:77], v[28:29], v[120:121], v[82:83] op_sel:[0,0,1] op_sel_hi:[1,0,1] neg_lo:[1,0,0] neg_hi:[1,0,0]\n"
    "v_exp_f32 v19, v19\n"
    "v_pk_fma_f32 v[78:79], v[30:31], v[120:121], v[82:83] op_sel:[0,0,1] op_sel_hi:[1,0,1] neg_lo:[1,0,0] neg_hi:[1,0,0]\n"
    "v_exp_f32 v20, v20\n"
    "v_cvt_pk_bf16_f32 v92, v40, v41\n"
    "v_exp_f32 v21, v21\n"
    "v_cvt_pk_bf16_f32 v93, v42, v43\n"
    "v_exp_f32 v22, v22\n"
    "ds_write_b64 v14, v[92:93] offset:16896\n"
    "s_add_u32 s69, s76, 309248\n"
    "buffer_load_dwordx4 v15, s[20:23], s69 offen offset:2048 lds\n"
    "v_exp_f32 v23, v23\n"
    "v_cvt_pk_bf16_f32 v94, v44, v45\n"
    "v_cvt_pk_bf16_f32 v95, v46, v47\n"
    "ds_write_b64 v14, v[94:95] offset:20992\n"
    "v_exp_f32 v72, v72\n"
    "v_pk_fma_f32 v[24:25], v[24:25], v[120:121], v[80:81] op_sel:[0,0,1] op_sel_hi:[1,0,1] neg_lo:[0,0,1] neg_hi:[0,0,1]\n"
    "v_exp_f32 v73, v73\n"
    "v_pk_fma_f32 v[26:27], v[26:27], v[120:121], v[80:81] op_sel:[0,0,1] op_sel_hi:[1,0,1] neg_lo:[0,0,1] neg_hi:[0,0,1]\n"
    "v_exp_f32 v74, v74\n"
    "v_pk_fma_f32 v[28:29], v[28:29], v[120:121], v[80:81] op_sel:[0,0,1] op_sel_hi:[1,0,1] neg_lo:[0,0,1] neg_hi:[0,0,1]\n"
    "v_exp_f32 v75, v75\n"
    "v_pk_fma_f32 v[30:31], v[30:31], v[120:121], v[80:81] op_sel:[0,0,1] op_sel_hi:[1,0,1] neg_lo:[0,0,1] neg_hi:[0,0,1]\n"
    "v_exp_f32 v76, v76\n"
    "v_accvgpr_read_b32 v32, a246\n"
    "v_exp_f32 v77, v77\n"
    "v_accvgpr_read_b32 v33, a247\n"
    "v_exp_f32 v78, v78\n"
    "v_accvgpr_read_b32 v34, a250\n"
    "v_exp_f32 v79, v79\n"
    "s_add_u32 s69, s76, 316416\n"
    "buffer_load_dwordx4 v15, s[20:23], s69 offen offset:3072 lds\n"
    "v_exp_f32 v24, v24\n"
    "v_pk_add_f32 v[92:93], v[64:65], v[66:67] op_sel:[0,0] op_sel_hi:[1,1]\n"
    "v_exp_f32 v25, v25\n"
    "v_pk_add_f32 v[94:95], v[68:69], v[70:71] op_sel:[0,0] op_sel_hi:[1,1]\n"
    "v_exp_f32 v26, v26\n"
    "v_pk_add_f32 v[84:85], v[16:17], v[18:19] op_sel:[0,0] op_sel_hi:[1,1]\n"
    "v_exp_f32 v27, v27\n"
    "v_pk_add_f32 v[86:87], v[20:21], v[22:23] op_sel:[0,0] op_sel_hi:[1,1]\n"
    "v_exp_f32 v28, v28\n"
    "v_pk_add_f32 v[96:97], v[72:73], v[74:75] op_sel:[0,0] op_sel_hi:[1,1]\n"
    "v_exp_f32 v29, v29\n"
    "v_pk_add_f32 v[98:99], v[76:77], v[78:79] op_sel:[0,0] op_sel_hi:[1,1]\n"
    "v_exp_f32 v30, v30\n"
    "v_exp_f32 v31, v31\n"
    "v_pk_add_f32 v[88:89], v[24:25], v[26:27] op_sel:[0,0] op_sel_hi:[1,1]\n"
    "v_pk_add_f32 v[90:91], v[28:29], v[30:31] op_sel:[0,0] op_sel_hi:[1,1]\n"
    "v_pk_add_f32 v[84:85], v[84:85], v[86:87] op_sel:[0,0] op_sel_hi:[1,1]\n"
    "v_pk_add_f32 v[88:89], v[88:89], v[90:91] op_sel:[0,0] op_sel_hi:[1,1]\n"
    "v_accvgpr_read_b32 v7, a251\n"
    "s_add_u32 s74, s83, 18878464\n"
    "buffer_store_dwordx4 a[236:239], v7, s[8:11], s74 offen sc1\n"
    "v_pk_add_f32 v[92:93], v[92:93], v[94:95] op_sel:[0,0] op_sel_hi:[1,1]\n"
    "v_pk_add_f32 v[96:97], v[96:97], v[98:99] op_sel:[0,0] op_sel_hi:[1,1]\n"
    "v_add_f32 v80, v84, v85\n"
    "v_add_f32 v81, v88, v89\n"
    "v_add_f32 v82, v92, v93\n"
    "v_add_f32 v83, v96, v97\n"
    "v_permlane32_swap_b32 v80, v81\n"
    "s_nop 0\n"
    "v_permlane32_swap_b32 v82, v83\n"
    "v_add_f32 v80, v80, v81\n"
    "v_add_f32 v82, v82, v83\n"
    "v_rcp_f32 v80, v80\n"
    "v_rcp_f32 v82, v82\n"
    "v_mov_b32 v81, v80\n"
    "v_mov_b32 v83, v82\n"
    "s_nop 0\n"
    "v_permlane32_swap_b32 v80, v81\n"
    "v_permlane32_swap_b32 v82, v83\n"
    "v_pk_mul_f32 v[16:17], v[16:17], v[80:81] op_sel:[0,0] op_sel_hi:[1,0]\n"
    "v_pk_mul_f32 v[18:19], v[18:19], v[80:81] op_sel:[0,0] op_sel_hi:[1,0]\n"
    "s_add_u32 s74, s83, 18878976\n"
    "buffer_store_dwordx4 a[236:239], v7, s[8:11], s74 offen sc1\n"
    "v_pk_mul_f32 v[20:21], v[20:21], v[80:81] op_sel:[0,0] op_sel_hi:[1,0]\n"
    "v_pk_mul_f32 v[22:23], v[22:23], v[80:81] op_sel:[0,0] op_sel_hi:[1,0]\n"
    "v_cvt_pk_bf16_f32 v100, v16, v17\n"
    "v_cvt_pk_bf16_f32 v101, v18, v19\n"
    "v_cvt_pk_bf16_f32 v102, v20, v21\n"
    "v_cvt_pk_bf16_f32 v103, v22, v23\n"
    "ds_write_b64 v10, v[100:101] offset:0\n"
    "ds_write_b64 v11, v[102:103] offset:0\n"
    "v_pk_mul_f32 v[64:65], v[64:65], v[82:83] op_sel:[0,0] op_sel_hi:[1,0]\n"
    "v_pk_mul_f32 v[66:67], v[66:67], v[82:83] op_sel:[0,0] op_sel_hi:[1,0]\n"
    "v_pk_mul_f32 v[68:69], v[68:69], v[82:83] op_sel:[0,0] op_sel_hi:[1,0]\n"
    "v_pk_mul_f32 v[70:71], v[70:71], v[82:83] op_sel:[0,0] op_sel_hi:[1,0]\n"
    "v_cvt_pk_bf16_f32 v104, v64, v65\n"
    "v_cvt_pk_bf16_f32 v105, v66, v67\n"
    "v_cvt_pk_bf16_f32 v106, v68, v69\n"
    "v_cvt_pk_bf16_f32 v107, v70, v71\n"
    "ds_write_b64 v10, v[104:105] offset:4096\n"
    "ds_write_b64 v11, v[106:107] offset:4096\n"
    "v_pk_mul_f32 v[24:25], v[24:25], v[80:81] op_sel:[0,1] op_sel_hi:[1,1]\n"
    "v_pk_mul_f32 v[26:27], v[26:27], v[80:81] op_sel:[0,1] op_sel_hi:[1,1]\n"
    "v_pk_mul_f32 v[28:29], v[28:29], v[80:81] op_sel:[0,1] op_sel_hi:[1,1]\n"
    "v_pk_mul_f32 v[30:31], v[30:31], v[80:81] op_sel:[0,1] op_sel_hi:[1,1]\n"
    "v_cvt_pk_bf16_f32 v108, v24, v25\n"
    "v_cvt_pk_bf16_f32 v109, v26, v27\n"
    "v_cvt_pk_bf16_f32 v110, v28, v29\n"
    "v_cvt_pk_bf16_f32 v111, v30, v31\n"
    "ds_write_b64 v10, v[108:109] offset:8192\n"
    "ds_write_b64 v11, v[110:111] offset:8192\n"
    "v_pk_mul_f32 v[72:73], v[72:73], v[82:83] op_sel:[0,1] op_sel_hi:[1,1]\n"
    "v_pk_mul_f32 v[74:75], v[74:75], v[82:83] op_sel:[0,1] op_sel_hi:[1,1]\n"
    "v_pk_mul_f32 v[76:77], v[76:77], v[82:83] op_sel:[0,1] op_sel_hi:[1,1]\n"
    "v_pk_mul_f32 v[78:79], v[78:79], v[82:83] op_sel:[0,1] op_sel_hi:[1,1]\n"
    "v_cvt_pk_bf16_f32 v112, v72, v73\n"
    "v_cvt_pk_bf16_f32 v113, v74, v75\n"
    "v_cvt_pk_bf16_f32 v114, v76, v77\n"
    "v_cvt_pk_bf16_f32 v115, v78, v79\n"
    "ds_write_b64 v10, v[112:113] offset:12288\n"
    "ds_write_b64 v11, v[114:115] offset:12288\n"
    "s_waitcnt vmcnt(6) lgkmcnt(0)\n"
    "s_barrier\n"
    "ds_read_b64_tr_b16 v[16:17], v32 offset:0\n"
    "ds_read_b64_tr_b16 v[18:19], v32 offset:512\n"
    "ds_read_b64_tr_b16 v[64:65], v33 offset:0\n"
    "ds_read_b64_tr_b16 v[66:67], v33 offset:512\n"
    "ds_read_b64_tr_b16 v[20:21], v32 offset:1024\n"
    "ds_read_b64_tr_b16 v[22:23], v32 offset:1536\n"
    "ds_read_b64_tr_b16 v[68:69], v33 offset:1024\n"
    "ds_read_b64_tr_b16 v[70:71], v33 offset:1536\n"
    "ds_read_b64_tr_b16 v[24:25], v32 offset:2048\n"
    "Lpad2:\n"
    "s_cbranch_execz Lpad6\n"
    "ds_read_b64_tr_b16 v[26:27], v32 offset:2560\n"
    "ds_read_b64_tr_b16 v[72:73], v33 offset:2048\n"
    "ds_read_b64_tr_b16 v[74:75], v33 offset:2560\n"
    "ds_read_b64_tr_b16 v[28:29], v32 offset:3072\n"
    "ds_read_b64_tr_b16 v[30:31], v32 offset:3584\n"
    "ds_read_b64_tr_b16 v[76:77], v33 offset:3072\n"
    "ds_read_b64_tr_b16 v[78:79], v33 offset:3584\n"
    "s_mov_b32 s65, 4096\n"
    "buffer_atomic_add a255, v34, s[8:11], s65 offen\n"
    "v_lshrrev_b32 v10, 1, v0\n"
    "v_and_b32 v11, 0xe0, v0\n"
    "v_cmp_eq_u32 vcc, 0, v11\n"
    "v_lshrrev_b32 v11, 4, v0\n"
    "v_and_b32 v11, 0x31, v11\n"
    "v_mul_u32_u24 v11, 65, v11\n"
    "v_and_b32 v11, 0x70, v11\n"
    "v_cndmask_b32 v11, 2.0, v11, vcc\n"
    "s_lshl_b32 s62, s59, 9\n"
    "s_add_u32 s62, s62, s90\n"
    "s_lshr_b32 s63, s62, 2\n"
    "s_add_u32 s62, s62, 659456\n"
    "s_add_u32 s63, s63, 1708032\n"
    "s_waitcnt lgkmcnt(12)\n"
    "v_mfma_f32_16x16x32_bf16 v[36:39], v[16:19], v[64:67], 0\n"
    "v_mfma_f32_16x16x32_bf16 v[40:43], v[16:19], a[240:243], 0\n"
    "s_waitcnt lgkmcnt(8)\n"
    "v_mfma_f32_16x16x32_bf16 v[36:39], v[20:23], v[68:71], v[36:39]\n"
    "v_mfma_f32_16x16x32_bf16 v[40:43], v[20:23], a[240:243], v[40:43]\n"
    "s_waitcnt lgkmcnt(4)\n"
    "v_mfma_f32_16x16x32_bf16 v[36:39], v[24:27], v[72:75], v[36:39]\n"
    "v_mfma_f32_16x16x32_bf16 v[40:43], v[24:27], a[240:243], v[40:43]\n"
    "s_waitcnt lgkmcnt(0)\n"
    "v_mfma_f32_16x16x32_bf16 v[36:39], v[28:31], v[76:79], v[36:39]\n"
    "v_mfma_f32_16x16x32_bf16 v[40:43], v[28:31], a[240:243], v[40:43]\n"
    "s_nop 6\n"
    "v_cvt_pk_bf16_f32 v36, v36, v37\n"
    "v_cvt_pk_bf16_f32 v37, v38, v39\n"
    "buffer_store_dwordx2 v[36:37], v10, s[8:11], s62 offen sc1\n"
    "buffer_store_dwordx4 v[40:43], v11, s[8:11], s63 offen sc1\n"
    "v_and_b32 v14, 0x3c0, v0\n"
    "v_add_u32 v14, v14, v0\n"
    "s_lshr_b32 s64, s90, 2\n"
    "v_accvgpr_read_b32 v11, a251\n"
    "ds_read_b128 v[80:83], v1 offset:0\n"
    "ds_read_b128 v[84:87], v1 offset:1024\n"
    "ds_read_b128 v[88:91], v1 offset:2048\n"
    "ds_read_b128 v[92:95], v1 offset:3072\n"
    "ds_read_b128 v[96:99], v1 offset:4096\n"
    "ds_read_b128 v[64:67], v1 offset:5120\n"
    "ds_read_b128 v[68:71], v1 offset:6144\n"
    "ds_read_b128 v[72:75], v1 offset:7168\n"
    "s_add_u32 s68, s94, 0\n"
    "v_mov_b32 v8, 0\n"
    "buffer_atomic_add v8, v6, s[8:11], s68 offen sc0\n"
    "v_mfma_f32_16x16x16_bf16 v[48:51], a[184:185], v[124:125], 0\n"
    "s_add_u32 m0, s66, 98304\n"
    "s_add_u32 s69, s76, 0\n"
    "v_mfma_f32_16x16x16_bf16 v[52:55], a[184:185], v[126:127], 0\n"
    "buffer_load_dwordx4 v15, s[20:23], s69 offen offset:0 lds\n"
    "v_mfma_f32_16x16x16_bf16 v[56:59], a[186:187], v[124:125], 0\n"
    "s_add_u32 s69, s76, 7168\n"
    "v_mfma_f32_16x16x16_bf16 v[60:63], a[186:187], v[126:127], 0\n"
    "s_waitcnt lgkmcnt(4)\n"
    "v_mfma_f32_16x16x32_bf16 v[48:51], v[80:83], v[128:131], v[48:51]\n"
    "ds_read_b128 v[76:79], v1 offset:8192\n"
    "v_mfma_f32_16x16x32_bf16 v[52:55], v[80:83], v[132:135], v[52:55]\n"
    "buffer_load_dwordx4 v15, s[20:23], s69 offen offset:1024 lds\n"
    "v_mfma_f32_16x16x32_bf16 v[60:63], v[84:87], v[132:135], v[60:63]\n"
    "ds_read_b128 v[80:83], v1 offset:9216\n"
    "v_mfma_f32_16x16x32_bf16 v[56:59], v[84:87], v[128:131], v[56:59]\n"
    "s_add_u32 s69, s76, 14336\n"
    "v_mfma_f32_16x16x32_bf16 v[48:51], v[88:91], v[136:139], v[48:51]\n"
    "ds_read_b128 v[84:87], v1 offset:10240\n"
    "v_mfma_f32_16x16x32_bf16 v[52:55], v[88:91], v[140:143], v[52:55]\n"
    "v_mfma_f32_16x16x32_bf16 v[60:63], v[92:95], v[140:143], v[60:63]\n"
    "ds_read_b128 v[88:91], v1 offset:11264\n"
    "v_mfma_f32_16x16x32_bf16 v[56:59], v[92:95], v[136:139], v[56:59]\n"
    "s_waitcnt lgkmcnt(4)\n"
    "v_mfma_f32_16x16x32_bf16 v[48:51], v[96:99], v[144:147], v[48:51]\n"
    "ds_read_b128 v[92:95], v1 offset:12288\n"
    "buffer_load_dwordx4 v15, s[20:23], s69 offen offset:2048 lds\n"
    "v_mfma_f32_16x16x32_bf16 v[52:55], v[96:99], v[148:151], v[52:55]\n"
    "s_add_u32 s69, s76, 21504\n"
    "v_mfma_f32_16x16x32_bf16 v[60:63], v[64:67], v[148:151], v[60:63]\n"
    "ds_read_b128 v[96:99], v1 offset:13312\n"
    "v_mfma_f32_16x16x32_bf16 v[56:59], v[64:67], v[144:147], v[56:59]\n"
    "buffer_load_dwordx4 v15, s[20:23], s69 offen offset:3072 lds\n"
    "v_mfma_f32_16x16x32_bf16 v[48:51], v[68:71], v[152:155], v[48:51]\n"
    "ds_read_b128 v[64:67], v1 offset:14336\n"
    "s_add_u32 s74, s81, 12193792\n"
    "v_mfma_f32_16x16x32_bf16 v[52:55], v[68:71], v[156:159], v[52:55]\n"
    "v_mfma_f32_16x16x32_bf16 v[60:63], v[72:75], v[156:159], v[60:63]\n"
    "ds_read_b128 v[68:71], v1 offset:15360\n"
    "v_mfma_f32_16x16x32_bf16 v[56:59], v[72:75], v[152:155], v[56:59]\n"
    "s_waitcnt lgkmcnt(4)\n"
    "v_mfma_f32_16x16x32_bf16 v[48:51], v[76:79], v[160:163], v[48:51]\n"
    "ds_read_b128 v[72:75], v1 offset:16384\n"
    "v_mfma_f32_16x16x32_bf16 v[52:55], v[76:79], v[164:167], v[52:55]\n"
    "buffer_store_dwordx4 a[236:239], v0, s[8:11], s74 offen sc1\n"
    "v_mfma_f32_16x16x32_bf16 v[60:63], v[80:83], v[164:167], v[60:63]\n"
    "ds_read_b128 v[76:79], v1 offset:17408\n"
    "s_add_u32 s74, s81, 12195840\n"
    "v_mfma_f32_16x16x32_bf16 v[56:59], v[80:83], v[160:163], v[56:59]\n"
    "v_mfma_f32_16x16x32_bf16 v[48:51], v[84:87], v[168:171], v[48:51]\n"
    "ds_read_b128 v[80:83], v1 offset:18432\n"
    "v_mfma_f32_16x16x32_bf16 v[52:55], v[84:87], v[172:175], v[52:55]\n"
    "buffer_store_dwordx4 a[236:239], v0, s[8:11], s74 offen sc1\n"
    "v_mfma_f32_16x16x32_bf16 v[60:63], v[88:91], v[172:175], v[60:63]\n"
    "ds_read_b128 v[84:87], v1 offset:19456\n"
    "v_mul_u32_u24 v7, 17, v0\n"
    "v_mfma_f32_16x16x32_bf16 v[56:59], v[88:91], v[168:171], v[56:59]\n"
    "v_mov_b32 v9, 0\n"
    "buffer_atomic_add v9, v6, s[8:11], s68 offen sc0\n"
    "s_waitcnt lgkmcnt(4)\n"
    "v_mfma_f32_16x16x32_bf16 v[48:51], v[92:95], v[176:179], v[48:51]\n"
    "ds_read_b128 v[88:91], v1 offset:20480\n"
    "v_cmp_gt_u32 vcc, 0x100, v0\n"
    "v_bfe_u32 v7, v7, 7, 4\n"
    "v_lshlrev_b32 v7, 3, v7\n"
    "v_cndmask_b32 v7, 2.0, v7, vcc\n"
    "v_mfma_f32_16x16x32_bf16 v[52:55], v[92:95], v[180:183], v[52:55]\n"
    "s_add_u32 s74, s82, 12193792\n"
    "v_mfma_f32_16x16x32_bf16 v[60:63], v[96:99], v[180:183], v[60:63]\n"
    "ds_read_b128 v[92:95], v1 offset:21504\n"
    "buffer_store_dwordx2 a[236:237], v7, s[8:11], s74 offen sc1\n"
    "v_mfma_f32_16x16x32_bf16 v[56:59], v[96:99], v[176:179], v[56:59]\n"
    "s_add_u32 m0, s66, 102400\n"
    "s_add_u32 s69, s76, 32768\n"
    "v_mfma_f32_16x16x32_bf16 v[48:51], v[64:67], v[184:187], v[48:51]\n"
    "ds_read_b128 v[96:99], v1 offset:22528\n"
    "v_mfma_f32_16x16x32_bf16 v[52:55], v[64:67], v[188:191], v[52:55]\n"
    "buffer_load_dwordx4 v15, s[20:23], s69 offen offset:0 lds\n"
    "v_mfma_f32_16x16x32_bf16 v[60:63], v[68:71], v[188:191], v[60:63]\n"
    "ds_read_b128 v[64:67], v1 offset:23552\n"
    "v_mfma_f32_16x16x32_bf16 v[56:59], v[68:71], v[184:187], v[56:59]\n"
    "s_add_u32 s69, s76, 39936\n"
    "s_waitcnt lgkmcnt(4)\n"
    "v_mfma_f32_16x16x32_bf16 v[48:51], v[72:75], v[192:195], v[48:51]\n"
    "ds_read_b128 v[68:71], v1 offset:24576\n"
    "v_mfma_f32_16x16x32_bf16 v[52:55], v[72:75], v[196:199], v[52:55]\n"
    "v_mfma_f32_16x16x32_bf16 v[60:63], v[76:79], v[196:199], v[60:63]\n"
    "ds_read_b128 v[72:75], v1 offset:25600\n"
    "v_mfma_f32_16x16x32_bf16 v[56:59], v[76:79], v[192:195], v[56:59]\n"
    "buffer_load_dwordx4 v15, s[20:23], s69 offen offset:1024 lds\n"
    "v_mfma_f32_16x16x32_bf16 v[48:51], v[80:83], v[200:203], v[48:51]\n"
    "ds_read_b128 v[76:79], v1 offset:26624\n"
    "s_add_u32 s69, s76, 47104\n"
    "v_mfma_f32_16x16x32_bf16 v[52:55], v[80:83], v[204:207], v[52:55]\n"
    "v_mfma_f32_16x16x32_bf16 v[60:63], v[84:87], v[204:207], v[60:63]\n"
    "ds_read_b128 v[80:83], v1 offset:27648\n"
    "v_mfma_f32_16x16x32_bf16 v[56:59], v[84:87], v[200:203], v[56:59]\n"
    "s_waitcnt lgkmcnt(4)\n"
    "v_mfma_f32_16x16x32_bf16 v[48:51], v[88:91], v[208:211], v[48:51]\n"
    "ds_read_b128 v[84:87], v1 offset:28672\n"
    "buffer_load_dwordx4 v15, s[20:23], s69 offen offset:2048 lds\n"
    "v_mfma_f32_16x16x32_bf16 v[52:55], v[88:91], v[212:215], v[52:55]\n"
    "s_add_u32 s69, s76, 54272\n"
    "v_mfma_f32_16x16x32_bf16 v[60:63], v[92:95], v[212:215], v[60:63]\n"
    "ds_read_b128 v[88:91], v1 offset:29696\n"
    "v_mfma_f32_16x16x32_bf16 v[56:59], v[92:95], v[208:211], v[56:59]\n"
    "buffer_load_dwordx4 v15, s[20:23], s69 offen offset:3072 lds\n"
    "v_mfma_f32_16x16x32_bf16 v[48:51], v[96:99], v[216:219], v[48:51]\n"
    "ds_read_b128 v[92:95], v1 offset:30720\n"
    "v_mfma_f32_16x16x32_bf16 v[52:55], v[96:99], v[220:223], v[52:55]\n"
    "v_mfma_f32_16x16x32_bf16 v[60:63], v[64:67], v[220:223], v[60:63]\n"
    "ds_read_b128 v[96:99], v1 offset:31744\n"
    "v_mfma_f32_16x16x32_bf16 v[56:59], v[64:67], v[216:219], v[56:59]\n"
    "s_waitcnt lgkmcnt(4)\n"
    "v_mfma_f32_16x16x32_bf16 v[48:51], v[68:71], v[224:227], v[48:51]\n"
    "v_mfma_f32_16x16x32_bf16 v[52:55], v[68:71], v[228:231], v[52:55]\n"
    "s_add_u32 s72, s64, 1708032\n"
    "v_mfma_f32_16x16x32_bf16 v[60:63], v[72:75], v[228:231], v[60:63]\n"
    "v_mfma_f32_16x16x32_bf16 v[56:59], v[72:75], v[224:227], v[56:59]\n"
    "s_waitcnt vmcnt(12)\n"
    "v_readfirstlane_b32 s29, v8\n"
    "s_nop 0\n"
    "s_cmp_lt_u32 s29, s95\n"
    "s_cbranch_scc1 Lslow5\n"
    "Lback5:\n"
    "s_barrier\n"
    "v_mfma_f32_16x16x32_bf16 v[48:51], v[76:79], v[232:235], v[48:51]\n"
    "buffer_load_dwordx4 v[160:163], v14, s[8:11], s72 offen sc1\n"
    "v_mfma_f32_16x16x32_bf16 v[52:55], v[76:79], v[236:239], v[52:55]\n"
    "s_add_u32 s71, s90, 659456\n"
    "v_mfma_f32_16x16x32_bf16 v[60:63], v[80:83], v[236:239], v[60:63]\n"
    "v_mfma_f32_16x16x32_bf16 v[56:59], v[80:83], v[232:235], v[56:59]\n"
    "s_waitcnt lgkmcnt(0)\n"
    "v_mfma_f32_16x16x32_bf16 v[48:51], v[84:87], v[240:243], v[48:51]\n"
    "buffer_load_dwordx4 v[128:131], v0, s[8:11], s71 offen offset:0 sc1\n"
    "v_mfma_f32_16x16x32_bf16 v[52:55], v[84:87], v[244:247], v[52:55]\n"
    "v_mfma_f32_16x16x32_bf16 v[60:63], v[88:91], v[244:247], v[60:63]\n"
    "v_mfma_f32_16x16x32_bf16 v[56:59], v[88:91], v[240:243], v[56:59]\n"
    "v_mfma_f32_16x16x32_bf16 v[48:51], v[92:95], v[248:251], v[48:51]\n"
    "buffer_load_dwordx4 v[132:135], v0, s[8:11], s71 offen offset:1024 sc1\n"
    "v_mfma_f32_16x16x32_bf16 v[52:55], v[92:95], v[252:255], v[52:55]\n"
    "v_mfma_f32_16x16x32_bf16 v[60:63], v[96:99], v[252:255], v[60:63]\n"
    "v_mfma_f32_16x16x32_bf16 v[56:59], v[96:99], v[248:251], v[56:59]\n"
    "buffer_load_dwordx4 v[136:139], v0, s[8:11], s71 offen offset:2048 sc1\n"
    "s_nop 4\n"
    "v_permlane16_swap_b32 v48, v52\n"
    "v_permlane16_swap_b32 v49, v53\n"
    "v_permlane16_swap_b32 v50, v54\n"
    "v_permlane16_swap_b32 v51, v55\n"
    "v_permlane16_swap_b32 v56, v60\n"
    "v_permlane16_swap_b32 v57, v61\n"
    "buffer_load_dwordx4 v[140:143], v0, s[8:11], s71 offen offset:3072 sc1\n"
    "v_permlane16_swap_b32 v58, v62\n"
    "v_permlane16_swap_b32 v59, v63\n"
    "v_max3_f32 v80, v48, v49, v50\n"
    "v_max3_f32 v81, v56, v57, v58\n"
    "v_min3_f32 v82, v48, v49, v50\n"
    "v_min3_f32 v83, v56, v57, v58\n"
    "s_add_u32 s71, s90, 663552\n"
    "buffer_load_dwordx4 v[144:147], v0, s[8:11], s71 offen offset:0 sc1\n"
    "v_max3_f32 v80, v80, v51, v52\n"
    "v_max3_f32 v81, v81, v59, v60\n"
    "v_min3_f32 v82, v82, v51, v52\n"
    "v_min3_f32 v83, v83, v59, v60\n"
    "v_max3_f32 v80, v80, v53, v54\n"
    "v_max3_f32 v81, v81, v61, v62\n"
    "buffer_load_dwordx4 v[148:151], v0, s[8:11], s71 offen offset:1024 sc1\n"
    "v_min3_f32 v82, v82, v53, v54\n"
    "v_min3_f32 v83, v83, v61, v62\n"
    "v_max_f32 v80, v80, v55\n"
    "v_max_f32 v81, v81, v63\n"
    "v_min_f32 v82, v82, v55\n"
    "v_min_f32 v83, v83, v63\n"
    "buffer_load_dwordx4 v[152:155], v0, s[8:11], s71 offen offset:2048 sc1\n"
    "v_permlane32_swap_b32 v80, v81\n"
    "v_permlane32_swap_b32 v82, v83\n"
    "v_max_f32 v80, v80, v81\n"
    "v_min_f32 v82, v82, v83\n"
    "v_mul_f32 v80, v80, v120\n"
    "v_mul_f32 v82, v82, v120\n"
    "buffer_load_dwordx4 v[156:159], v0, s[8:11], s71 offen offset:3072 sc1\n"
    "v_mov_b32 v81, v80\n"
    "v_mov_b32 v83, v82\n"
    "s_nop 0\n"
    "v_permlane32_swap_b32 v80, v81\n"
    "v_permlane32_swap_b32 v82, v83\n"
    "v_pk_fma_f32 v[64:65], v[48:49], v[120:121], v[82:83] op_sel:[0,0,0] op_sel_hi:[1,0,0] neg_lo:[1,0,0] neg_hi:[1,0,0]\n"
    "v_pk_fma_f32 v[66:67], v[50:51], v[120:121], v[82:83] op_sel:[0,0,0] op_sel_hi:[1,0,0] neg_lo:[1,0,0] neg_hi:[1,0,0]\n"
    "s_add_u32 s36, s79, s89\n"
    "s_add_u32 s70, s36, 40960\n"
    "buffer_load_dwordx4 a[160:163], v0, s[8:11], s70 offen offset:0 sc1\n"
    "v_pk_fma_f32 v[68:69], v[52:53], v[120:121], v[82:83] op_sel:[0,0,0] op_sel_hi:[1,0,0] neg_lo:[1,0,0] neg_hi:[1,0,0]\n"
    "v_pk_fma_f32 v[70:71], v[54:55], v[120:121], v[82:83] op_sel:[0,0,0] op_sel_hi:[1,0,0] neg_lo:[1,0,0] neg_hi:[1,0,0]\n"
    "v_exp_f32 v64, v64\n"
    "v_pk_fma_f32 v[48:49], v[48:49], v[120:121], v[80:81] op_sel:[0,0,0] op_sel_hi:[1,0,0] neg_lo:[0,0,1] neg_hi:[0,0,1]\n"
    "v_exp_f32 v65, v65\n"
    "v_pk_fma_f32 v[50:51], v[50:51], v[120:121], v[80:81] op_sel:[0,0,0] op_sel_hi:[1,0,0] neg_lo:[0,0,1] neg_hi:[0,0,1]\n"
    "v_exp_f32 v66, v66\n"
    "v_pk_fma_f32 v[52:53], v[52:53], v[120:121], v[80:81] op_sel:[0,0,0] op_sel_hi:[1,0,0] neg_lo:[0,0,1] neg_hi:[0,0,1]\n"
    "v_exp_f32 v67, v67\n"
    "v_pk_fma_f32 v[54:55], v[54:55], v[120:121], v[80:81] op_sel:[0,0,0] op_sel_hi:[1,0,0] neg_lo:[0,0,1] neg_hi:[0,0,1]\n"
    "v_exp_f32 v68, v68\n"
    "v_exp_f32 v69, v69\n"
    "v_exp_f32 v70, v70\n"
    "v_exp_f32 v71, v71\n"
    "v_exp_f32 v48, v48\n"
    "v_pk_fma_f32 v[72:73], v[56:57], v[120:121], v[82:83] op_sel:[0,0,1] op_sel_hi:[1,0,1] neg_lo:[1,0,0] neg_hi:[1,0,0]\n"
    "v_exp_f32 v49, v49\n"
    "v_pk_fma_f32 v[74:75], v[58:59], v[120:121], v[82:83] op_sel:[0,0,1] op_sel_hi:[1,0,1] neg_lo:[1,0,0] neg_hi:[1,0,0]\n"
    "v_exp_f32 v50, v50\n"
    "v_pk_fma_f32 v[76:77], v[60:61], v[120:121], v[82:83] op_sel:[0,0,1] op_sel_hi:[1,0,1] neg_lo:[1,0,0] neg_hi:[1,0,0]\n"
    "v_exp_f32 v51, v51\n"
    "v_pk_fma_f32 v[78:79], v[62:63], v[120:121], v[82:83] op_sel:[0,0,1] op_sel_hi:[1,0,1] neg_lo:[1,0,0] neg_hi:[1,0,0]\n"
    "v_exp_f32 v52, v52\n"
    "v_exp_f32 v53, v53\n"
    "v_exp_f32 v54, v54\n"
    "v_exp_f32 v55, v55\n"
    "buffer_load_dwordx4 a[164:167], v0, s[8:11], s70 offen offset:1024 sc1\n"
    "v_exp_f32 v72, v72\n"
    "v_pk_fma_f32 v[56:57], v[56:57], v[120:121], v[80:81] op_sel:[0,0,1] op_sel_hi:[1,0,1] neg_lo:[0,0,1] neg_hi:[0,0,1]\n"
    "v_exp_f32 v73, v73\n"
    "v_pk_fma_f32 v[58:59], v[58:59], v[120:121], v[80:81] op_sel:[0,0,1] op_sel_hi:[1,0,1] neg_lo:[0,0,1] neg_hi:[0,0,1]\n"
    "v_exp_f32 v74, v74\n"
    "v_pk_fma_f32 v[60:61], v[60:61], v[120:121], v[80:81] op_sel:[0,0,1] op_sel_hi:[1,0,1] neg_lo:[0,0,1] neg_hi:[0,0,1]\n"
    "v_exp_f32 v75, v75\n"
    "v_pk_fma_f32 v[62:63], v[62:63], v[120:121], v[80:81] op_sel:[0,0,1] op_sel_hi:[1,0,1] neg_lo:[0,0,1] neg_hi:[0,0,1]\n"
    "v_exp_f32 v76, v76\n"
    "v_exp_f32 v77, v77\n"
    "v_exp_f32 v78, v78\n"
    "v_exp_f32 v79, v79\n"
    "v_exp_f32 v56, v56\n"
    "v_pk_add_f32 v[92:93], v[64:65], v[66:67] op_sel:[0,0] op_sel_hi:[1,1]\n"
    "v_exp_f32 v57, v57\n"
    "v_pk_add_f32 v[94:95], v[68:69], v[70:71] op_sel:[0,0] op_sel_hi:[1,1]\n"
    "v_exp_f32 v58, v58\n"
    "v_pk_add_f32 v[84:85], v[48:49], v[50:51] op_sel:[0,0] op_sel_hi:[1,1]\n"
    "v_exp_f32 v59, v59\n"
    "v_pk_add_f32 v[86:87], v[52:53], v[54:55] op_sel:[0,0] op_sel_hi:[1,1]\n"
    "v_exp_f32 v60, v60\n"
    "v_pk_add_f32 v[96:97], v[72:73], v[74:75] op_sel:[0,0] op_sel_hi:[1,1]\n"
    "v_exp_f32 v61, v61\n"
    "v_pk_add_f32 v[98:99], v[76:77], v[78:79] op_sel:[0,0] op_sel_hi:[1,1]\n"
    "v_exp_f32 v62, v62\n"
    "v_exp_f32 v63, v63\n"
    "buffer_load_dwordx4 a[168:171], v0, s[8:11], s70 offen offset:2048 sc1\n"
    "v_pk_add_f32 v[88:89], v[56:57], v[58:59] op_sel:[0,0] op_sel_hi:[1,1]\n"
    "v_pk_add_f32 v[90:91], v[60:61], v[62:63] op_sel:[0,0] op_sel_hi:[1,1]\n"
    "v_pk_add_f32 v[84:85], v[84:85], v[86:87] op_sel:[0,0] op_sel_hi:[1,1]\n"
    "v_pk_add_f32 v[88:89], v[88:89], v[90:91] op_sel:[0,0] op_sel_hi:[1,1]\n"
    "v_pk_add_f32 v[92:93], v[92:93], v[94:95] op_sel:[0,0] op_sel_hi:[1,1]\n"
    "v_pk_add_f32 v[96:97], v[96:97], v[98:99] op_sel:[0,0] op_sel_hi:[1,1]\n"
    "v_add_f32 v80, v84, v85\n"
    "v_add_f32 v81, v88, v89\n"
    "v_add_f32 v82, v92, v93\n"
    "v_add_f32 v83, v96, v97\n"
    "v_permlane32_swap_b32 v80, v81\n"
    "s_nop 0\n"
    "v_permlane32_swap_b32 v82, v83\n"
    "v_add_f32 v80, v80, v81\n"
    "v_add_f32 v82, v82, v83\n"
    "v_rcp_f32 v80, v80\n"
    "v_rcp_f32 v82, v82\n"
    "v_mov_b32 v81, v80\n"
    "v_mov_b32 v83, v82\n"
    "s_nop 0\n"
    "v_permlane32_swap_b32 v80, v81\n"
    "v_permlane32_swap_b32 v82, v83\n"
    "v_pk_mul_f32 v[48:49], v[48:49], v[80:81] op_sel:[0,0] op_sel_hi:[1,0]\n"
    "v_pk_mul_f32 v[50:51], v[50:51], v[80:81] op_sel:[0,0] op_sel_hi:[1,0]\n"
    "v_pk_mul_f32 v[52:53], v[52:53], v[80:81] op_sel:[0,0] op_sel_hi:[1,0]\n"
    "v_pk_mul_f32 v[54:55], v[54:55], v[80:81] op_sel:[0,0] op_sel_hi:[1,0]\n"
    "v_cvt_pk_bf16_f32 v100, v48, v49\n"
    "v_cvt_pk_bf16_f32 v101, v50, v51\n"
    "buffer_load_dwordx4 a[172:175], v0, s[8:11], s70 offen offset:3072 sc1\n"
    "v_cvt_pk_bf16_f32 v102, v52, v53\n"
    "v_cvt_pk_bf16_f32 v103, v54, v55\n"
    "v_pk_mul_f32 v[64:65], v[64:65], v[82:83] op_sel:[0,0] op_sel_hi:[1,0]\n"
    "v_pk_mul_f32 v[66:67], v[66:67], v[82:83] op_sel:[0,0] op_sel_hi:[1,0]\n"
    "v_pk_mul_f32 v[68:69], v[68:69], v[82:83] op_sel:[0,0] op_sel_hi:[1,0]\n"
    "v_pk_mul_f32 v[70:71], v[70:71], v[82:83] op_sel:[0,0] op_sel_hi:[1,0]\n"
    "v_cvt_pk_bf16_f32 v104, v64, v65\n"
    "v_cvt_pk_bf16_f32 v105, v66, v67\n"
    "v_cvt_pk_bf16_f32 v106, v68, v69\n"
    "v_cvt_pk_bf16_f32 v107, v70, v71\n"
    "v_pk_mul_f32 v[56:57], v[56:57], v[80:81] op_sel:[0,1] op_sel_hi:[1,1]\n"
    "v_pk_mul_f32 v[58:59], v[58:59], v[80:81] op_sel:[0,1] op_sel_hi:[1,1]\n"
    "v_pk_mul_f32 v[60:61], v[60:61], v[80:81] op_sel:[0,1] op_sel_hi:[1,1]\n"
    "v_pk_mul_f32 v[62:63], v[62:63], v[80:81] op_sel:[0,1] op_sel_hi:[1,1]\n"
    "v_cvt_pk_bf16_f32 v108, v56, v57\n"
    "v_cvt_pk_bf16_f32 v109, v58, v59\n"
    "v_cvt_pk_bf16_f32 v110, v60, v61\n"
    "v_cvt_pk_bf16_f32 v111, v62, v63\n"
    "v_pk_mul_f32 v[72:73], v[72:73], v[82:83] op_sel:[0,1] op_sel_hi:[1,1]\n"
    "v_pk_mul_f32 v[74:75], v[74:75], v[82:83] op_sel:[0,1] op_sel_hi:[1,1]\n"
    "v_pk_mul_f32 v[76:77], v[76:77], v[82:83] op_sel:[0,1] op_sel_hi:[1,1]\n"
    "v_pk_mul_f32 v[78:79], v[78:79], v[82:83] op_sel:[0,1] op_sel_hi:[1,1]\n"
    "v_cvt_pk_bf16_f32 v112, v72, v73\n"
    "v_cvt_pk_bf16_f32 v113, v74, v75\n"
    "v_cvt_pk_bf16_f32 v114, v76, v77\n"
    "v_cvt_pk_bf16_f32 v115, v78, v79\n"
    "v_accvgpr_read_b32 v80, a249\n"
    "v_accvgpr_read_b32 v81, a248\n"
    "v_mov_b32 v82, 0x3f80\n"
    "v_mov_b32 v83, 1.0\n"
    "Lredo7:\n"
    "s_waitcnt vmcnt(12)\n"
    "v_permlane32_swap_b32 v160, v161\n"
    "v_permlane32_swap_b32 v162, v163\n"
    "v_add_f32 v160, v160, v161\n"
    "v_add_f32 v162, v162, v163\n"
    "s_add_u32 s70, s79, 0\n"
    "buffer_load_dwordx4 a[0:3], v0, s[8:11], s70 offen offset:0 sc1\n"
    "s_waitcnt vmcnt(12)\n"
    "v_lshlrev_b32 v16, 16, v128\n"
    "v_and_b32 v17, 0xffff0000, v128\n"
    "v_permlane16_swap_b32 v160, v162\n"
    "v_lshlrev_b32 v18, 16, v129\n"
    "v_and_b32 v19, 0xffff0000, v129\n"
    "v_add_f32 v160, v160, v162\n"
    "v_lshlrev_b32 v20, 16, v130\n"
    "v_and_b32 v21, 0xffff0000, v130\n"
    "buffer_load_dwordx4 a[4:7], v0, s[8:11], s70 offen offset:1024 sc1\n"
    "v_add_f32_dpp v160, v160, v160 row_ror:4 row_mask:0xf bank_mask:0xf\n"
    "v_lshlrev_b32 v22, 16, v131\n"
    "v_and_b32 v23, 0xffff0000, v131\n"
    "v_add_f32_dpp v160, v160, v160 row_ror:8 row_mask:0xf bank_mask:0xf\n"
    "s_waitcnt vmcnt(12)\n"
    "v_dot2c_f32_bf16 v16, v82, v132\n"
    "v_dot2c_f32_bf16 v17, v83, v132\n"
    "v_cvt_pk_bf16_f32 v161, v160, v160\n"
    "v_dot2c_f32_bf16 v18, v82, v133\n"
    "buffer_load_dwordx4 a[8:11], v0, s[8:11], s70 offen offset:2048 sc1\n"
    "v_dot2c_f32_bf16 v19, v83, v133\n"
    "ds_write_b16 v80, v161\n"
    "v_dot2c_f32_bf16 v20, v82, v134\n"
    "v_dot2c_f32_bf16 v21, v83, v134\n"
    "v_dot2c_f32_bf16 v22, v82, v135\n"
    "v_dot2c_f32_bf16 v23, v83, v135\n"
    "s_waitcnt vmcnt(12)\n"
    "v_dot2c_f32_bf16 v16, v82, v136\n"
    "v_dot2c_f32_bf16 v17, v83, v136\n"
    "v_dot2c_f32_bf16 v18, v82, v137\n"
    "buffer_load_dwordx4 a[12:15], v0, s[8:11], s70 offen offset:3072 sc1\n"
    "v_dot2c_f32_bf16 v19, v83, v137\n"
    "v_dot2c_f32_bf16 v20, v82, v138\n"
    "v_dot2c_f32_bf16 v21, v83, v138\n"
    "v_dot2c_f32_bf16 v22, v82, v139\n"
    "v_dot2c_f32_bf16 v23, v83, v139\n"
    "s_waitcnt vmcnt(12)\n"
    "v_dot2c_f32_bf16 v16, v82, v140\n"
    "v_dot2c_f32_bf16 v17, v83, v140\n"
    "v_dot2c_f32_bf16 v18, v82, v141\n"
    "s_add_u32 s70, s79, 4096\n"
    "buffer_load_dwordx4 a[16:19], v0, s[8:11], s70 offen offset:0 sc1\n"
    "v_dot2c_f32_bf16 v19, v83, v141\n"
    "v_dot2c_f32_bf16 v20, v82, v142\n"
    "v_dot2c_f32_bf16 v21, v83, v142\n"
    "v_dot2c_f32_bf16 v22, v82, v143\n"
    "v_dot2c_f32_bf16 v23, v83, v143\n"
    "s_waitcnt vmcnt(12)\n"
    "v_dot2c_f32_bf16 v16, v82, v144\n"
    "v_dot2c_f32_bf16 v17, v83, v144\n"
    "v_dot2c_f32_bf16 v18, v82, v145\n"
    "buffer_load_dwordx4 a[20:23], v0, s[8:11], s70 offen offset:1024 sc1\n"
    "v_dot2c_f32_bf16 v19, v83, v145\n"
    "v_dot2c_f32_bf16 v20, v82, v146\n"
    "v_dot2c_f32_bf16 v21, v83, v146\n"
    "v_dot2c_f32_bf16 v22, v82, v147\n"
    "v_dot2c_f32_bf16 v23, v83, v147\n"
    "s_waitcnt vmcnt(12)\n"
    "v_dot2c_f32_bf16 v16, v82, v148\n"
    "v_dot2c_f32_bf16 v17, v83, v148\n"
    "v_dot2c_f32_bf16 v18, v82, v149\n"
    "buffer_load_dwordx4 a[24:27], v0, s[8:11], s70 offen offset:2048 sc1\n"
    "v_dot2c_f32_bf16 v19, v83, v149\n"
    "v_dot2c_f32_bf16 v20, v82, v150\n"
    "v_dot2c_f32_bf16 v21, v83, v150\n"
    "v_dot2c_f32_bf16 v22, v82, v151\n"
    "v_dot2c_f32_bf16 v23, v83, v151\n"
    "s_waitcnt vmcnt(12)\n"
    "v_dot2c_f32_bf16 v16, v82, v152\n"
    "v_dot2c_f32_bf16 v17, v83, v152\n"
    "v_dot2c_f32_bf16 v18, v82, v153\n"
    "buffer_load_dwordx4 a[28:31], v0, s[8:11], s70 offen offset:3072 sc1\n"
    "v_dot2c_f32_bf16 v19, v83, v153\n"
    "v_dot2c_f32_bf16 v20, v82, v154\n"
    "v_dot2c_f32_bf16 v21, v83, v154\n"
    "v_dot2c_f32_bf16 v22, v82, v155\n"
    "v_dot2c_f32_bf16 v23, v83, v155\n"
    "s_waitcnt vmcnt(12)\n"
    "v_dot2c_f32_bf16 v16, v82, v156\n"
    "v_dot2c_f32_bf16 v17, v83, v156\n"
    "v_dot2c_f32_bf16 v18, v82, v157\n"
    "v_dot2c_f32_bf16 v19, v83, v157\n"
    "v_dot2c_f32_bf16 v20, v82, v158\n"
    "v_dot2c_f32_bf16 v21, v83, v158\n"
    "v_dot2c_f32_bf16 v22, v82, v159\n"
    "v_dot2c_f32_bf16 v23, v83, v159\n"
    "s_nop 2\n"
    "v_permlane32_swap_b32 v16, v20\n"
    "v_permlane32_swap_b32 v17, v21\n"
    "v_permlane32_swap_b32 v18, v22\n"
    "v_permlane32_swap_b32 v19, v23\n"
    "v_pk_add_f32 v[16:17], v[16:17], v[20:21] op_sel:[0,0] op_sel_hi:[1,1]\n"
    "v_pk_add_f32 v[18:19], v[18:19], v[22:23] op_sel:[0,0] op_sel_hi:[1,1]\n"
    "v_cmp_u_f32 s[30:31], v16, v18\n"
    "v_cmp_u_f32 s[24:25], v160, v160\n"
    "v_cvt_pk_bf16_f32 v20, v16, v17\n"
    "v_cvt_pk_bf16_f32 v21, v18, v19\n"
    "ds_write_b64 v81, v[20:21]\n"
    "s_or_b64 s[30:31], s[30:31], s[24:25]\n"
    "s_cbranch_scc1 Lbad8\n"
    "Lok8:\n"
    "s_waitcnt lgkmcnt(0)\n"
    "s_barrier\n"
    "ds_read_b128 v[80:83], v0 offset:24576\n"
    "ds_read_b128 v[84:87], v0 offset:25600\n"
    "ds_read_b128 v[88:91], v0 offset:26624\n"
    "ds_read_b128 v[92:95], v0 offset:27648\n"
    "s_mov_b32 s65, 36864\n"
    "buffer_atomic_add a255, v3, s[8:11], s65 offen\n"
    "s_waitcnt lgkmcnt(3)\n"
    "v_mfma_f32_32x32x16_bf16 v[16:31], v[80:83], v[100:103], 0\n"
    "s_lshr_b32 s39, s90, 1\n"
    "s_add_u32 s74, s39, 1970176\n"
    "buffer_load_dwordx4 v[192:195], v0, s[8:11], s74 offen offset:0 sc1\n"
    "s_waitcnt lgkmcnt(2)\n"
    "v_mfma_f32_32x32x16_bf16 v[16:31], v[84:87], v[104:107], v[16:31]\n"
    "buffer_load_dwordx4 v[196:199], v0, s[8:11], s74 offen offset:1024 sc1\n"
    "s_waitcnt lgkmcnt(1)\n"
    "v_mfma_f32_32x32x16_bf16 v[32:47], v[88:91], v[108:111], 0\n"
    "buffer_load_dwordx4 v[200:203], v0, s[8:11], s74 offen offset:2048 sc1\n"
    "s_waitcnt lgkmcnt(0)\n"
    "v_mfma_f32_32x32x16_bf16 v[32:47], v[92:95], v[112:115], v[32:47]\n"
    "buffer_load_dwordx4 v[204:207], v0, s[8:11], s74 offen offset:3072 sc1\n"
    "s_add_u32 s73, s83, 18878464\n"
    "s_nop 3\n"
    "v_add_f32 v24, 0x358637bd, v24\n"
    "v_rcp_f32 v24, v24\n"
    "s_nop 0\n"
    "v_pk_mul_f32 v[16:17], v[16:17], v[24:25] op_sel:[0,0] op_sel_hi:[1,0]\n"
    "v_pk_mul_f32 v[18:19], v[18:19], v[24:25] op_sel:[0,0] op_sel_hi:[1,0]\n"
    "v_pk_mul_f32 v[20:21], v[20:21], v[24:25] op_sel:[0,0] op_sel_hi:[1,0]\n"
    "v_pk_mul_f32 v[22:23], v[22:23], v[24:25] op_sel:[0,0] op_sel_hi:[1,0]\n"
    "v_cvt_pk_bf16_f32 v28, v16, v17\n"
    "v_cvt_pk_bf16_f32 v29, v18, v19\n"
    "v_cvt_pk_bf16_f32 v30, v20, v21\n"
    "v_cvt_pk_bf16_f32 v31, v22, v23\n"
    "buffer_store_dwordx4 v[28:31], v11, s[8:11], s73 offen offset:0 sc1\n"
    "v_add_f32 v40, 0x358637bd, v40\n"
    "v_rcp_f32 v40, v40\n"
    "s_nop 0\n"
    "v_pk_mul_f32 v[32:33], v[32:33], v[40:41] op_sel:[0,0] op_sel_hi:[1,0]\n"
    "v_pk_mul_f32 v[34:35], v[34:35], v[40:41] op_sel:[0,0] op_sel_hi:[1,0]\n"
    "v_pk_mul_f32 v[36:37], v[36:37], v[40:41] op_sel:[0,0] op_sel_hi:[1,0]\n"
    "v_pk_mul_f32 v[38:39], v[38:39], v[40:41] op_sel:[0,0] op_sel_hi:[1,0]\n"
    "v_cvt_pk_bf16_f32 v44, v32, v33\n"
    "v_cvt_pk_bf16_f32 v45, v34, v35\n"
    "v_cvt_pk_bf16_f32 v46, v36, v37\n"
    "v_cvt_pk_bf16_f32 v47, v38, v39\n"
    "buffer_store_dwordx4 v[44:47], v11, s[8:11], s73 offen offset:512 sc1\n"
    "s_add_u32 s68, s94, 32768\n"
    "v_mov_b32 v8, 0\n"
    "buffer_atomic_add v8, v6, s[8:11], s68 offen sc0\n"
    "v_accvgpr_read_b32 v12, a253\n"
    "v_xor_b32 v13, 32, v12\n"
    "v_xor_b32 v14, 64, v12\n"
    "v_xor_b32 v15, 0x60, v12\n"
    "s_cmp_eq_u32 s4, 0\n"
    "s_nop 0\n"
    "s_cselect_b32 s62, 0, 2.0\n"
    "s_add_u32 m0, s66, 65536\n"
    "s_add_u32 s69, s77, 32768\n"
    "buffer_load_dwordx4 v0, s[8:11], s69 offen offset:0 sc1 lds\n"
    "v_accvgpr_read_b32 v16, a220\n"
    "buffer_load_dwordx4 v0, s[8:11], s69 offen offset:1024 sc1 lds\n"
    "v_accvgpr_read_b32 v17, a221\n"
    "buffer_load_dwordx4 v0, s[8:11], s69 offen offset:2048 sc1 lds\n"
    "v_accvgpr_read_b32 v18, a222\n"
    "buffer_load_dwordx4 v0, s[8:11], s69 offen offset:3072 sc1 lds\n"
    "v_accvgpr_read_b32 v19, a223\n"
    "s_add_u32 m0, s66, 32768\n"
    "s_nop 0\n"
    "s_add_u32 s69, s77, 0\n"
    "buffer_load_dwordx4 v0, s[8:11], s69 offen offset:0 sc1 lds\n"
    "v_accvgpr_read_b32 v20, a224\n"
    "buffer_load_dwordx4 v0, s[8:11], s69 offen offset:1024 sc1 lds\n"
    "v_accvgpr_read_b32 v21, a225\n"
    "buffer_load_dwordx4 v0, s[8:11], s69 offen offset:2048 sc1 lds\n"
    "v_accvgpr_read_b32 v22, a226\n"
    "buffer_load_dwordx4 v0, s[8:11], s69 offen offset:3072 sc1 lds\n"
    "v_accvgpr_read_b32 v23, a227\n"
    "s_add_u32 m0, s66, 36864\n"
    "s_add_u32 s69, s77, 4096\n"
    "buffer_load_dwordx4 v0, s[8:11], s69 offen offset:0 sc1 lds\n"
    "v_accvgpr_read_b32 v24, a228\n"
    "buffer_load_dwordx4 v0, s[8:11], s69 offen offset:1024 sc1 lds\n"
    "v_accvgpr_read_b32 v25, a229\n"
    "buffer_load_dwordx4 v0, s[8:11], s69 offen offset:2048 sc1 lds\n"
    "v_accvgpr_read_b32 v26, a230\n"
    "v_mov_b32_e64 v9, 0\n"
    "buffer_atomic_add v9, v6, s[8:11], s68 offen sc0\n"
    "buffer_load_dwordx4 v0, s[8:11], s69 offen offset:3072 sc1 lds\n"
    "v_accvgpr_read_b32 v27, a231\n"
    "v_accvgpr_read_b32 v28, a232\n"
    "v_accvgpr_read_b32 v29, a233\n"
    "v_accvgpr_read_b32 v30, a234\n"
    "v_accvgpr_read_b32 v31, a235\n"
    "ds_read_b64_tr_b16 v[48:49], v12 offset:0\n"
    "ds_read_b64_tr_b16 v[50:51], v12 offset:1024\n"
    "ds_read_b64_tr_b16 v[52:53], v13 offset:0\n"
    "ds_read_b64_tr_b16 v[54:55], v13 offset:1024\n"
    "ds_read_b64_tr_b16 v[56:57], v14 offset:0\n"
    "ds_read_b64_tr_b16 v[58:59], v14 offset:1024\n"
    "ds_read_b64_tr_b16 v[60:61], v15 offset:0\n"
    "ds_read_b64_tr_b16 v[62:63], v15 offset:1024\n"
    "ds_read_b64_tr_b16 v[64:65], v12 offset:4096\n"
    "ds_read_b64_tr_b16 v[66:67], v12 offset:5120\n"
    "ds_read_b64_tr_b16 v[68:69], v13 offset:4096\n"
    "ds_read_b64_tr_b16 v[70:71], v13 offset:5120\n"
    "ds_read_b64_tr_b16 v[72:73], v14 offset:4096\n"
    "ds_read_b64_tr_b16 v[74:75], v14 offset:5120\n"
    "ds_read_b64_tr_b16 v[76:77], v15 offset:4096\n"
    "ds_read_b64_tr_b16 v[78:79], v15 offset:5120\n"
    "ds_read_b64_tr_b16 v[80:81], v12 offset:8192\n"
    "ds_read_b64_tr_b16 v[82:83], v12 offset:9216\n"
    "ds_read_b64_tr_b16 v[84:85], v13 offset:8192\n"
    "ds_read_b64_tr_b16 v[86:87], v13 offset:9216\n"
    "ds_read_b64_tr_b16 v[88:89], v14 offset:8192\n"
    "ds_read_b64_tr_b16 v[90:91], v14 offset:9216\n"
    "ds_read_b64_tr_b16 v[92:93], v15 offset:8192\n"
    "ds_read_b64_tr_b16 v[94:95], v15 offset:9216\n"
    "ds_read_b64_tr_b16 v[96:97], v12 offset:12288\n"
    "ds_read_b64_tr_b16 v[98:99], v12 offset:13312\n"
    "ds_read_b64_tr_b16 v[100:101], v13 offset:12288\n"
    "ds_read_b64_tr_b16 v[102:103], v13 offset:13312\n"
    "ds_read_b64_tr_b16 v[104:105], v14 offset:12288\n"
    "ds_read_b64_tr_b16 v[106:107], v14 offset:13312\n"
    "ds_read_b64_tr_b16 v[108:109], v15 offset:12288\n"
    "ds_read_b64_tr_b16 v[110:111], v15 offset:13312\n"
    "ds_read_b64_tr_b16 v[112:113], v12 offset:16384\n"
    "ds_read_b64_tr_b16 v[114:115], v12 offset:17408\n"
    "ds_read_b64_tr_b16 v[116:117], v13 offset:16384\n"
    "ds_read_b64_tr_b16 v[118:119], v13 offset:17408\n"
    "ds_read_b64_tr_b16 v[120:121], v14 offset:16384\n"
    "ds_read_b64_tr_b16 v[122:123], v14 offset:17408\n"
    "ds_read_b64_tr_b16 v[124:125], v15 offset:16384\n"
    "ds_read_b64_tr_b16 v[126:127], v15 offset:17408\n"
    "ds_read_b64_tr_b16 v[208:209], v12 offset:20480\n"
    "ds_read_b64_tr_b16 v[210:211], v12 offset:21504\n"
    "ds_read_b64_tr_b16 v[212:213], v13 offset:20480\n"
    "ds_read_b64_tr_b16 v[214:215], v13 offset:21504\n"
    "ds_read_b64_tr_b16 v[216:217], v14 offset:20480\n"
    "ds_read_b64_tr_b16 v[218:219], v14 offset:21504\n"
    "ds_read_b64_tr_b16 v[220:221], v15 offset:20480\n"
    "ds_read_b64_tr_b16 v[222:223], v15 offset:21504\n"
    "s_waitcnt vmcnt(13)\n"
    "v_readfirstlane_b32 s29, v8\n"
    "s_nop 0\n"
    "s_cmp_lt_u32 s29, s95\n"
    "s_cbranch_scc1 Lslow10\n"
    "Lback10:\n"
    "s_barrier\n"
    "s_add_u32 s71, s84, 18878464\n"
    "buffer_load_dwordx4 v[128:131], v0, s[8:11], s71 offen offset:0 sc1\n"
    "ds_read_b64_tr_b16 v[224:225], v12 offset:24576\n"
    "ds_read_b64_tr_b16 v[226:227], v12 offset:25600\n"
    "buffer_load_dwordx4 v[132:135], v0, s[8:11], s71 offen offset:1024 sc1\n"
    "ds_read_b64_tr_b16 v[228:229], v13 offset:24576\n"
    "ds_read_b64_tr_b16 v[230:231], v13 offset:25600\n"
    "buffer_load_dwordx4 v[136:139], v0, s[8:11], s71 offen offset:2048 sc1\n"
    "Lpad3:\n"
    "s_cbranch_execz Lpad7\n"
    "ds_read_b64_tr_b16 v[232:233], v14 offset:24576\n"
    "ds_read_b64_tr_b16 v[234:235], v14 offset:25600\n"
    "buffer_load_dwordx4 v[140:143], v0, s[8:11], s71 offen offset:3072 sc1\n"
    "ds_read_b64_tr_b16 v[236:237], v15 offset:24576\n"
    "ds_read_b64_tr_b16 v[238:239], v15 offset:25600\n"
    "s_add_u32 s71, s84, 18882560\n"
    "buffer_load_dwordx4 v[144:147], v0, s[8:11], s71 offen offset:0 sc1\n"
    "ds_read_b64_tr_b16 v[240:241], v12 offset:28672\n"
    "ds_read_b64_tr_b16 v[242:243], v12 offset:29696\n"
    "ds_read_b64_tr_b16 v[244:245], v13 offset:28672\n"
    "buffer_load_dwordx4 v[148:151], v0, s[8:11], s71 offen offset:1024 sc1\n"
    "ds_read_b64_tr_b16 v[246:247], v13 offset:29696\n"
    "ds_read_b64_tr_b16 v[248:249], v14 offset:28672\n"
    "buffer_load_dwordx4 v[152:155], v0, s[8:11], s71 offen offset:2048 sc1\n"
    "ds_read_b64_tr_b16 v[250:251], v14 offset:29696\n"
    "ds_read_b64_tr_b16 v[252:253], v15 offset:28672\n"
    "buffer_load_dwordx4 v[156:159], v0, s[8:11], s71 offen offset:3072 sc1\n"
    "ds_read_b64_tr_b16 v[254:255], v15 offset:29696\n"
    "ds_read_b64_tr_b16 v[32:33], v12 offset:32768\n"
    "s_add_u32 s71, s84, 18886656\n"
    "buffer_load_dwordx4 v[160:163], v0, s[8:11], s71 offen offset:0 sc1\n"
    "ds_read_b64_tr_b16 v[34:35], v12 offset:33792\n"
    "ds_read_b64_tr_b16 v[36:37], v13 offset:32768\n"
    "buffer_load_dwordx4 v[164:167], v0, s[8:11], s71 offen offset:1024 sc1\n"
    "ds_read_b64_tr_b16 v[38:39], v13 offset:33792\n"
    "ds_read_b64_tr_b16 v[40:41], v14 offset:32768\n"
    "ds_read_b64_tr_b16 v[42:43], v14 offset:33792\n"
    "s_mov_b32 s65, 69632\n"
    "Lredo11:\n"
    "s_waitcnt lgkmcnt(15) vmcnt(9)\n"
    "v_mfma_f32_16x16x32_bf16 v[16:19], v[48:51], v[128:131], v[16:19]\n"
    "ds_read_b64_tr_b16 v[44:45], v15 offset:32768\n"
    "ds_read_b64_tr_b16 v[46:47], v15 offset:33792\n"
    "v_mfma_f32_16x16x32_bf16 v[20:23], v[52:55], v[128:131], v[20:23]\n"
    "ds_read_b64_tr_b16 v[48:49], v12 offset:36864\n"
    "ds_read_b64_tr_b16 v[50:51], v12 offset:37888\n"
    "v_mfma_f32_16x16x32_bf16 v[24:27], v[56:59], v[128:131], v[24:27]\n"
    "ds_read_b64_tr_b16 v[52:53], v13 offset:36864\n"
    "ds_read_b64_tr_b16 v[54:55], v13 offset:37888\n"
    "buffer_load_dwordx4 v[168:171], v0, s[8:11], s71 offen offset:2048 sc1\n"
    "v_mfma_f32_16x16x32_bf16 v[28:31], v[60:63], v[128:131], v[28:31]\n"
    "ds_read_b64_tr_b16 v[56:57], v14 offset:36864\n"
    "ds_read_b64_tr_b16 v[58:59], v14 offset:37888\n"
    "s_waitcnt vmcnt(9)\n"
    "v_mfma_f32_16x16x32_bf16 v[16:19], v[64:67], v[132:135], v[16:19]\n"
    "ds_read_b64_tr_b16 v[60:61], v15 offset:36864\n"
    "ds_read_b64_tr_b16 v[62:63], v15 offset:37888\n"
    "v_mfma_f32_16x16x32_bf16 v[20:23], v[68:71], v[132:135], v[20:23]\n"
    "ds_read_b64_tr_b16 v[64:65], v12 offset:40960\n"
    "ds_read_b64_tr_b16 v[66:67], v12 offset:41984\n"
    "v_mfma_f32_16x16x32_bf16 v[24:27], v[72:75], v[132:135], v[24:27]\n"
    "ds_read_b64_tr_b16 v[68:69], v13 offset:40960\n"
    "ds_read_b64_tr_b16 v[70:71], v13 offset:41984\n"
    "buffer_load_dwordx4 v[172:175], v0, s[8:11], s71 offen offset:3072 sc1\n"
    "v_mfma_f32_16x16x32_bf16 v[28:31], v[76:79], v[132:135], v[28:31]\n"
    "ds_read_b64_tr_b16 v[72:73], v14 offset:40960\n"
    "ds_read_b64_tr_b16 v[74:75], v14 offset:41984\n"
    "s_waitcnt vmcnt(9)\n"
    "v_mfma_f32_16x16x32_bf16 v[16:19], v[80:83], v[136:139], v[16:19]\n"
    "ds_read_b64_tr_b16 v[76:77], v15 offset:40960\n"
    "ds_read_b64_tr_b16 v[78:79], v15 offset:41984\n"
    "v_mfma_f32_16x16x32_bf16 v[20:23], v[84:87], v[136:139], v[20:23]\n"
    "ds_read_b64_tr_b16 v[80:81], v12 offset:45056\n"
    "ds_read_b64_tr_b16 v[82:83], v12 offset:46080\n"
    "s_add_u32 s71, s84, 18890752\n"
    "v_mfma_f32_16x16x32_bf16 v[24:27], v[88:91], v[136:139], v[24:27]\n"
    "ds_read_b64_tr_b16 v[84:85], v13 offset:45056\n"
    "ds_read_b64_tr_b16 v[86:87], v13 offset:46080\n"
    "buffer_load_dwordx4 v[176:179], v0, s[8:11], s71 offen offset:0 sc1\n"
    "v_mfma_f32_16x16x32_bf16 v[28:31], v[92:95], v[136:139], v[28:31]\n"
    "ds_read_b64_tr_b16 v[88:89], v14 offset:45056\n"
    "ds_read_b64_tr_b16 v[90:91], v14 offset:46080\n"
    "s_waitcnt vmcnt(9)\n"
    "v_mfma_f32_16x16x32_bf16 v[16:19], v[96:99], v[140:143], v[16:19]\n"
    "ds_read_b64_tr_b16 v[92:93], v15 offset:45056\n"
    "ds_read_b64_tr_b16 v[94:95], v15 offset:46080\n"
    "v_mfma_f32_16x16x32_bf16 v[20:23], v[100:103], v[140:143], v[20:23]\n"
    "ds_read_b64_tr_b16 v[96:97], v12 offset:49152\n"
    "ds_read_b64_tr_b16 v[98:99], v12 offset:50176\n"
    "v_mfma_f32_16x16x32_bf16 v[24:27], v[104:107], v[140:143], v[24:27]\n"
    "ds_read_b64_tr_b16 v[100:101], v13 offset:49152\n"
    "ds_read_b64_tr_b16 v[102:103], v13 offset:50176\n"
    "buffer_load_dwordx4 v[180:183], v0, s[8:11], s71 offen offset:1024 sc1\n"
    "v_mfma_f32_16x16x32_bf16 v[28:31], v[108:111], v[140:143], v[28:31]\n"
    "ds_read_b64_tr_b16 v[104:105], v14 offset:49152\n"
    "ds_read_b64_tr_b16 v[106:107], v14 offset:50176\n"
    "s_waitcnt vmcnt(9)\n"
    "v_mfma_f32_16x16x32_bf16 v[16:19], v[112:115], v[144:147], v[16:19]\n"
    "ds_read_b64_tr_b16 v[108:109], v15 offset:49152\n"
    "ds_read_b64_tr_b16 v[110:111], v15 offset:50176\n"
    "v_mfma_f32_16x16x32_bf16 v[20:23], v[116:119], v[144:147], v[20:23]\n"
    "ds_read_b64_tr_b16 v[112:113], v12 offset:53248\n"
    "ds_read_b64_tr_b16 v[114:115], v12 offset:54272\n"
    "v_mfma_f32_16x16x32_bf16 v[24:27], v[120:123], v[144:147], v[24:27]\n"
    "ds_read_b64_tr_b16 v[116:117], v13 offset:53248\n"
    "ds_read_b64_tr_b16 v[118:119], v13 offset:54272\n"
    "buffer_load_dwordx4 v[184:187], v0, s[8:11], s71 offen offset:2048 sc1\n"
    "v_mfma_f32_16x16x32_bf16 v[28:31], v[124:127], v[144:147], v[28:31]\n"
    "ds_read_b64_tr_b16 v[120:121], v14 offset:53248\n"
    "ds_read_b64_tr_b16 v[122:123], v14 offset:54272\n"
    "s_waitcnt vmcnt(9)\n"
    "v_mfma_f32_16x16x32_bf16 v[16:19], v[208:211], v[148:151], v[16:19]\n"
    "ds_read_b64_tr_b16 v[124:125], v15 offset:53248\n"
    "ds_read_b64_tr_b16 v[126:127], v15 offset:54272\n"
    "v_mfma_f32_16x16x32_bf16 v[20:23], v[212:215], v[148:151], v[20:23]\n"
    "ds_read_b64_tr_b16 v[208:209], v12 offset:57344\n"
    "ds_read_b64_tr_b16 v[210:211], v12 offset:58368\n"
    "v_mfma_f32_16x16x32_bf16 v[24:27], v[216:219], v[148:151], v[24:27]\n"
    "ds_read_b64_tr_b16 v[212:213], v13 offset:57344\n"
    "ds_read_b64_tr_b16 v[214:215], v13 offset:58368\n"
    "buffer_load_dwordx4 v[188:191], v0, s[8:11], s71 offen offset:3072 sc1\n"
    "v_mfma_f32_16x16x32_bf16 v[28:31], v[220:223], v[148:151], v[28:31]\n"
    "ds_read_b64_tr_b16 v[216:217], v14 offset:57344\n"
    "ds_read_b64_tr_b16 v[218:219], v14 offset:58368\n"
    "s_waitcnt lgkmcnt(15) vmcnt(9)\n"
    "v_mfma_f32_16x16x32_bf16 v[16:19], v[224:227], v[152:155], v[16:19]\n"
    "ds_read_b64_tr_b16 v[220:221], v15 offset:57344\n"
    "ds_read_b64_tr_b16 v[222:223], v15 offset:58368\n"
    "v_mfma_f32_16x16x32_bf16 v[20:23], v[228:231], v[152:155], v[20:23]\n"
    "ds_read_b64_tr_b16 v[224:225], v12 offset:61440\n"
    "ds_read_b64_tr_b16 v[226:227], v12 offset:62464\n"
    "s_add_u32 m0, s66, 69632\n"
    "s_add_u32 s69, s77, 36864\n"
    "v_mfma_f32_16x16x32_bf16 v[24:27], v[232:235], v[152:155], v[24:27]\n"
    "ds_read_b64_tr_b16 v[228:229], v13 offset:61440\n"
    "ds_read_b64_tr_b16 v[230:231], v13 offset:62464\n"
    "buffer_load_dwordx4 v0, s[8:11], s69 offen offset:0 sc1 lds\n"
    "v_mfma_f32_16x16x32_bf16 v[28:31], v[236:239], v[152:155], v[28:31]\n"
    "ds_read_b64_tr_b16 v[232:233], v14 offset:61440\n"
    "ds_read_b64_tr_b16 v[234:235], v14 offset:62464\n"
    "s_waitcnt vmcnt(9)\n"
    "v_mfma_f32_16x16x32_bf16 v[16:19], v[240:243], v[156:159], v[16:19]\n"
    "ds_read_b64_tr_b16 v[236:237], v15 offset:61440\n"
    "ds_read_b64_tr_b16 v[238:239], v15 offset:62464\n"
    "v_mfma_f32_16x16x32_bf16 v[20:23], v[244:247], v[156:159], v[20:23]\n"
    "buffer_load_dwordx4 v0, s[8:11], s69 offen offset:1024 sc1 lds\n"
    "v_mfma_f32_16x16x32_bf16 v[24:27], v[248:251], v[156:159], v[24:27]\n"
    "v_mfma_f32_16x16x32_bf16 v[28:31], v[252:255], v[156:159], v[28:31]\n"
    "s_waitcnt vmcnt(9)\n"
    "v_mfma_f32_16x16x32_bf16 v[16:19], v[32:35], v[160:163], v[16:19]\n"
    "buffer_atomic_add a255, v3, s[8:11], s65 offen\n"
    "s_mov_b32 s65, 2.0\n"
    "v_mfma_f32_16x16x32_bf16 v[20:23], v[36:39], v[160:163], v[20:23]\n"
    "buffer_load_dwordx4 v0, s[8:11], s69 offen offset:2048 sc1 lds\n"
    "v_mfma_f32_16x16x32_bf16 v[24:27], v[40:43], v[160:163], v[24:27]\n"
    "v_mfma_f32_16x16x32_bf16 v[28:31], v[44:47], v[160:163], v[28:31]\n"
    "s_waitcnt vmcnt(10)\n"
    "v_mfma_f32_16x16x32_bf16 v[16:19], v[48:51], v[164:167], v[16:19]\n"
    "buffer_load_dwordx4 v0, s[8:11], s69 offen offset:3072 sc1 lds\n"
    "v_mfma_f32_16x16x32_bf16 v[20:23], v[52:55], v[164:167], v[20:23]\n"
    "s_add_u32 m0, s66, 0\n"
    "s_add_u32 s69, s78, 0\n"
    "v_mfma_f32_16x16x32_bf16 v[24:27], v[56:59], v[164:167], v[24:27]\n"
    "v_mfma_f32_16x16x32_bf16 v[28:31], v[60:63], v[164:167], v[28:31]\n"
    "s_waitcnt vmcnt(10)\n"
    "v_mfma_f32_16x16x32_bf16 v[16:19], v[64:67], v[168:171], v[16:19]\n"
    "buffer_load_dwordx4 v0, s[8:11], s69 offen offset:0 sc1 lds\n"
    "v_mfma_f32_16x16x32_bf16 v[20:23], v[68:71], v[168:171], v[20:23]\n"
    "v_mfma_f32_16x16x32_bf16 v[24:27], v[72:75], v[168:171], v[24:27]\n"
    "v_mfma_f32_16x16x32_bf16 v[28:31], v[76:79], v[168:171], v[28:31]\n"
    "s_waitcnt vmcnt(10)\n"
    "v_mfma_f32_16x16x32_bf16 v[16:19], v[80:83], v[172:175], v[16:19]\n"
    "buffer_load_dwordx4 v0, s[8:11], s69 offen offset:1024 sc1 lds\n"
    "v_mfma_f32_16x16x32_bf16 v[20:23], v[84:87], v[172:175], v[20:23]\n"
    "v_mfma_f32_16x16x32_bf16 v[24:27], v[88:91], v[172:175], v[24:27]\n"
    "v_mfma_f32_16x16x32_bf16 v[28:31], v[92:95], v[172:175], v[28:31]\n"
    "s_waitcnt lgkmcnt(15) vmcnt(10)\n"
    "v_mfma_f32_16x16x32_bf16 v[16:19], v[96:99], v[176:179], v[16:19]\n"
    "buffer_load_dwordx4 v0, s[8:11], s69 offen offset:2048 sc1 lds\n"
    "v_mfma_f32_16x16x32_bf16 v[20:23], v[100:103], v[176:179], v[20:23]\n"
    "v_mfma_f32_16x16x32_bf16 v[24:27], v[104:107], v[176:179], v[24:27]\n"
    "v_mfma_f32_16x16x32_bf16 v[28:31], v[108:111], v[176:179], v[28:31]\n"
    "s_waitcnt vmcnt(10)\n"
    "v_mfma_f32_16x16x32_bf16 v[16:19], v[112:115], v[180:183], v[16:19]\n"
    "buffer_load_dwordx4 v0, s[8:11], s69 offen offset:3072 sc1 lds\n"
    "v_mfma_f32_16x16x32_bf16 v[20:23], v[116:119], v[180:183], v[20:23]\n"
    "v_mfma_f32_16x16x32_bf16 v[24:27], v[120:123], v[180:183], v[24:27]\n"
    "v_mfma_f32_16x16x32_bf16 v[28:31], v[124:127], v[180:183], v[28:31]\n"
    "s_waitcnt lgkmcnt(0) vmcnt(10)\n"
    "v_mfma_f32_16x16x32_bf16 v[16:19], v[208:211], v[184:187], v[16:19]\n"
    "buffer_store_dwordx2 v[4:5], v6, s[8:11], s62 offen sc1\n"
    "v_mfma_f32_16x16x32_bf16 v[20:23], v[212:215], v[184:187], v[20:23]\n"
    "v_mfma_f32_16x16x32_bf16 v[24:27], v[216:219], v[184:187], v[24:27]\n"
    "v_mfma_f32_16x16x32_bf16 v[28:31], v[220:223], v[184:187], v[28:31]\n"
    "s_waitcnt vmcnt(10)\n"
    "v_mfma_f32_16x16x32_bf16 v[16:19], v[224:227], v[188:191], v[16:19]\n"
    "v_mfma_f32_16x16x32_bf16 v[20:23], v[228:231], v[188:191], v[20:23]\n"
    "v_mfma_f32_16x16x32_bf16 v[24:27], v[232:235], v[188:191], v[24:27]\n"
    "v_mfma_f32_16x16x32_bf16 v[28:31], v[236:239], v[188:191], v[28:31]\n"
    "s_nop 0\n"
    "s_nop 4\n"
    "v_cmp_u_f32 s[30:31], v16, v16\n"
    "v_cvt_pk_bf16_f32 v80, v16, v17\n"
    "v_cvt_pk_bf16_f32 v81, v18, v19\n"
    "v_cvt_pk_bf16_f32 v82, v20, v21\n"
    "v_cvt_pk_bf16_f32 v83, v22, v23\n"
    "v_cvt_pk_bf16_f32 v84, v24, v25\n"
    "v_cvt_pk_bf16_f32 v85, v26, v27\n"
    "v_cvt_pk_bf16_f32 v86, v28, v29\n"
    "v_cvt_pk_bf16_f32 v87, v30, v31\n"
    "s_or_b64 s[30:31], s[30:31], s[30:31]\n"
    "s_cbranch_scc1 Lbad12\n"
    "Lok12:\n"
    "s_add_u32 s73, s81, 12193792\n"
    "buffer_store_dwordx4 v[80:83], v0, s[8:11], s73 offen sc1\n"
    "buffer_store_dwordx4 v[84:87], v0, s[8:11], s73 offen offset:2048 sc1\n"
    "s_add_u32 s68, s94, 65536\n"
    "v_mov_b32 v8, 0\n"
    "buffer_atomic_add v8, v6, s[8:11], s68 offen sc0\n"
    "s_barrier\n"
    "v_mov_b32 v88, 0x3f803f80\n"
    "s_add_u32 s70, s79, 8192\n"
    "buffer_load_dwordx4 a[32:35], v0, s[8:11], s70 offen offset:0 sc1\n"
    "v_mov_b32 v92, 0\n"
    "v_mov_b32 v93, 0\n"
    "buffer_load_dwordx4 a[36:39], v0, s[8:11], s70 offen offset:1024 sc1\n"
    "v_dot2c_f32_bf16 v92, v88, v80\n"
    "v_dot2c_f32_bf16 v93, v80, v80\n"
    "buffer_load_dwordx4 a[40:43], v0, s[8:11], s70 offen offset:2048 sc1\n"
    "v_dot2c_f32_bf16 v92, v88, v81\n"
    "v_dot2c_f32_bf16 v93, v81, v81\n"
    "buffer_load_dwordx4 a[44:47], v0, s[8:11], s70 offen offset:3072 sc1\n"
    "v_dot2c_f32_bf16 v92, v88, v82\n"
    "v_dot2c_f32_bf16 v93, v82, v82\n"
    "s_add_u32 s70, s79, 12288\n"
    "buffer_load_dwordx4 a[48:51], v0, s[8:11], s70 offen offset:0 sc1\n"
    "v_dot2c_f32_bf16 v92, v88, v83\n"
    "v_dot2c_f32_bf16 v93, v83, v83\n"
    "buffer_load_dwordx4 a[52:55], v0, s[8:11], s70 offen offset:1024 sc1\n"
    "v_dot2c_f32_bf16 v92, v88, v84\n"
    "v_dot2c_f32_bf16 v93, v84, v84\n"
    "buffer_load_dwordx4 a[56:59], v0, s[8:11], s70 offen offset:2048 sc1\n"
    "v_dot2c_f32_bf16 v92, v88, v85\n"
    "v_dot2c_f32_bf16 v93, v85, v85\n"
    "buffer_load_dwordx4 a[60:63], v0, s[8:11], s70 offen offset:3072 sc1\n"
    "v_dot2c_f32_bf16 v92, v88, v86\n"
    "v_dot2c_f32_bf16 v93, v86, v86\n"
    "v_dot2c_f32_bf16 v92, v88, v87\n"
    "v_dot2c_f32_bf16 v93, v87, v87\n"
    "v_mul_u32_u24 v7, 17, v0\n"
    "v_cmp_gt_u32 vcc, 0x100, v0\n"
    "s_nop 0\n"
    "v_permlane32_swap_b32 v92, v93\n"
    "v_add_f32 v92, v92, v93\n"
    "v_mov_b32 v93, v92\n"
    "v_bfe_u32 v7, v7, 7, 4\n"
    "v_lshlrev_b32 v7, 3, v7\n"
    "v_permlane16_swap_b32 v92, v93\n"
    "v_add_f32 v92, v92, v93\n"
    "v_mov_b32 v93, v92\n"
    "v_cndmask_b32 v7, 2.0, v7, vcc\n"
    "s_add_u32 s73, s82, 12193792\n"
    "v_permlane32_swap_b32_e64 v92, v93\n"
    "buffer_store_dwordx2 v[92:93], v7, s[8:11], s73 offen sc1\n"
    "v_accvgpr_write_b32 a220, v16\n"
    "v_accvgpr_write_b32 a221, v17\n"
    "v_accvgpr_write_b32 a222, v18\n"
    "v_accvgpr_write_b32 a223, v19\n"
    "v_accvgpr_write_b32 a224, v20\n"
    "v_accvgpr_write_b32 a225, v21\n"
    "v_accvgpr_write_b32 a226, v22\n"
    "v_accvgpr_write_b32 a227, v23\n"
    "v_accvgpr_write_b32 a228, v24\n"
    "v_accvgpr_write_b32 a229, v25\n"
    "v_accvgpr_write_b32 a230, v26\n"
    "v_accvgpr_write_b32 a231, v27\n"
    "v_accvgpr_write_b32 a232, v28\n"
    "v_accvgpr_write_b32 a233, v29\n"
    "v_accvgpr_write_b32 a234, v30\n"
    "v_accvgpr_write_b32 a235, v31\n"
    "v_and_b32 v15, 0x200, v0\n"
    "v_add_u32 v15, v0, v15\n"
    "v_xor_b32 v15, s92, v15\n"
    "s_and_b32 s63, s60, 4\n"
    "s_lshl_b32 s63, s63, 8\n"
    "v_and_b32 v14, 0x200, v0\n"
    "v_add_u32 v14, v0, v14\n"
    "v_xor_b32 v14, s63, v14\n"
    "v_cndmask_b32 v14, v14, 2.0, s[54:55]\n"
    "v_add_f32 v192, v192, v196\n"
    "v_add_f32 v200, v200, v204\n"
    "v_add_f32 v193, v193, v197\n"
    "v_add_f32 v201, v201, v205\n"
    "v_add_f32 v194, v194, v198\n"
    "v_add_f32 v202, v202, v206\n"
    "v_add_f32 v192, v192, v200\n"
    "v_add_f32 v193, v193, v201\n"
    "v_add_f32 v194, v194, v202\n"
    "v_cvt_pk_bf16_f32 v196, v192, v192\n"
    "v_lshlrev_b32 v196, 16, v196\n"
    "v_sub_f32 v192, v192, v196\n"
    "v_cvt_pk_bf16_f32 v200, v196, v192\n"
    "v_and_b32 v201, 0xffff0000, v200\n"
    "v_sub_f32 v192, v192, v201\n"
    "v_cvt_pk_bf16_f32 v201, v192, v196\n"
    "v_cvt_pk_bf16_f32 v197, v193, v193\n"
    "v_lshlrev_b32 v197, 16, v197\n"
    "v_sub_f32 v193, v193, v197\n"
    "v_cvt_pk_bf16_f32 v202, v197, v193\n"
    "v_and_b32 v203, 0xffff0000, v202\n"
    "v_sub_f32 v193, v193, v203\n"
    "v_cvt_pk_bf16_f32 v203, v193, v197\n"
    "v_cvt_pk_bf16_f32 v198, v194, v194\n"
    "v_lshlrev_b32 v198, 16, v198\n"
    "v_sub_f32 v194, v194, v198\n"
    "v_cvt_pk_bf16_f32 v204, v198, v194\n"
    "v_and_b32 v205, 0xffff0000, v204\n"
    "v_sub_f32 v194, v194, v205\n"
    "v_cvt_pk_bf16_f32 v205, v194, v198\n"
    "v_lshrrev_b32 v13, 1, v0\n"
    "s_and_b32 s62, s58, 1\n"
    "s_lshl_b32 s62, s62, 13\n"
    "s_lshr_b32 s63, s58, 1\n"
    "s_lshl_b32 s63, s63, 9\n"
    "s_add_u32 s63, s62, s63\n"
    "v_add_u32 v192, s63, v13\n"
    "ds_write_b64 v192, v[200:201] offset:4096\n"
    "ds_write_b64 v192, v[202:203] offset:5120\n"
    "ds_write_b64 v192, v[204:205] offset:6144\n"
    "s_waitcnt lgkmcnt(0) vmcnt(9)\n"
    "v_readfirstlane_b32 s29, v8\n"
    "s_nop 0\n"
    "s_cmp_lt_u32 s29, s95\n"
    "s_cbranch_scc1 Lslow13\n"
    "Lback13:\n"
    "s_nop 0\n"
    "s_barrier\n"
    "s_add_u32 s71, s80, 12193792\n"
    "buffer_load_dwordx4 v[128:131], v0, s[8:11], s71 offen offset:0 sc1\n"
    "buffer_load_dwordx4 v[132:135], v0, s[8:11], s71 offen offset:1024 sc1\n"
    "buffer_load_dwordx4 v[136:139], v0, s[8:11], s71 offen offset:2048 sc1\n"
    "buffer_load_dwordx4 v[140:143], v0, s[8:11], s71 offen offset:3072 sc1\n"
    "s_add_u32 s71, s80, 12197888\n"
    "buffer_load_dwordx4 v[144:147], v0, s[8:11], s71 offen offset:0 sc1\n"
    "buffer_load_dwordx4 v[148:151], v0, s[8:11], s71 offen offset:1024 sc1\n"
    "ds_read_b64 a[188:189], v13 offset:4096\n"
    "ds_read_b64 a[190:191], v13 offset:4608\n"
    "ds_read_b64 a[192:193], v13 offset:5120\n"
    "ds_read_b64 a[194:195], v13 offset:5632\n"
    "ds_read_b64 a[196:197], v13 offset:6144\n"
    "ds_read_b64 a[198:199], v13 offset:6656\n"
    "ds_read_b64 a[200:201], v13 offset:12288\n"
    "ds_read_b64 a[202:203], v13 offset:12800\n"
    "ds_read_b64 a[204:205], v13 offset:13312\n"
    "ds_read_b64 a[206:207], v13 offset:13824\n"
    "ds_read_b64 a[208:209], v13 offset:14336\n"
    "buffer_load_dwordx4 v[152:155], v0, s[8:11], s71 offen offset:2048 sc1\n"
    "buffer_load_dwordx4 v[156:159], v0, s[8:11], s71 offen offset:3072 sc1\n"
    "s_add_u32 s71, s80, 12201984\n"
    "buffer_load_dwordx4 v[160:163], v0, s[8:11], s71 offen offset:0 sc1\n"
    "buffer_load_dwordx4 v[164:167], v0, s[8:11], s71 offen offset:1024 sc1\n"
    "s_waitcnt lgkmcnt(0)\n"
    "s_barrier\n"
    "Lredo16:\n"
    "ds_read_b128 v[80:83], v0 offset:32768\n"
    "ds_read_b128 v[84:87], v0 offset:33792\n"
    "ds_read_b128 v[88:91], v1 offset:0\n"
    "ds_read_b128 v[92:95], v1 offset:1024\n"
    "ds_read_b128 v[96:99], v0 offset:0\n"
    "ds_read_b128 v[64:67], v0 offset:34816\n"
    "ds_read_b128 v[68:71], v0 offset:35840\n"
    "ds_read_b128 v[72:75], v1 offset:2048\n"
    "buffer_load_dwordx4 v[168:171], v0, s[8:11], s71 offen offset:2048 sc1\n"
    "buffer_load_dwordx4 v[172:175], v0, s[8:11], s71 offen offset:3072 sc1\n"
    "s_waitcnt vmcnt(11)\n"
    "v_max3_u32 v7, v128, v129, v130\n"
    "v_max_u32 v7, v7, v131\n"
    "s_waitcnt vmcnt(10)\n"
    "v_max3_u32 v7, v7, v132, v133\n"
    "v_max3_u32 v7, v7, v134, v135\n"
    "v_cmp_eq_u32 s[26:27], -1, v7\n"
    "s_nop 0\n"
    "s_cmp_lg_u64 s[26:27], 0\n"
    "s_cbranch_scc1 Lgb18\n"
    "Lgo18:\n"
    "s_waitcnt lgkmcnt(3)\n"
    "v_mfma_f32_16x16x32_bf16 v[16:19], v[80:83], v[128:131], 0\n"
    "ds_read_b128 v[76:79], v1 offset:3072\n"
    "s_add_u32 s71, s80, 12206080\n"
    "v_mfma_f32_16x16x32_bf16 v[24:27], v[80:83], v[132:135], 0\n"
    "buffer_load_dwordx4 v[176:179], v0, s[8:11], s71 offen offset:0 sc1\n"
    "v_mfma_f32_16x16x32_bf16 v[28:31], v[84:87], v[132:135], 0\n"
    "ds_read_b128 v[80:83], v0 offset:1024\n"
    "v_mfma_f32_16x16x32_bf16 v[20:23], v[84:87], v[128:131], 0\n"
    "v_mfma_f32_16x16x32_bf16 v[32:35], v[88:91], v[128:131], 0\n"
    "ds_read_b128 v[84:87], v0 offset:36864\n"
    "s_waitcnt vmcnt(10)\n"
    "v_max3_u32 v7, v136, v137, v138\n"
    "v_max_u32 v7, v7, v139\n"
    "v_mfma_f32_16x16x32_bf16 v[40:43], v[88:91], v[132:135], 0\n"
    "buffer_load_dwordx4 v[180:183], v0, s[8:11], s71 offen offset:1024 sc1\n"
    "s_waitcnt vmcnt(10)\n"
    "v_max3_u32 v7, v7, v140, v141\n"
    "v_mfma_f32_16x16x32_bf16 v[44:47], v[92:95], v[132:135], 0\n"
    "ds_read_b128 v[88:91], v0 offset:37888\n"
    "v_max3_u32 v7, v7, v142, v143\n"
    "v_mfma_f32_16x16x32_bf16 v[36:39], v[92:95], v[128:131], 0\n"
    "v_cmp_eq_u32 s[26:27], -1, v7\n"
    "v_mfma_f32_16x16x32_bf16 v[48:51], v[96:99], v[128:131], 0\n"
    "ds_read_b128 v[92:95], v1 offset:4096\n"
    "s_cmp_lg_u64 s[26:27], 0\n"
    "s_cbranch_scc1 Lgb19\n"
    "Lgo19:\n"
    "v_mfma_f32_16x16x32_bf16 v[56:59], v[96:99], v[132:135], 0\n"
    "s_waitcnt lgkmcnt(3)\n"
    "v_mfma_f32_16x16x32_bf16 v[24:27], v[64:67], v[140:143], v[24:27]\n"
    "ds_read_b128 v[96:99], v1 offset:5120\n"
    "buffer_load_dwordx4 v[184:187], v0, s[8:11], s71 offen offset:2048 sc1\n"
    "v_mfma_f32_16x16x32_bf16 v[16:19], v[64:67], v[136:139], v[16:19]\n"
    "v_mfma_f32_16x16x32_bf16 v[20:23], v[68:71], v[136:139], v[20:23]\n"
    "ds_read_b128 v[64:67], v0 offset:2048\n"
    "v_mfma_f32_16x16x32_bf16 v[28:31], v[68:71], v[140:143], v[28:31]\n"
    "buffer_load_dwordx4 v[188:191], v0, s[8:11], s71 offen offset:3072 sc1\n"
    "v_mfma_f32_16x16x32_bf16 v[40:43], v[72:75], v[140:143], v[40:43]\n"
    "ds_read_b128 v[68:71], v0 offset:38912\n"
    "s_add_u32 s72, s80, 12226560\n"
    "v_mfma_f32_16x16x32_bf16 v[32:35], v[72:75], v[136:139], v[32:35]\n"
    "v_mfma_f32_16x16x32_bf16 v[36:39], v[76:79], v[136:139], v[36:39]\n"
    "ds_read_b128 v[72:75], v0 offset:39936\n"
    "v_mfma_f32_16x16x32_bf16 v[44:47], v[76:79], v[140:143], v[44:47]\n"
    "buffer_load_dwordx4 v[100:103], v0, s[8:11], s72 offen offset:0 sc1\n"
    "v_mfma_f32_16x16x32_bf16 v[56:59], v[80:83], v[140:143], v[56:59]\n"
    "ds_read_b128 v[76:79], v1 offset:6144\n"
    "v_mfma_f32_16x16x32_bf16 v[48:51], v[80:83], v[136:139], v[48:51]\n"
    "s_add_u32 s71, s80, 12210176\n"
    "s_waitcnt vmcnt(11) lgkmcnt(3)\n"
    "v_mfma_f32_16x16x32_bf16 v[16:19], v[84:87], v[144:147], v[16:19]\n"
    "ds_read_b128 v[80:83], v1 offset:7168\n"
    "v_mfma_f32_16x16x32_bf16 v[24:27], v[84:87], v[148:151], v[24:27]\n"
    "v_mfma_f32_16x16x32_bf16 v[28:31], v[88:91], v[148:151], v[28:31]\n"
    "ds_read_b128 v[84:87], v0 offset:3072\n"
    "v_mfma_f32_16x16x32_bf16 v[20:23], v[88:91], v[144:147], v[20:23]\n"
    "buffer_load_dwordx4 v[192:195], v0, s[8:11], s71 offen offset:0 sc1\n"
    "v_mfma_f32_16x16x32_bf16 v[32:35], v[92:95], v[144:147], v[32:35]\n"
    "ds_read_b128 v[88:91], v0 offset:40960\n"
    "v_mfma_f32_16x16x32_bf16 v[40:43], v[92:95], v[148:151], v[40:43]\n"
    "v_mfma_f32_16x16x32_bf16 v[44:47], v[96:99], v[148:151], v[44:47]\n"
    "ds_read_b128 v[92:95], v0 offset:41984\n"
    "v_mfma_f32_16x16x32_bf16 v[36:39], v[96:99], v[144:147], v[36:39]\n"
    "buffer_load_dwordx4 v[196:199], v0, s[8:11], s71 offen offset:1024 sc1\n"
    "v_mfma_f32_16x16x32_bf16 v[48:51], v[64:67], v[144:147], v[48:51]\n"
    "ds_read_b128 v[96:99], v1 offset:8192\n"
    "v_mfma_f32_16x16x32_bf16 v[56:59], v[64:67], v[148:151], v[56:59]\n"
    "s_waitcnt vmcnt(11) lgkmcnt(3)\n"
    "v_mfma_f32_16x16x32_bf16 v[24:27], v[68:71], v[156:159], v[24:27]\n"
    "ds_read_b128 v[64:67], v1 offset:9216\n"
    "v_mfma_f32_16x16x32_bf16 v[16:19], v[68:71], v[152:155], v[16:19]\n"
    "buffer_load_dwordx4 v[104:107], v0, s[8:11], s72 offen offset:1024 sc1\n"
    "v_mfma_f32_16x16x32_bf16 v[20:23], v[72:75], v[152:155], v[20:23]\n"
    "ds_read_b128 v[68:71], v0 offset:8192\n"
    "v_mfma_f32_16x16x32_bf16 v[28:31], v[72:75], v[156:159], v[28:31]\n"
    "v_mfma_f32_16x16x32_bf16 v[40:43], v[76:79], v[156:159], v[40:43]\n"
    "ds_read_b128 v[72:75], v0 offset:43008\n"
    "v_mfma_f32_16x16x32_bf16 v[32:35], v[76:79], v[152:155], v[32:35]\n"
    "buffer_load_dwordx4 v[200:203], v0, s[8:11], s71 offen offset:2048 sc1\n"
    "v_mfma_f32_16x16x32_bf16 v[36:39], v[80:83], v[152:155], v[36:39]\n"
    "ds_read_b128 v[76:79], v0 offset:44032\n"
    "v_mfma_f32_16x16x32_bf16 v[44:47], v[80:83], v[156:159], v[44:47]\n"
    "v_mfma_f32_16x16x32_bf16 v[56:59], v[84:87], v[156:159], v[56:59]\n"
    "ds_read_b128 v[80:83], v1 offset:10240\n"
    "v_mfma_f32_16x16x32_bf16 v[48:51], v[84:87], v[152:155], v[48:51]\n"
    "s_waitcnt vmcnt(11) lgkmcnt(3)\n"
    "v_mfma_f32_16x16x32_bf16 v[16:19], v[88:91], v[160:163], v[16:19]\n"
    "ds_read_b128 v[84:87], v1 offset:11264\n"
    "v_mfma_f32_16x16x32_bf16 v[24:27], v[88:91], v[164:167], v[24:27]\n"
    "buffer_load_dwordx4 v[204:207], v0, s[8:11], s71 offen offset:3072 sc1\n"
    "v_mfma_f32_16x16x32_bf16 v[28:31], v[92:95], v[164:167], v[28:31]\n"
    "ds_read_b128 v[88:91], v0 offset:9216\n"
    "s_add_u32 s71, s80, 12214272\n"
    "v_mfma_f32_16x16x32_bf16 v[20:23], v[92:95], v[160:163], v[20:23]\n"
    "v_mfma_f32_16x16x32_bf16 v[32:35], v[96:99], v[160:163], v[32:35]\n"
    "ds_read_b128 v[92:95], v0 offset:45056\n"
    "v_mfma_f32_16x16x32_bf16 v[40:43], v[96:99], v[164:167], v[40:43]\n"
    "buffer_load_dwordx4 v[208:211], v0, s[8:11], s71 offen offset:0 sc1\n"
    "v_mfma_f32_16x16x32_bf16 v[44:47], v[64:67], v[164:167], v[44:47]\n"
    "ds_read_b128 v[96:99], v0 offset:46080\n"
    "v_mfma_f32_16x16x32_bf16 v[36:39], v[64:67], v[160:163], v[36:39]\n"
    "v_cmp_u_f32 s[30:31], v16, v24\n"
    "v_mfma_f32_16x16x32_bf16 v[48:51], v[68:71], v[160:163], v[48:51]\n"
    "ds_read_b128 v[64:67], v1 offset:12288\n"
    "v_mfma_f32_16x16x32_bf16 v[56:59], v[68:71], v[164:167], v[56:59]\n"
    "s_or_b64 s[30:31], s[30:31], s[30:31]\n"
    "s_cbranch_scc1 Lbad17\n"
    "Lok17:\n"
    "s_waitcnt vmcnt(11) lgkmcnt(3)\n"
    "v_mfma_f32_16x16x32_bf16 v[24:27], v[72:75], v[172:175], v[24:27]\n"
    "ds_read_b128 v[68:71], v1 offset:13312\n"
    "buffer_load_dwordx4 v[212:215], v0, s[8:11], s71 offen offset:1024 sc1\n"
    "v_mfma_f32_16x16x32_bf16 v[16:19], v[72:75], v[168:171], v[16:19]\n"
    "v_mfma_f32_16x16x32_bf16 v[20:23], v[76:79], v[168:171], v[20:23]\n"
    "ds_read_b128 v[72:75], v0 offset:10240\n"
    "v_mfma_f32_16x16x32_bf16 v[28:31], v[76:79], v[172:175], v[28:31]\n"
    "buffer_load_dwordx4 v[216:219], v0, s[8:11], s71 offen offset:2048 sc1\n"
    "v_mfma_f32_16x16x32_bf16 v[40:43], v[80:83], v[172:175], v[40:43]\n"
    "ds_read_b128 v[76:79], v0 offset:47104\n"
    "v_mfma_f32_16x16x32_bf16 v[32:35], v[80:83], v[168:171], v[32:35]\n"
    "v_mfma_f32_16x16x32_bf16 v[36:39], v[84:87], v[168:171], v[36:39]\n"
    "ds_read_b128 v[80:83], v0 offset:48128\n"
    "v_mfma_f32_16x16x32_bf16 v[44:47], v[84:87], v[172:175], v[44:47]\n"
    "buffer_load_dwordx4 v[220:223], v0, s[8:11], s71 offen offset:3072 sc1\n"
    "v_mfma_f32_16x16x32_bf16 v[56:59], v[88:91], v[172:175], v[56:59]\n"
    "ds_read_b128 v[84:87], v1 offset:14336\n"
    "v_mfma_f32_16x16x32_bf16 v[48:51], v[88:91], v[168:171], v[48:51]\n"
    "s_add_u32 s71, s80, 12218368\n"
    "s_waitcnt vmcnt(12) lgkmcnt(3)\n"
    "v_mfma_f32_16x16x32_bf16 v[16:19], v[92:95], v[176:179], v[16:19]\n"
    "ds_read_b128 v[88:91], v1 offset:15360\n"
    "v_mfma_f32_16x16x32_bf16 v[24:27], v[92:95], v[180:183], v[24:27]\n"
    "v_mfma_f32_16x16x32_bf16 v[28:31], v[96:99], v[180:183], v[28:31]\n"
    "ds_read_b128 v[92:95], v0 offset:11264\n"
    "v_mfma_f32_16x16x32_bf16 v[20:23], v[96:99], v[176:179], v[20:23]\n"
    "buffer_load_dwordx4 v[224:227], v0, s[8:11], s71 offen offset:0 sc1\n"
    "v_mfma_f32_16x16x32_bf16 v[32:35], v[64:67], v[176:179], v[32:35]\n"
    "ds_read_b128 v[96:99], v0 offset:49152\n"
    "v_mfma_f32_16x16x32_bf16 v[40:43], v[64:67], v[180:183], v[40:43]\n"
    "v_mfma_f32_16x16x32_bf16 v[44:47], v[68:71], v[180:183], v[44:47]\n"
    "ds_read_b128 v[64:67], v0 offset:50176\n"
    "v_mfma_f32_16x16x32_bf16 v[36:39], v[68:71], v[176:179], v[36:39]\n"
    "buffer_load_dwordx4 v[228:231], v0, s[8:11], s71 offen offset:1024 sc1\n"
    "v_mfma_f32_16x16x32_bf16 v[48:51], v[72:75], v[176:179], v[48:51]\n"
    "ds_read_b128 v[68:71], v1 offset:16384\n"
    "v_mfma_f32_16x16x32_bf16 v[56:59], v[72:75], v[180:183], v[56:59]\n"
    "s_waitcnt vmcnt(12) lgkmcnt(3)\n"
    "v_mfma_f32_16x16x32_bf16 v[24:27], v[76:79], v[188:191], v[24:27]\n"
    "ds_read_b128 v[72:75], v1 offset:17408\n"
    "v_mfma_f32_16x16x32_bf16 v[16:19], v[76:79], v[184:187], v[16:19]\n"
    "buffer_load_dwordx4 v[232:235], v0, s[8:11], s71 offen offset:2048 sc1\n"
    "v_mfma_f32_16x16x32_bf16 v[20:23], v[80:83], v[184:187], v[20:23]\n"
    "ds_read_b128 v[76:79], v0 offset:16384\n"
    "v_mfma_f32_16x16x32_bf16 v[28:31], v[80:83], v[188:191], v[28:31]\n"
    "v_mfma_f32_16x16x32_bf16 v[40:43], v[84:87], v[188:191], v[40:43]\n"
    "ds_read_b128 v[80:83], v0 offset:51200\n"
    "v_mfma_f32_16x16x32_bf16 v[32:35], v[84:87], v[184:187], v[32:35]\n"
    "buffer_load_dwordx4 v[236:239], v0, s[8:11], s71 offen offset:3072 sc1\n"
    "v_mfma_f32_16x16x32_bf16 v[36:39], v[88:91], v[184:187], v[36:39]\n"
    "ds_read_b128 v[84:87], v0 offset:52224\n"
    "v_mfma_f32_16x16x32_bf16 v[44:47], v[88:91], v[188:191], v[44:47]\n"
    "s_add_u32 s71, s80, 12222464\n"
    "v_mfma_f32_16x16x32_bf16 v[56:59], v[92:95], v[188:191], v[56:59]\n"
    "ds_read_b128 v[88:91], v1 offset:18432\n"
    "v_mfma_f32_16x16x32_bf16 v[48:51], v[92:95], v[184:187], v[48:51]\n"
    "s_waitcnt vmcnt(11) lgkmcnt(3)\n"
    "v_mfma_f32_16x16x32_bf16 v[16:19], v[96:99], v[192:195], v[16:19]\n"
    "ds_read_b128 v[92:95], v1 offset:19456\n"
    "v_mfma_f32_16x16x32_bf16 v[24:27], v[96:99], v[196:199], v[24:27]\n"
    "buffer_load_dwordx4 v[240:243], v0, s[8:11], s71 offen offset:0 sc1\n"
    "v_mfma_f32_16x16x32_bf16 v[28:31], v[64:67], v[196:199], v[28:31]\n"
    "ds_read_b128 v[96:99], v0 offset:17408\n"
    "v_mfma_f32_16x16x32_bf16 v[20:23], v[64:67], v[192:195], v[20:23]\n"
    "v_mfma_f32_16x16x32_bf16 v[32:35], v[68:71], v[192:195], v[32:35]\n"
    "ds_read_b128 v[64:67], v0 offset:53248\n"
    "v_mfma_f32_16x16x32_bf16 v[40:43], v[68:71], v[196:199], v[40:43]\n"
    "buffer_load_dwordx4 v[244:247], v0, s[8:11], s71 offen offset:1024 sc1\n"
    "v_mfma_f32_16x16x32_bf16 v[44:47], v[72:75], v[196:199], v[44:47]\n"
    "ds_read_b128 v[68:71], v0 offset:54272\n"
    "v_mfma_f32_16x16x32_bf16 v[36:39], v[72:75], v[192:195], v[36:39]\n"
    "v_mfma_f32_16x16x32_bf16 v[48:51], v[76:79], v[192:195], v[48:51]\n"
    "ds_read_b128 v[72:75], v1 offset:20480\n"
    "v_mfma_f32_16x16x32_bf16 v[56:59], v[76:79], v[196:199], v[56:59]\n"
    "s_waitcnt vmcnt(10) lgkmcnt(3)\n"
    "v_mfma_f32_16x16x32_bf16 v[24:27], v[80:83], v[204:207], v[24:27]\n"
    "ds_read_b128 v[76:79], v1 offset:21504\n"
    "buffer_load_dwordx4 v[248:251], v0, s[8:11], s71 offen offset:2048 sc1\n"
    "v_max3_u32 v110, v100, v101, v102\n"
    "v_max_u32 v110, v110, v103\n"
    "v_mfma_f32_16x16x32_bf16 v[16:19], v[80:83], v[200:203], v[16:19]\n"
    "v_max3_u32 v110, v110, v104, v105\n"
    "v_max3_u32 v110, v110, v106, v107\n"
    "v_mfma_f32_16x16x32_bf16 v[20:23], v[84:87], v[200:203], v[20:23]\n"
    "ds_read_b128 v[80:83], v0 offset:18432\n"
    "v_cmp_eq_u32 s[26:27], -1, v110\n"
    "v_mfma_f32_16x16x32_bf16 v[28:31], v[84:87], v[204:207], v[28:31]\n"
    "buffer_load_dwordx4 v[252:255], v0, s[8:11], s71 offen offset:3072 sc1\n"
    "s_cmp_lg_u64 s[26:27], 0\n"
    "s_cbranch_scc1 Lgb20\n"
    "Lgo20:\n"
    "v_mfma_f32_16x16x32_bf16 v[40:43], v[88:91], v[204:207], v[40:43]\n"
    "ds_read_b128 v[84:87], v0 offset:55296\n"
    "s_and_b32 s74, s60, 4\n"
    "v_mfma_f32_16x16x32_bf16 v[32:35], v[88:91], v[200:203], v[32:35]\n"
    "Lpad4:\n"
    "s_cbranch_execz Lpad8\n"
    "s_lshl_b32 s74, s74, 8\n"
    "v_and_b32 v7, 0x200, v0\n"
    "v_mfma_f32_16x16x32_bf16 v[36:39], v[92:95], v[200:203], v[36:39]\n"
    "ds_read_b128 v[88:91], v0 offset:56320\n"
    "v_add_u32 v7, v0, v7\n"
    "v_add_f32_dpp v112, v100, v100 row_ror:8 row_mask:0xf bank_mask:3\n"
    "v_mfma_f32_16x16x32_bf16 v[44:47], v[92:95], v[204:207], v[44:47]\n"
    "v_xor_b32 v7, s74, v7\n"
    "v_cndmask_b32 v7, v7, 2.0, s[54:55]\n"
    "v_mfma_f32_16x16x32_bf16 v[56:59], v[96:99], v[204:207], v[56:59]\n"
    "ds_read_b128 v[92:95], v1 offset:22528\n"
    "s_add_u32 s74, s86, 23072768\n"
    "v_mfma_f32_16x16x32_bf16 v[48:51], v[96:99], v[200:203], v[48:51]\n"
    "buffer_store_dwordx4 a[236:239], v7, s[8:11], s74 offen sc1\n"
    "v_add_f32_dpp v113, v101, v101 row_ror:8 row_mask:0xf bank_mask:3\n"
    "s_waitcnt vmcnt(11) lgkmcnt(3)\n"
    "v_mfma_f32_16x16x32_bf16 v[16:19], v[64:67], v[208:211], v[16:19]\n"
    "ds_read_b128 v[96:99], v1 offset:23552\n"
    "s_add_u32 m0, s66, 98304\n"
    "v_mfma_f32_16x16x32_bf16 v[24:27], v[64:67], v[212:215], v[24:27]\n"
    "s_add_u32 s69, s77, 65536\n"
    "v_add_f32_dpp v114, v104, v104 row_ror:8 row_mask:0xf bank_mask:3\n"
    "v_add_f32_dpp v115, v105, v105 row_ror:8 row_mask:0xf bank_mask:3\n"
    "v_mfma_f32_16x16x32_bf16 v[28:31], v[68:71], v[212:215], v[28:31]\n"
    "ds_read_b128 v[64:67], v0 offset:19456\n"
    "buffer_load_dwordx4 v0, s[8:11], s69 offen offset:0 sc1 lds\n"
    "v_mfma_f32_16x16x32_bf16 v[20:23], v[68:71], v[208:211], v[20:23]\n"
    "buffer_load_dwordx4 v0, s[8:11], s69 offen offset:1024 sc1 lds\n"
    "v_mfma_f32_16x16x32_bf16 v[32:35], v[72:75], v[208:211], v[32:35]\n"
    "ds_read_b128 v[68:71], v0 offset:57344\n"
    "v_add_f32_dpp v112, v102, v102 row_ror:8 row_mask:0xf bank_mask:12\n"
    "v_add_f32_dpp v113, v103, v103 row_ror:8 row_mask:0xf bank_mask:12\n"
    "v_mfma_f32_16x16x32_bf16 v[40:43], v[72:75], v[212:215], v[40:43]\n"
    "buffer_load_dwordx4 v0, s[8:11], s69 offen offset:2048 sc1 lds\n"
    "v_mfma_f32_16x16x32_bf16 v[44:47], v[76:79], v[212:215], v[44:47]\n"
    "ds_read_b128 v[72:75], v0 offset:58368\n"
    "v_add_f32_dpp v114, v106, v106 row_ror:8 row_mask:0xf bank_mask:12\n"
    "v_mfma_f32_16x16x32_bf16 v[36:39], v[76:79], v[208:211], v[36:39]\n"
    "v_add_f32_dpp v115, v107, v107 row_ror:8 row_mask:0xf bank_mask:12\n"
    "v_permlane32_swap_b32 v112, v113\n"
    "v_mfma_f32_16x16x32_bf16 v[48:51], v[80:83], v[208:211], v[48:51]\n"
    "ds_read_b128 v[76:79], v1 offset:24576\n"
    "v_mfma_f32_16x16x32_bf16 v[56:59], v[80:83], v[212:215], v[56:59]\n"
    "v_permlane32_swap_b32 v114, v115\n"
    "s_waitcnt vmcnt(12) lgkmcnt(3)\n"
    "v_mfma_f32_16x16x32_bf16 v[24:27], v[84:87], v[220:223], v[24:27]\n"
    "ds_read_b128 v[80:83], v1 offset:25600\n"
    "buffer_load_dwordx4 v0, s[8:11], s69 offen offset:3072 sc1 lds\n"
    "v_mfma_f32_16x16x32_bf16 v[16:19], v[84:87], v[216:219], v[16:19]\n"
    "s_add_u32 m0, s66, 102400\n"
    "s_add_u32 s69, s77, 69632\n"
    "v_mfma_f32_16x16x32_bf16 v[20:23], v[88:91], v[216:219], v[20:23]\n"
    "ds_read_b128 v[84:87], v0 offset:24576\n"
    "v_add_f32 v112, v112, v113\n"
    "v_add_f32 v114, v114, v115\n"
    "v_mfma_f32_16x16x32_bf16 v[28:31], v[88:91], v[220:223], v[28:31]\n"
    "buffer_load_dwordx4 v0, s[8:11], s69 offen offset:0 sc1 lds\n"
    "v_mfma_f32_16x16x32_bf16 v[40:43], v[92:95], v[220:223], v[40:43]\n"
    "ds_read_b128 v[88:91], v0 offset:59392\n"
    "v_mfma_f32_16x16x32_bf16 v[32:35], v[92:95], v[216:219], v[32:35]\n"
    "buffer_load_dwordx4 v0, s[8:11], s69 offen offset:1024 sc1 lds\n"
    "v_permlane16_swap_b32 v112, v114\n"
    "v_mfma_f32_16x16x32_bf16 v[36:39], v[96:99], v[216:219], v[36:39]\n"
    "ds_read_b128 v[92:95], v0 offset:60416\n"
    "v_and_b32 v7, 0x200, v0\n"
    "v_mfma_f32_16x16x32_bf16 v[44:47], v[96:99], v[220:223], v[44:47]\n"
    "v_add_u32 v7, v0, v7\n"
    "v_xor_b32 v7, s92, v7\n"
    "s_add_u32 s74, s85, 23072768\n"
    "v_mfma_f32_16x16x32_bf16 v[56:59], v[64:67], v[220:223], v[56:59]\n"
    "ds_read_b128 v[96:99], v1 offset:26624\n"
    "buffer_store_dwordx4 a[236:239], v7, s[8:11], s74 offen sc1\n"
    "v_add_f32 v112, v112, v114\n"
    "v_mfma_f32_16x16x32_bf16 v[48:51], v[64:67], v[216:219], v[48:51]\n"
    "s_add_u32 m0, s66, 102400\n"
    "s_waitcnt vmcnt(14) lgkmcnt(3)\n"
    "v_mfma_f32_16x16x32_bf16 v[16:19], v[68:71], v[224:227], v[16:19]\n"
    "ds_read_b128 v[64:67], v1 offset:27648\n"
    "s_add_u32 s69, s77, 69632\n"
    "v_mfma_f32_16x16x32_bf16 v[24:27], v[68:71], v[228:231], v[24:27]\n"
    "buffer_load_dwordx4 v0, s[8:11], s69 offen offset:2048 sc1 lds\n"
    "v_mul_f32 v108, 0xbb000000, v112\n"
    "v_mfma_f32_16x16x32_bf16 v[28:31], v[72:75], v[228:231], v[28:31]\n"
    "ds_read_b128 v[68:71], v0 offset:25600\n"
    "v_mul_f32 v116, 0xbb000000, v112\n"
    "v_mfma_f32_16x16x32_bf16 v[20:23], v[72:75], v[224:227], v[20:23]\n"
    "buffer_load_dwordx4 v0, s[8:11], s69 offen offset:3072 sc1 lds\n"
    "v_mfma_f32_16x16x32_bf16 v[32:35], v[76:79], v[224:227], v[32:35]\n"
    "ds_read_b128 v[72:75], v0 offset:61440\n"
    "s_add_u32 s74, s85, 23076864\n"
    "v_mfma_f32_16x16x32_bf16 v[40:43], v[76:79], v[228:231], v[40:43]\n"
    "buffer_store_dwordx4 a[236:239], v7, s[8:11], s74 offen sc1\n"
    "v_permlane32_swap_b32 v108, v116\n"
    "v_mfma_f32_16x16x32_bf16 v[44:47], v[80:83], v[228:231], v[44:47]\n"
    "ds_read_b128 v[76:79], v0 offset:62464\n"
    "s_add_u32 m0, s66, 131072\n"
    "v_mfma_f32_16x16x32_bf16 v[36:39], v[80:83], v[224:227], v[36:39]\n"
    "s_add_u32 s69, s77, 98304\n"
    "v_fma_f32 v109, -v108, v108, -v116\n"
    "v_mfma_f32_16x16x32_bf16 v[48:51], v[84:87], v[224:227], v[48:51]\n"
    "ds_read_b128 v[80:83], v1 offset:28672\n"
    "v_cvt_pk_bf16_f32 v124, v108, v108\n"
    "v_max_f32 v116, 0, v109\n"
    "v_mfma_f32_16x16x32_bf16 v[56:59], v[84:87], v[228:231], v[56:59]\n"
    "buffer_load_dwordx4 v0, s[8:11], s69 offen offset:0 sc1 lds\n"
    "s_waitcnt vmcnt(16) lgkmcnt(3)\n"
    "v_mfma_f32_16x16x32_bf16 v[24:27], v[88:91], v[236:239], v[24:27]\n"
    "ds_read_b128 v[84:87], v1 offset:29696\n"
    "v_lshlrev_b32 v112, 16, v124\n"
    "v_mfma_f32_16x16x32_bf16 v[16:19], v[88:91], v[232:235], v[16:19]\n"
    "buffer_load_dwordx4 v0, s[8:11], s69 offen offset:1024 sc1 lds\n"
    "v_add_f32 v116, 0x3727c5ac, v116\n"
    "v_mfma_f32_16x16x32_bf16 v[20:23], v[92:95], v[232:235], v[20:23]\n"
    "ds_read_b128 v[88:91], v0 offset:26624\n"
    "s_add_u32 s74, s85, 23080960\n"
    "v_mfma_f32_16x16x32_bf16 v[28:31], v[92:95], v[236:239], v[28:31]\n"
    "buffer_store_dwordx4 a[236:239], v7, s[8:11], s74 offen sc1\n"
    "v_sub_f32 v112, v108, v112\n"
    "v_mfma_f32_16x16x32_bf16 v[40:43], v[96:99], v[236:239], v[40:43]\n"
    "ds_read_b128 v[92:95], v0 offset:63488\n"
    "s_add_u32 m0, s66, 131072\n"
    "v_mfma_f32_16x16x32_bf16 v[32:35], v[96:99], v[232:235], v[32:35]\n"
    "s_add_u32 s69, s77, 98304\n"
    "v_fmac_f32 v116, 0, v109\n"
    "v_mfma_f32_16x16x32_bf16 v[36:39], v[64:67], v[232:235], v[36:39]\n"
    "ds_read_b128 v[96:99], v0 offset:64512\n"
    "v_cvt_pk_bf16_f32 v125, v108, v112\n"
    "v_mov_b32 v126, v124\n"
    "v_mfma_f32_16x16x32_bf16 v[44:47], v[64:67], v[236:239], v[44:47]\n"
    "buffer_load_dwordx4 v0, s[8:11], s69 offen offset:2048 sc1 lds\n"
    "v_mfma_f32_16x16x32_bf16 v[56:59], v[68:71], v[236:239], v[56:59]\n"
    "ds_read_b128 v[64:67], v1 offset:30720\n"
    "v_mfma_f32_16x16x32_bf16 v[48:51], v[68:71], v[232:235], v[48:51]\n"
    "v_rsq_f32 v118, v116\n"
    "s_waitcnt vmcnt(17) lgkmcnt(3)\n"
    "v_mfma_f32_16x16x32_bf16 v[16:19], v[72:75], v[240:243], v[16:19]\n"
    "ds_read_b128 v[68:71], v1 offset:31744\n"
    "buffer_load_dwordx4 v0, s[8:11], s69 offen offset:3072 sc1 lds\n"
    "v_mfma_f32_16x16x32_bf16 v[24:27], v[72:75], v[244:247], v[24:27]\n"
    "s_add_u32 s74, s85, 23085056\n"
    "v_mov_b32 v127, v125\n"
    "v_mov_b32 v117, v116\n"
    "v_mfma_f32_16x16x32_bf16 v[28:31], v[76:79], v[244:247], v[28:31]\n"
    "ds_read_b128 v[72:75], v0 offset:27648\n"
    "v_mfma_f32_16x16x32_bf16 v[20:23], v[76:79], v[240:243], v[20:23]\n"
    "buffer_store_dwordx4 a[236:239], v7, s[8:11], s74 offen sc1\n"
    "v_mfma_f32_16x16x32_bf16 v[32:35], v[80:83], v[240:243], v[32:35]\n"
    "s_add_u32 m0, s66, 135168\n"
    "s_add_u32 s69, s77, 102400\n"
    "v_permlane16_swap_b32 v124, v126\n"
    "v_mfma_f32_16x16x32_bf16 v[40:43], v[80:83], v[244:247], v[40:43]\n"
    "buffer_load_dwordx4 v0, s[8:11], s69 offen offset:0 sc1 lds\n"
    "v_mfma_f32_16x16x32_bf16 v[44:47], v[84:87], v[244:247], v[44:47]\n"
    "v_permlane16_swap_b32 v125, v127\n"
    "v_mfma_f32_16x16x32_bf16 v[36:39], v[84:87], v[240:243], v[36:39]\n"
    "buffer_load_dwordx4 v0, s[8:11], s69 offen offset:1024 sc1 lds\n"
    "v_mul_f32 v120, 0xc0135761, v118\n"
    "v_mfma_f32_16x16x32_bf16 v[48:51], v[88:91], v[240:243], v[48:51]\n"
    "v_mul_f32 v109, v118, v118\n"
    "v_mfma_f32_16x16x32_bf16 v[56:59], v[88:91], v[244:247], v[56:59]\n"
    "buffer_load_dwordx4 v0, s[8:11], s69 offen offset:2048 sc1 lds\n"
    "v_mfma_f32_16x16x16_bf16 v[16:19], a[188:189], v[124:125], v[16:19]\n"
    "v_mfma_f32_16x16x16_bf16 v[24:27], a[188:189], v[126:127], v[24:27]\n"
    "v_mfma_f32_16x16x16_bf16 v[20:23], a[190:191], v[124:125], v[20:23]\n"
    "v_mfma_f32_16x16x16_bf16 v[28:31], a[190:191], v[126:127], v[28:31]\n"
    "v_mfma_f32_16x16x16_bf16 v[32:35], a[192:193], v[124:125], v[32:35]\n"
    "v_mfma_f32_16x16x16_bf16 v[40:43], a[192:193], v[126:127], v[40:43]\n"
    "v_mfma_f32_16x16x16_bf16 v[36:39], a[194:195], v[124:125], v[36:39]\n"
    "v_mfma_f32_16x16x16_bf16 v[44:47], a[194:195], v[126:127], v[44:47]\n"
    "v_mfma_f32_16x16x16_bf16 v[48:51], a[208:209], v[124:125], v[48:51]\n"
    "v_mfma_f32_16x16x16_bf16 v[56:59], a[208:209], v[126:127], v[56:59]\n"
    "s_waitcnt vmcnt(20) lgkmcnt(0)\n"
    "v_mfma_f32_16x16x32_bf16 v[24:27], v[92:95], v[252:255], v[24:27]\n"
    "v_permlane16_swap_b32 v116, v117\n"
    "v_mul_f32 v109, v109, v120\n"
    "v_mfma_f32_16x16x32_bf16 v[16:19], v[92:95], v[248:251], v[16:19]\n"
    "v_mov_b32 v121, v120\n"
    "v_mul_f32 v122, 0x3d372713, v109\n"
    "v_mfma_f32_16x16x32_bf16 v[20:23], v[96:99], v[248:251], v[20:23]\n"
    "buffer_load_dwordx4 v0, s[8:11], s69 offen offset:3072 sc1 lds\n"
    "v_mul_f32 v123, 0x3d372713, v109\n"
    "v_mfma_f32_16x16x32_bf16 v[28:31], v[96:99], v[252:255], v[28:31]\n"
    "s_add_u32 s74, s85, 23089152\n"
    "v_mfma_f32_16x16x32_bf16 v[40:43], v[64:67], v[252:255], v[40:43]\n"
    "v_permlane16_swap_b32 v120, v121\n"
    "v_mfma_f32_16x16x32_bf16 v[32:35], v[64:67], v[248:251], v[32:35]\n"
    "buffer_store_dwordx4 a[236:239], v7, s[8:11], s74 offen sc1\n"
    "v_mfma_f32_16x16x32_bf16 v[36:39], v[68:71], v[248:251], v[36:39]\n"
    "v_permlane16_swap_b32 v122, v123\n"
    "v_mfma_f32_16x16x32_bf16 v[44:47], v[68:71], v[252:255], v[44:47]\n"
    "v_mfma_f32_16x16x32_bf16 v[56:59], v[72:75], v[252:255], v[56:59]\n"
    "v_mfma_f32_16x16x32_bf16 v[48:51], v[72:75], v[248:251], v[48:51]\n"
    "v_cmp_u_f32 s[30:31], v16, v24\n"
    "v_cmp_u_f32 s[24:25], v116, v117\n"
    "v_mfma_f32_16x16x16_bf16 v[64:67], a[196:197], v[124:125], 0\n"
    "v_mfma_f32_16x16x16_bf16 v[72:75], a[196:197], v[126:127], 0\n"
    "v_mfma_f32_16x16x16_bf16 v[68:71], a[198:199], v[124:125], 0\n"
    "v_mfma_f32_16x16x16_bf16 v[76:79], a[198:199], v[126:127], 0\n"
    "s_or_b64 s[30:31], s[30:31], s[24:25]\n"
    "s_cbranch_scc1 Lbad21\n"
    "Lok21:\n"
    "s_waitcnt vmcnt(12)\n"
    "s_barrier\n"
    "ds_read_b128 v[80:83], v1 offset:32768\n"
    "ds_read_b128 v[84:87], v1 offset:34816\n"
    "ds_read_b128 v[88:91], v1 offset:36864\n"
    "ds_read_b128 v[92:95], v1 offset:38912\n"
    "ds_read_b128 v[96:99], v1 offset:40960\n"
    "ds_read_b128 v[112:115], v1 offset:43008\n"
    "v_permlane32_swap_b32 v48, v56\n"
    "v_permlane32_swap_b32 v49, v57\n"
    "v_permlane32_swap_b32 v50, v58\n"
    "v_permlane32_swap_b32 v51, v59\n"
    "s_mov_b32 s30, 0\n"
    "s_mov_b32 s31, -1\n"
    "v_cndmask_b32 v52, v120, v121, s[30:31]\n"
    "v_cndmask_b32 v53, v122, v123, s[30:31]\n"
    "v_cndmask_b32 v54, v116, v117, s[30:31]\n"
    "v_mul_f32 v100, v48, v48\n"
    "v_mul_f32 v101, v49, v49\n"
    "v_mul_f32 v102, v50, v50\n"
    "v_mul_f32 v103, v51, v51\n"
    "v_fma_f32 v100, v100, v53, v52\n"
    "v_fma_f32 v101, v101, v53, v52\n"
    "v_fma_f32 v102, v102, v53, v52\n"
    "s_waitcnt lgkmcnt(3)\n"
    "v_mfma_f32_16x16x32_bf16 v[64:67], v[80:83], v[128:131], v[64:67]\n"
    "ds_read_b128 v[10:13], v1 offset:45056\n"
    "s_add_u32 m0, s66, 32768\n"
    "v_fma_f32 v103, v103, v53, v52\n"
    "v_mfma_f32_16x16x32_bf16 v[72:75], v[80:83], v[132:135], v[72:75]\n"
    "s_add_u32 s69, s77, 131072\n"
    "v_mul_f32 v56, v48, v56\n"
    "v_mul_f32 v48, v100, v48\n"
    "v_mfma_f32_16x16x32_bf16 v[72:75], v[84:87], v[140:143], v[72:75]\n"
    "ds_read_b128 v[80:83], v1 offset:47104\n"
    "v_mul_f32 v57, v49, v57\n"
    "v_mul_f32 v49, v101, v49\n"
    "v_mfma_f32_16x16x32_bf16 v[64:67], v[84:87], v[136:139], v[64:67]\n"
    "buffer_load_dwordx4 v0, s[8:11], s69 offen offset:0 sc1 lds\n"
    "v_mul_f32 v58, v50, v58\n"
    "v_mfma_f32_16x16x32_bf16 v[64:67], v[88:91], v[144:147], v[64:67]\n"
    "ds_read_b128 v[84:87], v1 offset:49152\n"
    "v_mul_f32 v50, v102, v50\n"
    "v_mul_f32 v59, v51, v59\n"
    "v_mfma_f32_16x16x32_bf16 v[72:75], v[88:91], v[148:151], v[72:75]\n"
    "v_mul_f32 v51, v103, v51\n"
    "s_waitcnt lgkmcnt(3)\n"
    "v_mfma_f32_16x16x32_bf16 v[72:75], v[92:95], v[156:159], v[72:75]\n"
    "ds_read_b128 v[88:91], v1 offset:51200\n"
    "v_exp_f32 v48, v48\n"
    "v_mfma_f32_16x16x32_bf16 v[64:67], v[92:95], v[152:155], v[64:67]\n"
    "buffer_load_dwordx4 v0, s[8:11], s69 offen offset:1024 sc1 lds\n"
    "v_exp_f32 v49, v49\n"
    "v_mfma_f32_16x16x32_bf16 v[64:67], v[96:99], v[160:163], v[64:67]\n"
    "ds_read_b128 v[92:95], v1 offset:53248\n"
    "v_exp_f32 v50, v50\n"
    "v_mfma_f32_16x16x32_bf16 v[72:75], v[96:99], v[164:167], v[72:75]\n"
    "v_exp_f32 v51, v51\n"
    "v_fma_f32 v48, v48, v54, v54\n"
    "v_fma_f32 v49, v49, v54, v54\n"
    "v_mfma_f32_16x16x32_bf16 v[72:75], v[112:115], v[172:175], v[72:75]\n"
    "ds_read_b128 v[96:99], v1 offset:55296\n"
    "v_fma_f32 v50, v50, v54, v54\n"
    "v_fma_f32 v51, v51, v54, v54\n"
    "v_mfma_f32_16x16x32_bf16 v[64:67], v[112:115], v[168:171], v[64:67]\n"
    "v_rcp_f32 v48, v48\n"
    "s_waitcnt lgkmcnt(3)\n"
    "v_mfma_f32_16x16x32_bf16 v[64:67], v[10:13], v[176:179], v[64:67]\n"
    "ds_read_b128 v[112:115], v1 offset:57344\n"
    "v_rcp_f32 v49, v49\n"
    "v_mfma_f32_16x16x32_bf16 v[72:75], v[10:13], v[180:183], v[72:75]\n"
    "buffer_load_dwordx4 v0, s[8:11], s69 offen offset:2048 sc1 lds\n"
    "v_mfma_f32_16x16x32_bf16 v[72:75], v[80:83], v[188:191], v[72:75]\n"
    "ds_read_b128 v[10:13], v1 offset:59392\n"
    "v_rcp_f32 v50, v50\n"
    "v_mfma_f32_16x16x32_bf16 v[64:67], v[80:83], v[184:187], v[64:67]\n"
    "buffer_load_dwordx4 v0, s[8:11], s69 offen offset:3072 sc1 lds\n"
    "v_rcp_f32 v51, v51\n"
    "v_mul_f32 v56, v56, v48\n"
    "v_mfma_f32_16x16x32_bf16 v[64:67], v[84:87], v[192:195], v[64:67]\n"
    "ds_read_b128 v[80:83], v1 offset:61440\n"
    "v_mul_f32 v57, v57, v49\n"
    "v_mul_f32 v58, v58, v50\n"
    "v_mfma_f32_16x16x32_bf16 v[72:75], v[84:87], v[196:199], v[72:75]\n"
    "s_add_u32 m0, s66, 36864\n"
    "v_mul_f32 v59, v59, v51\n"
    "s_waitcnt lgkmcnt(3)\n"
    "v_mfma_f32_16x16x32_bf16 v[72:75], v[88:91], v[204:207], v[72:75]\n"
    "ds_read_b128 v[84:87], v1 offset:63488\n"
    "s_add_u32 s69, s77, 135168\n"
    "v_cvt_pk_bf16_f32 v108, v56, v57\n"
    "v_mfma_f32_16x16x32_bf16 v[64:67], v[88:91], v[200:203], v[64:67]\n"
    "v_cvt_pk_bf16_f32 v109, v58, v59\n"
    "v_cvt_pk_bf16_f32 v110, v56, v57\n"
    "v_cvt_pk_bf16_f32 v111, v58, v59\n"
    "v_mfma_f32_16x16x32_bf16 v[64:67], v[92:95], v[208:211], v[64:67]\n"
    "ds_read_b128 v[88:91], v1 offset:33792\n"
    "s_add_u32 s73, s86, 23072768\n"
    "v_mfma_f32_16x16x32_bf16 v[72:75], v[92:95], v[212:215], v[72:75]\n"
    "buffer_load_dwordx4 v0, s[8:11], s69 offen offset:0 sc1 lds\n"
    "v_permlane16_swap_b32 v108, v110\n"
    "v_mfma_f32_16x16x32_bf16 v[72:75], v[96:99], v[220:223], v[72:75]\n"
    "ds_read_b128 v[92:95], v1 offset:35840\n"
    "v_permlane16_swap_b32 v109, v111\n"
    "v_mfma_f32_16x16x32_bf16 v[64:67], v[96:99], v[216:219], v[64:67]\n"
    "buffer_store_dwordx4 v[108:111], v14, s[8:11], s73 offen sc1\n"
    "s_waitcnt lgkmcnt(3)\n"
    "v_mfma_f32_16x16x32_bf16 v[64:67], v[112:115], v[224:227], v[64:67]\n"
    "ds_read_b128 v[96:99], v1 offset:37888\n"
    "v_mul_f32 v100, v16, v16\n"
    "v_mul_f32 v101, v17, v17\n"
    "v_mfma_f32_16x16x32_bf16 v[72:75], v[112:115], v[228:231], v[72:75]\n"
    "buffer_load_dwordx4 v0, s[8:11], s69 offen offset:1024 sc1 lds\n"
    "v_mul_f32 v102, v18, v18\n"
    "v_mfma_f32_16x16x32_bf16 v[72:75], v[10:13], v[236:239], v[72:75]\n"
    "ds_read_b128 v[112:115], v1 offset:39936\n"
    "s_add_u32 s70, s79, 16384\n"
    "v_mul_f32 v103, v19, v19\n"
    "v_mfma_f32_16x16x32_bf16 v[64:67], v[10:13], v[232:235], v[64:67]\n"
    "v_fma_f32 v100, v100, v122, v120\n"
    "v_fma_f32 v101, v101, v122, v120\n"
    "v_fma_f32 v102, v102, v122, v120\n"
    "v_mfma_f32_16x16x32_bf16 v[64:67], v[80:83], v[240:243], v[64:67]\n"
    "ds_read_b128 v[10:13], v1 offset:41984\n"
    "v_fma_f32 v103, v103, v122, v120\n"
    "v_mul_f32 v20, v16, v20\n"
    "v_mfma_f32_16x16x32_bf16 v[72:75], v[80:83], v[244:247], v[72:75]\n"
    "v_mul_f32 v16, v100, v16\n"
    "v_mul_f32 v21, v17, v21\n"
    "s_waitcnt lgkmcnt(3)\n"
    "v_mfma_f32_16x16x32_bf16 v[72:75], v[84:87], v[252:255], v[72:75]\n"
    "ds_read_b128 v[80:83], v1 offset:44032\n"
    "v_mul_f32 v17, v101, v17\n"
    "v_mul_f32 v22, v18, v22\n"
    "v_mfma_f32_16x16x32_bf16 v[64:67], v[84:87], v[248:251], v[64:67]\n"
    "buffer_load_dwordx4 a[64:67], v0, s[8:11], s70 offen offset:0 sc1\n"
    "v_mul_f32 v18, v102, v18\n"
    "v_mul_f32 v23, v19, v23\n"
    "v_mfma_f32_16x16x32_bf16 v[68:71], v[88:91], v[128:131], v[68:71]\n"
    "ds_read_b128 v[84:87], v1 offset:46080\n"
    "v_mul_f32 v19, v103, v19\n"
    "v_mfma_f32_16x16x32_bf16 v[76:79], v[88:91], v[132:135], v[76:79]\n"
    "v_exp_f32 v16, v16\n"
    "v_exp_f32 v17, v17\n"
    "v_mfma_f32_16x16x32_bf16 v[76:79], v[92:95], v[140:143], v[76:79]\n"
    "ds_read_b128 v[88:91], v1 offset:48128\n"
    "v_exp_f32 v18, v18\n"
    "v_mfma_f32_16x16x32_bf16 v[68:71], v[92:95], v[136:139], v[68:71]\n"
    "v_exp_f32 v19, v19\n"
    "s_waitcnt lgkmcnt(3)\n"
    "v_mfma_f32_16x16x32_bf16 v[68:71], v[96:99], v[144:147], v[68:71]\n"
    "ds_read_b128 v[92:95], v1 offset:50176\n"
    "buffer_load_dwordx4 a[68:71], v0, s[8:11], s70 offen offset:1024 sc1\n"
    "v_fma_f32 v16, v16, v116, v116\n"
    "v_mfma_f32_16x16x32_bf16 v[76:79], v[96:99], v[148:151], v[76:79]\n"
    "v_fma_f32 v17, v17, v116, v116\n"
    "v_fma_f32 v18, v18, v116, v116\n"
    "v_fma_f32 v19, v19, v116, v116\n"
    "v_mfma_f32_16x16x32_bf16 v[76:79], v[112:115], v[156:159], v[76:79]\n"
    "ds_read_b128 v[96:99], v1 offset:52224\n"
    "v_rcp_f32 v16, v16\n"
    "v_mfma_f32_16x16x32_bf16 v[68:71], v[112:115], v[152:155], v[68:71]\n"
    "buffer_load_dwordx4 a[72:75], v0, s[8:11], s70 offen offset:2048 sc1\n"
    "v_rcp_f32 v17, v17\n"
    "v_mfma_f32_16x16x32_bf16 v[68:71], v[10:13], v[160:163], v[68:71]\n"
    "ds_read_b128 v[112:115], v1 offset:54272\n"
    "v_rcp_f32 v18, v18\n"
    "v_mfma_f32_16x16x32_bf16 v[76:79], v[10:13], v[164:167], v[76:79]\n"
    "v_rcp_f32 v19, v19\n"
    "s_waitcnt lgkmcnt(3)\n"
    "v_mfma_f32_16x16x32_bf16 v[76:79], v[80:83], v[172:175], v[76:79]\n"
    "ds_read_b128 v[10:13], v1 offset:56320\n"
    "v_mul_f32 v20, v20, v16\n"
    "v_mul_f32 v21, v21, v17\n"
    "v_mfma_f32_16x16x32_bf16 v[68:71], v[80:83], v[168:171], v[68:71]\n"
    "buffer_load_dwordx4 a[76:79], v0, s[8:11], s70 offen offset:3072 sc1\n"
    "v_mul_f32 v22, v22, v18\n"
    "v_mfma_f32_16x16x32_bf16 v[68:71], v[84:87], v[176:179], v[68:71]\n"
    "ds_read_b128 v[80:83], v1 offset:58368\n"
    "s_add_u32 s70, s79, 20480\n"
    "v_mul_f32 v23, v23, v19\n"
    "v_mfma_f32_16x16x32_bf16 v[76:79], v[84:87], v[180:183], v[76:79]\n"
    "v_cvt_pk_bf16_f32 v104, v20, v21\n"
    "v_cvt_pk_bf16_f32 v105, v22, v23\n"
    "v_mul_f32 v100, v24, v24\n"
    "v_mfma_f32_16x16x32_bf16 v[76:79], v[88:91], v[188:191], v[76:79]\n"
    "ds_read_b128 v[84:87], v1 offset:60416\n"
    "v_mul_f32 v101, v25, v25\n"
    "v_mul_f32 v102, v26, v26\n"
    "v_mfma_f32_16x16x32_bf16 v[68:71], v[88:91], v[184:187], v[68:71]\n"
    "v_mul_f32 v103, v27, v27\n"
    "v_fma_f32 v100, v100, v123, v121\n"
    "s_waitcnt vmcnt(11)\n"
    "s_barrier\n"
    "s_waitcnt lgkmcnt(3)\n"
    "v_mfma_f32_16x16x32_bf16 v[68:71], v[92:95], v[192:195], v[68:71]\n"
    "ds_read_b128 v[88:91], v1 offset:62464\n"
    "v_fma_f32 v101, v101, v123, v121\n"
    "v_fma_f32 v102, v102, v123, v121\n"
    "v_mfma_f32_16x16x32_bf16 v[76:79], v[92:95], v[196:199], v[76:79]\n"
    "buffer_load_dwordx4 a[80:83], v0, s[8:11], s70 offen offset:0 sc1\n"
    "v_fma_f32 v103, v103, v123, v121\n"
    "v_mfma_f32_16x16x32_bf16 v[76:79], v[96:99], v[204:207], v[76:79]\n"
    "ds_read_b128 v[92:95], v1 offset:64512\n"
    "v_mul_f32 v28, v24, v28\n"
    "v_mul_f32 v24, v100, v24\n"
    "v_mfma_f32_16x16x32_bf16 v[68:71], v[96:99], v[200:203], v[68:71]\n"
    "v_mul_f32 v29, v25, v29\n"
    "v_mul_f32 v25, v101, v25\n"
    "v_mul_f32 v30, v26, v30\n"
    "v_mfma_f32_16x16x32_bf16 v[68:71], v[112:115], v[208:211], v[68:71]\n"
    "ds_read_b128 v[96:99], v2 offset:0\n"
    "v_mul_f32 v26, v102, v26\n"
    "v_mul_f32 v31, v27, v31\n"
    "v_mfma_f32_16x16x32_bf16 v[76:79], v[112:115], v[212:215], v[76:79]\n"
    "v_mul_f32 v27, v103, v27\n"
    "s_waitcnt lgkmcnt(3)\n"
    "v_mfma_f32_16x16x32_bf16 v[76:79], v[10:13], v[220:223], v[76:79]\n"
    "ds_read_b128 v[112:115], v2 offset:2048\n"
    "buffer_load_dwordx4 a[84:87], v0, s[8:11], s70 offen offset:1024 sc1\n"
    "v_exp_f32 v24, v24\n"
    "v_mfma_f32_16x16x32_bf16 v[68:71], v[10:13], v[216:219], v[68:71]\n"
    "v_exp_f32 v25, v25\n"
    "v_exp_f32 v26, v26\n"
    "v_mfma_f32_16x16x32_bf16 v[68:71], v[80:83], v[224:227], v[68:71]\n"
    "ds_read_b128 v[10:13], v2 offset:4096\n"
    "v_exp_f32 v27, v27\n"
    "v_mfma_f32_16x16x32_bf16 v[76:79], v[80:83], v[228:231], v[76:79]\n"
    "buffer_load_dwordx4 a[88:91], v0, s[8:11], s70 offen offset:2048 sc1\n"
    "v_fma_f32 v24, v24, v117, v117\n"
    "v_mfma_f32_16x16x32_bf16 v[76:79], v[84:87], v[236:239], v[76:79]\n"
    "ds_read_b128 v[80:83], v2 offset:6144\n"
    "v_fma_f32 v25, v25, v117, v117\n"
    "v_fma_f32 v26, v26, v117, v117\n"
    "v_mfma_f32_16x16x32_bf16 v[68:71], v[84:87], v[232:235], v[68:71]\n"
    "v_fma_f32 v27, v27, v117, v117\n"
    "s_waitcnt lgkmcnt(3)\n"
    "v_mfma_f32_16x16x32_bf16 v[68:71], v[88:91], v[240:243], v[68:71]\n"
    "ds_read_b128 v[84:87], v2 offset:8192\n"
    "v_rcp_f32 v24, v24\n"
    "v_mfma_f32_16x16x32_bf16 v[76:79], v[88:91], v[244:247], v[76:79]\n"
    "buffer_load_dwordx4 a[92:95], v0, s[8:11], s70 offen offset:3072 sc1\n"
    "v_rcp_f32 v25, v25\n"
    "v_mfma_f32_16x16x32_bf16 v[76:79], v[92:95], v[252:255], v[76:79]\n"
    "ds_read_b128 v[88:91], v2 offset:10240\n"
    "v_rcp_f32 v26, v26\n"
    "v_mfma_f32_16x16x32_bf16 v[68:71], v[92:95], v[248:251], v[68:71]\n"
    "v_rcp_f32 v27, v27\n"
    "v_mul_f32 v28, v28, v24\n"
    "v_mul_f32 v29, v29, v25\n"
    "v_mfma_f32_16x16x16_bf16 v[48:51], a[200:201], v[124:125], 0\n"
    "v_mul_f32 v30, v30, v26\n"
    "v_mul_f32 v31, v31, v27\n"
    "v_cvt_pk_bf16_f32 v106, v28, v29\n"
    "v_mfma_f32_16x16x16_bf16 v[56:59], a[200:201], v[126:127], 0\n"
    "v_cvt_pk_bf16_f32 v107, v30, v31\n"
    "s_add_u32 s73, s85, 23072768\n"
    "v_mfma_f32_16x16x16_bf16 v[52:55], a[202:203], v[124:125], 0\n"
    "v_permlane32_swap_b32 v104, v106\n"
    "v_permlane32_swap_b32 v105, v107\n"
    "v_mfma_f32_16x16x16_bf16 v[60:63], a[202:203], v[126:127], 0\n"
    "s_add_u32 m0, s66, 36864\n"
    "s_add_u32 s69, s77, 135168\n"
    "s_mov_b32 s65, 102400\n"
    "v_mfma_f32_16x16x32_bf16 v[48:51], v[96:99], v[128:131], v[48:51]\n"
    "ds_read_b128 v[92:95], v2 offset:12288\n"
    "v_mfma_f32_16x16x32_bf16 v[56:59], v[96:99], v[132:135], v[56:59]\n"
    "buffer_atomic_add a255, v3, s[8:11], s65 offen\n"
    "s_waitcnt lgkmcnt(3)\n"
    "v_mfma_f32_16x16x32_bf16 v[56:59], v[112:115], v[140:143], v[56:59]\n"
    "ds_read_b128 v[96:99], v2 offset:14336\n"
    "v_mfma_f32_16x16x32_bf16 v[48:51], v[112:115], v[136:139], v[48:51]\n"
    "buffer_load_dwordx4 v0, s[8:11], s69 offen offset:2048 sc1 lds\n"
    "buffer_store_dwordx4 v[104:107], v15, s[8:11], s73 offen sc1\n"
    "v_mfma_f32_16x16x32_bf16 v[48:51], v[10:13], v[144:147], v[48:51]\n"
    "ds_read_b128 v[112:115], v2 offset:16384\n"
    "v_mul_f32 v100, v32, v32\n"
    "v_mul_f32 v101, v33, v33\n"
    "v_mfma_f32_16x16x32_bf16 v[56:59], v[10:13], v[148:151], v[56:59]\n"
    "v_mul_f32 v102, v34, v34\n"
    "v_mul_f32 v103, v35, v35\n"
    "v_fma_f32 v100, v100, v122, v120\n"
    "v_mfma_f32_16x16x32_bf16 v[56:59], v[80:83], v[156:159], v[56:59]\n"
    "ds_read_b128 v[10:13], v2 offset:18432\n"
    "v_fma_f32 v101, v101, v122, v120\n"
    "v_fma_f32 v102, v102, v122, v120\n"
    "v_mfma_f32_16x16x32_bf16 v[48:51], v[80:83], v[152:155], v[48:51]\n"
    "v_fma_f32 v103, v103, v122, v120\n"
    "v_mul_f32 v36, v32, v36\n"
    "s_waitcnt lgkmcnt(3)\n"
    "v_mfma_f32_16x16x32_bf16 v[48:51], v[84:87], v[160:163], v[48:51]\n"
    "ds_read_b128 v[80:83], v2 offset:20480\n"
    "buffer_load_dwordx4 v0, s[8:11], s69 offen offset:3072 sc1 lds\n"
    "v_mul_f32 v32, v100, v32\n"
    "v_mfma_f32_16x16x32_bf16 v[56:59], v[84:87], v[164:167], v[56:59]\n"
    "s_add_u32 s70, s79, 24576\n"
    "v_mul_f32 v37, v33, v37\n"
    "v_mul_f32 v33, v101, v33\n"
    "v_mfma_f32_16x16x32_bf16 v[56:59], v[88:91], v[172:175], v[56:59]\n"
    "ds_read_b128 v[84:87], v2 offset:22528\n"
    "v_mul_f32 v38, v34, v38\n"
    "v_mul_f32 v34, v102, v34\n"
    "v_mfma_f32_16x16x32_bf16 v[48:51], v[88:91], v[168:171], v[48:51]\n"
    "buffer_load_dwordx4 a[96:99], v0, s[8:11], s70 offen offset:0 sc1\n"
    "v_mul_f32 v39, v35, v39\n"
    "v_mul_f32 v35, v103, v35\n"
    "v_mfma_f32_16x16x32_bf16 v[48:51], v[92:95], v[176:179], v[48:51]\n"
    "ds_read_b128 v[88:91], v2 offset:24576\n"
    "v_exp_f32 v32, v32\n"
    "v_mfma_f32_16x16x32_bf16 v[56:59], v[92:95], v[180:183], v[56:59]\n"
    "v_exp_f32 v33, v33\n"
    "s_waitcnt lgkmcnt(3)\n"
    "v_mfma_f32_16x16x32_bf16 v[56:59], v[96:99], v[188:191], v[56:59]\n"
    "ds_read_b128 v[92:95], v2 offset:26624\n"
    "v_exp_f32 v34, v34\n"
    "v_mfma_f32_16x16x32_bf16 v[48:51], v[96:99], v[184:187], v[48:51]\n"
    "v_exp_f32 v35, v35\n"
    "v_fma_f32 v32, v32, v116, v116\n"
    "v_mfma_f32_16x16x32_bf16 v[48:51], v[112:115], v[192:195], v[48:51]\n"
    "ds_read_b128 v[96:99], v2 offset:28672\n"
    "v_fma_f32 v33, v33, v116, v116\n"
    "v_fma_f32 v34, v34, v116, v116\n"
    "v_mfma_f32_16x16x32_bf16 v[56:59], v[112:115], v[196:199], v[56:59]\n"
    "buffer_load_dwordx4 a[100:103], v0, s[8:11], s70 offen offset:1024 sc1\n"
    "v_fma_f32 v35, v35, v116, v116\n"
    "v_mfma_f32_16x16x32_bf16 v[56:59], v[10:13], v[204:207], v[56:59]\n"
    "ds_read_b128 v[112:115], v2 offset:30720\n"
    "v_rcp_f32 v32, v32\n"
    "v_mfma_f32_16x16x32_bf16 v[48:51], v[10:13], v[200:203], v[48:51]\n"
    "v_rcp_f32 v33, v33\n"
    "s_waitcnt lgkmcnt(3)\n"
    "v_mfma_f32_16x16x32_bf16 v[48:51], v[80:83], v[208:211], v[48:51]\n"
    "ds_read_b128 v[10:13], v2 offset:1024\n"
    "v_rcp_f32 v34, v34\n"
    "v_mfma_f32_16x16x32_bf16 v[56:59], v[80:83], v[212:215], v[56:59]\n"
    "buffer_load_dwordx4 a[104:107], v0, s[8:11], s70 offen offset:2048 sc1\n"
    "v_rcp_f32 v35, v35\n"
    "v_mfma_f32_16x16x32_bf16 v[56:59], v[84:87], v[220:223], v[56:59]\n"
    "ds_read_b128 v[80:83], v2 offset:3072\n"
    "v_mul_f32 v36, v36, v32\n"
    "v_mul_f32 v37, v37, v33\n"
    "v_mfma_f32_16x16x32_bf16 v[48:51], v[84:87], v[216:219], v[48:51]\n"
    "v_mul_f32 v38, v38, v34\n"
    "v_mul_f32 v39, v39, v35\n"
    "v_cvt_pk_bf16_f32 v108, v36, v37\n"
    "v_mfma_f32_16x16x32_bf16 v[48:51], v[88:91], v[224:227], v[48:51]\n"
    "ds_read_b128 v[84:87], v2 offset:5120\n"
    "v_cvt_pk_bf16_f32 v109, v38, v39\n"
    "v_mul_f32 v100, v40, v40\n"
    "v_mfma_f32_16x16x32_bf16 v[56:59], v[88:91], v[228:231], v[56:59]\n"
    "v_mul_f32 v101, v41, v41\n"
    "v_mul_f32 v102, v42, v42\n"
    "s_waitcnt lgkmcnt(3)\n"
    "v_mfma_f32_16x16x32_bf16 v[56:59], v[92:95], v[236:239], v[56:59]\n"
    "ds_read_b128 v[88:91], v2 offset:7168\n"
    "v_mul_f32 v103, v43, v43\n"
    "v_fma_f32 v100, v100, v123, v121\n"
    "v_mfma_f32_16x16x32_bf16 v[48:51], v[92:95], v[232:235], v[48:51]\n"
    "buffer_load_dwordx4 a[108:111], v0, s[8:11], s70 offen offset:3072 sc1\n"
    "v_fma_f32 v101, v101, v123, v121\n"
    "v_fma_f32 v102, v102, v123, v121\n"
    "v_mfma_f32_16x16x32_bf16 v[48:51], v[96:99], v[240:243], v[48:51]\n"
    "ds_read_b128 v[92:95], v2 offset:9216\n"
    "s_add_u32 s70, s79, 28672\n"
    "v_fma_f32 v103, v103, v123, v121\n"
    "v_mfma_f32_16x16x32_bf16 v[56:59], v[96:99], v[244:247], v[56:59]\n"
    "v_mul_f32 v44, v40, v44\n"
    "v_mul_f32 v40, v100, v40\n"
    "v_mul_f32 v45, v41, v45\n"
    "v_mfma_f32_16x16x32_bf16 v[56:59], v[112:115], v[252:255], v[56:59]\n"
    "ds_read_b128 v[96:99], v2 offset:11264\n"
    "v_mul_f32 v41, v101, v41\n"
    "v_mul_f32 v46, v42, v46\n"
    "v_mfma_f32_16x16x32_bf16 v[48:51], v[112:115], v[248:251], v[48:51]\n"
    "v_mul_f32 v42, v102, v42\n"
    "v_mul_f32 v47, v43, v47\n"
    "s_waitcnt lgkmcnt(3)\n"
    "v_mfma_f32_16x16x32_bf16 v[52:55], v[10:13], v[128:131], v[52:55]\n"
    "ds_read_b128 v[112:115], v2 offset:13312\n"
    "v_mul_f32 v43, v103, v43\n"
    "v_mfma_f32_16x16x32_bf16 v[60:63], v[10:13], v[132:135], v[60:63]\n"
    "buffer_load_dwordx4 a[112:115], v0, s[8:11], s70 offen offset:0 sc1\n"
    "v_exp_f32 v40, v40\n"
    "v_mfma_f32_16x16x32_bf16 v[60:63], v[80:83], v[140:143], v[60:63]\n"
    "ds_read_b128 v[10:13], v2 offset:15360\n"
    "v_exp_f32 v41, v41\n"
    "v_mfma_f32_16x16x32_bf16 v[52:55], v[80:83], v[136:139], v[52:55]\n"
    "v_exp_f32 v42, v42\n"
    "v_exp_f32 v43, v43\n"
    "v_mfma_f32_16x16x32_bf16 v[52:55], v[84:87], v[144:147], v[52:55]\n"
    "ds_read_b128 v[80:83], v2 offset:17408\n"
    "v_fma_f32 v40, v40, v117, v117\n"
    "v_fma_f32 v41, v41, v117, v117\n"
    "v_mfma_f32_16x16x32_bf16 v[60:63], v[84:87], v[148:151], v[60:63]\n"
    "v_fma_f32 v42, v42, v117, v117\n"
    "v_fma_f32 v43, v43, v117, v117\n"
    "s_waitcnt lgkmcnt(3)\n"
    "v_mfma_f32_16x16x32_bf16 v[60:63], v[88:91], v[156:159], v[60:63]\n"
    "ds_read_b128 v[84:87], v2 offset:19456\n"
    "buffer_load_dwordx4 a[116:119], v0, s[8:11], s70 offen offset:1024 sc1\n"
    "s_add_u32 s68, s67, 98304\n"
    "Lpad5:\n"
    "s_cbranch_execz Lpad9\n"
    "v_mov_b32 v8, 0\n"
    "buffer_atomic_add v8, v6, s[8:11], s68 offen sc0\n"
    "v_mfma_f32_16x16x32_bf16 v[52:55], v[88:91], v[152:155], v[52:55]\n"
    "v_rcp_f32 v40, v40\n"
    "v_rcp_f32 v41, v41\n"
    "v_mfma_f32_16x16x32_bf16 v[52:55], v[92:95], v[160:163], v[52:55]\n"
    "ds_read_b128 v[88:91], v2 offset:21504\n"
    "v_rcp_f32 v42, v42\n"
    "v_mfma_f32_16x16x32_bf16 v[60:63], v[92:95], v[164:167], v[60:63]\n"
    "buffer_load_dwordx4 a[120:123], v0, s[8:11], s70 offen offset:2048 sc1\n"
    "v_mfma_f32_16x16x32_bf16 v[60:63], v[96:99], v[172:175], v[60:63]\n"
    "ds_read_b128 v[92:95], v2 offset:23552\n"
    "v_rcp_f32 v43, v43\n"
    "v_mul_f32 v44, v44, v40\n"
    "v_mfma_f32_16x16x32_bf16 v[52:55], v[96:99], v[168:171], v[52:55]\n"
    "v_mul_f32 v45, v45, v41\n"
    "v_mul_f32 v46, v46, v42\n"
    "v_mul_f32 v47, v47, v43\n"
    "s_waitcnt lgkmcnt(3)\n"
    "v_mfma_f32_16x16x32_bf16 v[52:55], v[112:115], v[176:179], v[52:55]\n"
    "ds_read_b128 v[96:99], v2 offset:25600\n"
    "v_cvt_pk_bf16_f32 v110, v44, v45\n"
    "v_cvt_pk_bf16_f32 v111, v46, v47\n"
    "v_mfma_f32_16x16x32_bf16 v[60:63], v[112:115], v[180:183], v[60:63]\n"
    "s_add_u32 s73, s85, 23076864\n"
    "v_permlane32_swap_b32 v108, v110\n"
    "v_mfma_f32_16x16x32_bf16 v[60:63], v[10:13], v[188:191], v[60:63]\n"
    "ds_read_b128 v[112:115], v2 offset:27648\n"
    "v_permlane32_swap_b32 v109, v111\n"
    "v_mfma_f32_16x16x32_bf16 v[52:55], v[10:13], v[184:187], v[52:55]\n"
    "s_waitcnt vmcnt(8)\n"
    "s_barrier\n"
    "v_mfma_f32_16x16x32_bf16 v[52:55], v[80:83], v[192:195], v[52:55]\n"
    "ds_read_b128 v[10:13], v2 offset:29696\n"
    "buffer_load_dwordx4 a[124:127], v0, s[8:11], s70 offen offset:3072 sc1\n"
    "buffer_store_dwordx4 v[108:111], v15, s[8:11], s73 offen sc1\n"
    "v_mfma_f32_16x16x32_bf16 v[60:63], v[80:83], v[196:199], v[60:63]\n"
    "s_add_u32 s74, s81, 14422016\n"
    "v_mul_f32 v100, v64, v64\n"
    "s_waitcnt lgkmcnt(3)\n"
    "v_mfma_f32_16x16x32_bf16 v[60:63], v[84:87], v[204:207], v[60:63]\n"
    "ds_read_b128 v[80:83], v2 offset:31744\n"
    "v_mul_f32 v101, v65, v65\n"
    "v_mul_f32 v102, v66, v66\n"
    "v_mfma_f32_16x16x32_bf16 v[52:55], v[84:87], v[200:203], v[52:55]\n"
    "buffer_store_dwordx4 a[236:239], v0, s[8:11], s74 offen sc1\n"
    "v_mul_f32 v103, v67, v67\n"
    "v_mfma_f32_16x16x32_bf16 v[52:55], v[88:91], v[208:211], v[52:55]\n"
    "ds_read_b128 v[84:87], v0 offset:32768\n"
    "v_fma_f32 v100, v100, v122, v120\n"
    "v_fma_f32 v101, v101, v122, v120\n"
    "v_mfma_f32_16x16x32_bf16 v[60:63], v[88:91], v[212:215], v[60:63]\n"
    "s_add_u32 s74, s81, 14424064\n"
    "v_fma_f32 v102, v102, v122, v120\n"
    "v_fma_f32 v103, v103, v122, v120\n"
    "v_mfma_f32_16x16x32_bf16 v[60:63], v[92:95], v[220:223], v[60:63]\n"
    "ds_read_b128 v[88:91], v0 offset:33792\n"
    "v_mul_f32 v68, v64, v68\n"
    "v_mul_f32 v64, v100, v64\n"
    "v_mfma_f32_16x16x32_bf16 v[52:55], v[92:95], v[216:219], v[52:55]\n"
    "v_mul_f32 v69, v65, v69\n"
    "v_mul_f32 v65, v101, v65\n"
    "s_waitcnt lgkmcnt(3)\n"
    "v_mfma_f32_16x16x32_bf16 v[52:55], v[96:99], v[224:227], v[52:55]\n"
    "ds_read_b128 v[92:95], v0 offset:34816\n"
    "v_mul_f32 v70, v66, v70\n"
    "v_mul_f32 v66, v102, v66\n"
    "v_mul_f32 v71, v67, v71\n"
    "v_mfma_f32_16x16x32_bf16 v[60:63], v[96:99], v[228:231], v[60:63]\n"
    "buffer_store_dwordx4 a[236:239], v0, s[8:11], s74 offen sc1\n"
    "v_mul_f32 v67, v103, v67\n"
    "v_mfma_f32_16x16x32_bf16 v[60:63], v[112:115], v[236:239], v[60:63]\n"
    "ds_read_b128 v[96:99], v0 offset:35840\n"
    "v_mul_u32_u24 v7, 17, v0\n"
    "v_mfma_f32_16x16x32_bf16 v[52:55], v[112:115], v[232:235], v[52:55]\n"
    "v_cmp_gt_u32 vcc, 0x100, v0\n"
    "v_bfe_u32 v7, v7, 7, 4\n"
    "v_lshlrev_b32 v7, 3, v7\n"
    "v_cndmask_b32 v7, 2.0, v7, vcc\n"
    "v_exp_f32 v64, v64\n"
    "v_mfma_f32_16x16x32_bf16 v[52:55], v[10:13], v[240:243], v[52:55]\n"
    "ds_read_b128 v[112:115], v0 offset:36864\n"
    "s_add_u32 s74, s82, 14422016\n"
    "v_mfma_f32_16x16x32_bf16 v[60:63], v[10:13], v[244:247], v[60:63]\n"
    "v_exp_f32 v65, v65\n"
    "s_waitcnt lgkmcnt(3)\n"
    "v_mfma_f32_16x16x32_bf16 v[60:63], v[80:83], v[252:255], v[60:63]\n"
    "ds_read_b128 v[10:13], v0 offset:37888\n"
    "v_exp_f32 v66, v66\n"
    "v_mfma_f32_16x16x32_bf16 v[52:55], v[80:83], v[248:251], v[52:55]\n"
    "buffer_store_dwordx2 a[236:237], v7, s[8:11], s74 offen sc1\n"
    "v_exp_f32 v67, v67\n"
    "v_mfma_f32_16x16x16_bf16 v[32:35], a[204:205], v[124:125], 0\n"
    "v_fma_f32 v64, v64, v116, v116\n"
    "v_fma_f32 v65, v65, v116, v116\n"
    "v_fma_f32 v66, v66, v116, v116\n"
    "v_fma_f32 v67, v67, v116, v116\n"
    "v_mfma_f32_16x16x16_bf16 v[40:43], a[204:205], v[126:127], 0\n"
    "v_rcp_f32 v64, v64\n"
    "v_mfma_f32_16x16x16_bf16 v[36:39], a[206:207], v[124:125], 0\n"
    "v_rcp_f32 v65, v65\n"
    "v_rcp_f32 v66, v66\n"
    "v_mfma_f32_16x16x16_bf16 v[44:47], a[206:207], v[126:127], 0\n"
    "s_add_u32 s70, s79, 32768\n"
    "v_rcp_f32 v67, v67\n"
    "v_mfma_f32_16x16x32_bf16 v[32:35], v[84:87], v[128:131], v[32:35]\n"
    "ds_read_b128 v[80:83], v0 offset:38912\n"
    "v_mul_f32 v68, v68, v64\n"
    "v_mul_f32 v69, v69, v65\n"
    "v_mfma_f32_16x16x32_bf16 v[40:43], v[84:87], v[132:135], v[40:43]\n"
    "v_mul_f32 v70, v70, v66\n"
    "v_mul_f32 v71, v71, v67\n"
    "v_cvt_pk_bf16_f32 v104, v68, v69\n"
    "v_mfma_f32_16x16x32_bf16 v[44:47], v[88:91], v[132:135], v[44:47]\n"
    "ds_read_b128 v[84:87], v0 offset:39936\n"
    "v_cvt_pk_bf16_f32 v105, v70, v71\n"
    "v_mul_f32 v100, v72, v72\n"
    "v_mfma_f32_16x16x32_bf16 v[36:39], v[88:91], v[128:131], v[36:39]\n"
    "v_mul_f32 v101, v73, v73\n"
    "v_mul_f32 v102, v74, v74\n"
    "s_waitcnt lgkmcnt(3)\n"
    "v_mfma_f32_16x16x32_bf16 v[32:35], v[92:95], v[136:139], v[32:35]\n"
    "ds_read_b128 v[88:91], v0 offset:40960\n"
    "buffer_load_dwordx4 a[128:131], v0, s[8:11], s70 offen offset:0 sc1\n"
    "v_mul_f32 v103, v75, v75\n"
    "v_fma_f32 v100, v100, v123, v121\n"
    "v_mfma_f32_16x16x32_bf16 v[40:43], v[92:95], v[140:143], v[40:43]\n"
    "v_fma_f32 v101, v101, v123, v121\n"
    "v_fma_f32 v102, v102, v123, v121\n"
    "v_fma_f32 v103, v103, v123, v121\n"
    "v_mfma_f32_16x16x32_bf16 v[44:47], v[96:99], v[140:143], v[44:47]\n"
    "ds_read_b128 v[92:95], v0 offset:41984\n"
    "v_mul_f32 v76, v72, v76\n"
    "v_mul_f32 v72, v100, v72\n"
    "v_mfma_f32_16x16x32_bf16 v[36:39], v[96:99], v[136:139], v[36:39]\n"
    "v_mul_f32 v77, v73, v77\n"
    "v_mul_f32 v73, v101, v73\n"
    "v_mul_f32 v78, v74, v78\n"
    "v_mfma_f32_16x16x32_bf16 v[32:35], v[112:115], v[144:147], v[32:35]\n"
    "ds_read_b128 v[96:99], v0 offset:43008\n"
    "v_mul_f32 v74, v102, v74\n"
    "v_mul_f32 v79, v75, v79\n"
    "v_mfma_f32_16x16x32_bf16 v[40:43], v[112:115], v[148:151], v[40:43]\n"
    "v_mul_f32 v75, v103, v75\n"
    "s_waitcnt lgkmcnt(3)\n"
    "v_mfma_f32_16x16x32_bf16 v[44:47], v[10:13], v[148:151], v[44:47]\n"
    "ds_read_b128 v[112:115], v0 offset:44032\n"
    "v_exp_f32 v72, v72\n"
    "v_mfma_f32_16x16x32_bf16 v[36:39], v[10:13], v[144:147], v[36:39]\n"
    "s_waitcnt vmcnt(7)\n"
    "v_readfirstlane_b32 s29, v8\n"
    "v_exp_f32 v73, v73\n"
    "v_mfma_f32_16x16x32_bf16 v[32:35], v[80:83], v[152:155], v[32:35]\n"
    "ds_read_b128 v[10:13], v0 offset:45056\n"
    "s_cmp_lt_u32 s29, 16\n"
    "s_cbranch_scc1 Lslow23\n"
    "Lback23:\n"
    "v_exp_f32 v74, v74\n"
    "v_mfma_f32_16x16x32_bf16 v[40:43], v[80:83], v[156:159], v[40:43]\n"
    "s_add_u32 s71, s87, 23093248\n"
    "s_add_u32 s71, s71, s89\n"
    "v_exp_f32 v75, v75\n"
    "v_mfma_f32_16x16x32_bf16 v[44:47], v[84:87], v[156:159], v[44:47]\n"
    "ds_read_b128 v[80:83], v0 offset:46080\n"
    "v_fma_f32 v72, v72, v117, v117\n"
    "v_fma_f32 v73, v73, v117, v117\n"
    "v_mfma_f32_16x16x32_bf16 v[36:39], v[84:87], v[152:155], v[36:39]\n"
    "v_fma_f32 v74, v74, v117, v117\n"
    "v_fma_f32 v75, v75, v117, v117\n"
    "s_waitcnt lgkmcnt(3)\n"
    "v_mfma_f32_16x16x32_bf16 v[32:35], v[88:91], v[160:163], v[32:35]\n"
    "ds_read_b128 v[84:87], v0 offset:47104\n"
    "buffer_load_dwordx4 v[16:19], v0, s[8:11], s71 offen offset:0 sc1\n"
    "v_mfma_f32_16x16x32_bf16 v[40:43], v[88:91], v[164:167], v[40:43]\n"
    "buffer_load_dwordx4 v[20:23], v0, s[8:11], s71 offen offset:1024 sc1\n"
    "v_rcp_f32 v72, v72\n"
    "v_mfma_f32_16x16x32_bf16 v[44:47], v[92:95], v[164:167], v[44:47]\n"
    "ds_read_b128 v[88:91], v0 offset:48128\n"
    "s_add_u32 s72, s88, 23093248\n"
    "v_mfma_f32_16x16x32_bf16 v[36:39], v[92:95], v[160:163], v[36:39]\n"
    "s_add_u32 s72, s72, s89\n"
    "v_rcp_f32 v73, v73\n"
    "v_mfma_f32_16x16x32_bf16 v[32:35], v[96:99], v[168:171], v[32:35]\n"
    "ds_read_b128 v[92:95], v0 offset:49152\n"
    "v_rcp_f32 v74, v74\n"
    "v_rcp_f32 v75, v75\n"
    "v_mfma_f32_16x16x32_bf16 v[40:43], v[96:99], v[172:175], v[40:43]\n"
    "v_mul_f32 v76, v76, v72\n"
    "v_mul_f32 v77, v77, v73\n"
    "s_waitcnt lgkmcnt(3)\n"
    "v_mfma_f32_16x16x32_bf16 v[44:47], v[112:115], v[172:175], v[44:47]\n"
    "ds_read_b128 v[96:99], v0 offset:50176\n"
    "v_mul_f32 v78, v78, v74\n"
    "v_mul_f32 v79, v79, v75\n"
    "v_mfma_f32_16x16x32_bf16 v[36:39], v[112:115], v[168:171], v[36:39]\n"
    "buffer_load_dwordx4 v[24:27], v0, s[8:11], s72 offen offset:0 sc1\n"
    "v_cvt_pk_bf16_f32 v106, v76, v77\n"
    "v_mfma_f32_16x16x32_bf16 v[32:35], v[10:13], v[176:179], v[32:35]\n"
    "ds_read_b128 v[112:115], v0 offset:51200\n"
    "v_cvt_pk_bf16_f32 v107, v78, v79\n"
    "s_add_u32 s73, s85, 23080960\n"
    "v_mfma_f32_16x16x32_bf16 v[40:43], v[10:13], v[180:183], v[40:43]\n"
    "buffer_load_dwordx4 v[28:31], v0, s[8:11], s72 offen offset:1024 sc1\n"
    "v_mfma_f32_16x16x32_bf16 v[44:47], v[80:83], v[180:183], v[44:47]\n"
    "ds_read_b128 v[10:13], v0 offset:52224\n"
    "s_add_u32 s71, s87, 23072768\n"
    "v_permlane32_swap_b32 v104, v106\n"
    "v_mfma_f32_16x16x32_bf16 v[36:39], v[80:83], v[176:179], v[36:39]\n"
    "v_permlane32_swap_b32 v105, v107\n"
    "s_waitcnt lgkmcnt(3)\n"
    "v_mfma_f32_16x16x32_bf16 v[32:35], v[84:87], v[184:187], v[32:35]\n"
    "ds_read_b128 v[80:83], v0 offset:53248\n"
    "buffer_store_dwordx4 v[104:107], v15, s[8:11], s73 offen sc1\n"
    "v_mfma_f32_16x16x32_bf16 v[40:43], v[84:87], v[188:191], v[40:43]\n"
    "buffer_load_dwordx4 v[128:131], v0, s[8:11], s71 offen offset:0 sc1\n"
    "v_mul_f32 v100, v48, v48\n"
    "v_mfma_f32_16x16x32_bf16 v[44:47], v[88:91], v[188:191], v[44:47]\n"
    "ds_read_b128 v[84:87], v0 offset:54272\n"
    "v_mul_f32 v101, v49, v49\n"
    "v_mul_f32 v102, v50, v50\n"
    "v_mfma_f32_16x16x32_bf16 v[36:39], v[88:91], v[184:187], v[36:39]\n"
    "v_mul_f32 v103, v51, v51\n"
    "v_fma_f32 v100, v100, v122, v120\n"
    "v_fma_f32 v101, v101, v122, v120\n"
    "v_mfma_f32_16x16x32_bf16 v[32:35], v[92:95], v[192:195], v[32:35]\n"
    "ds_read_b128 v[88:91], v0 offset:55296\n"
    "v_fma_f32 v102, v102, v122, v120\n"
    "v_fma_f32 v103, v103, v122, v120\n"
    "v_mfma_f32_16x16x32_bf16 v[40:43], v[92:95], v[196:199], v[40:43]\n"
    "v_mul_f32 v52, v48, v52\n"
    "v_mul_f32 v48, v100, v48\n"
    "s_waitcnt lgkmcnt(3)\n"
    "v_mfma_f32_16x16x32_bf16 v[44:47], v[96:99], v[196:199], v[44:47]\n"
    "ds_read_b128 v[92:95], v0 offset:56320\n"
    "v_mul_f32 v53, v49, v53\n"
    "v_mul_f32 v49, v101, v49\n"
    "v_mfma_f32_16x16x32_bf16 v[36:39], v[96:99], v[192:195], v[36:39]\n"
    "buffer_load_dwordx4 v[132:135], v0, s[8:11], s71 offen offset:1024 sc1\n"
    "v_mul_f32 v54, v50, v54\n"
    "v_mfma_f32_16x16x32_bf16 v[32:35], v[112:115], v[200:203], v[32:35]\n"
    "ds_read_b128 v[96:99], v0 offset:57344\n"
    "buffer_load_dwordx4 a[132:135], v0, s[8:11], s70 offen offset:1024 sc1\n"
    "v_mul_f32 v50, v102, v50\n"
    "v_mul_f32 v55, v51, v55\n"
    "v_mfma_f32_16x16x32_bf16 v[40:43], v[112:115], v[204:207], v[40:43]\n"
    "s_add_u32 s72, s88, 23072768\n"
    "v_mul_f32 v51, v103, v51\n"
    "v_mfma_f32_16x16x32_bf16 v[44:47], v[10:13], v[204:207], v[44:47]\n"
    "ds_read_b128 v[112:115], v0 offset:58368\n"
    "buffer_load_dwordx4 v[136:139], v0, s[8:11], s72 offen offset:0 sc1\n"
    "v_exp_f32 v48, v48\n"
    "v_mfma_f32_16x16x32_bf16 v[36:39], v[10:13], v[200:203], v[36:39]\n"
    "v_exp_f32 v49, v49\n"
    "s_waitcnt lgkmcnt(3)\n"
    "v_mfma_f32_16x16x32_bf16 v[32:35], v[80:83], v[208:211], v[32:35]\n"
    "ds_read_b128 v[10:13], v0 offset:59392\n"
    "v_exp_f32 v50, v50\n"
    "v_mfma_f32_16x16x32_bf16 v[40:43], v[80:83], v[212:215], v[40:43]\n"
    "buffer_load_dwordx4 v[140:143], v0, s[8:11], s72 offen offset:1024 sc1\n"
    "v_mfma_f32_16x16x32_bf16 v[44:47], v[84:87], v[212:215], v[44:47]\n"
    "ds_read_b128 v[80:83], v0 offset:60416\n"
    "v_exp_f32 v51, v51\n"
    "v_fma_f32 v48, v48, v116, v116\n"
    "v_mfma_f32_16x16x32_bf16 v[36:39], v[84:87], v[208:211], v[36:39]\n"
    "v_fma_f32 v49, v49, v116, v116\n"
    "v_fma_f32 v50, v50, v116, v116\n"
    "v_fma_f32 v51, v51, v116, v116\n"
    "v_mfma_f32_16x16x32_bf16 v[32:35], v[88:91], v[216:219], v[32:35]\n"
    "ds_read_b128 v[84:87], v0 offset:61440\n"
    "v_rcp_f32 v48, v48\n"
    "v_mfma_f32_16x16x32_bf16 v[40:43], v[88:91], v[220:223], v[40:43]\n"
    "v_rcp_f32 v49, v49\n"
    "s_waitcnt lgkmcnt(3)\n"
    "v_mfma_f32_16x16x32_bf16 v[44:47], v[92:95], v[220:223], v[44:47]\n"
    "ds_read_b128 v[88:91], v0 offset:62464\n"
    "buffer_load_dwordx4 v[64:67], v0, s[8:11], s71 offen offset:2048 sc1\n"
    "v_rcp_f32 v50, v50\n"
    "v_mfma_f32_16x16x32_bf16 v[36:39], v[92:95], v[216:219], v[36:39]\n"
    "v_rcp_f32 v51, v51\n"
    "v_mul_f32 v52, v52, v48\n"
    "v_mfma_f32_16x16x32_bf16 v[32:35], v[96:99], v[224:227], v[32:35]\n"
    "ds_read_b128 v[92:95], v0 offset:63488\n"
    "v_mul_f32 v53, v53, v49\n"
    "v_mul_f32 v54, v54, v50\n"
    "v_mfma_f32_16x16x32_bf16 v[40:43], v[96:99], v[228:231], v[40:43]\n"
    "buffer_load_dwordx4 v[68:71], v0, s[8:11], s71 offen offset:3072 sc1\n"
    "v_mul_f32 v55, v55, v51\n"
    "v_mfma_f32_16x16x32_bf16 v[44:47], v[112:115], v[228:231], v[44:47]\n"
    "ds_read_b128 v[96:99], v0 offset:64512\n"
    "v_cvt_pk_bf16_f32 v108, v52, v53\n"
    "v_cvt_pk_bf16_f32 v109, v54, v55\n"
    "v_mfma_f32_16x16x32_bf16 v[36:39], v[112:115], v[224:227], v[36:39]\n"
    "v_mul_f32 v100, v56, v56\n"
    "v_mul_f32 v101, v57, v57\n"
    "s_waitcnt lgkmcnt(3)\n"
    "v_mfma_f32_16x16x32_bf16 v[32:35], v[10:13], v[232:235], v[32:35]\n"
    "buffer_load_dwordx4 v[72:75], v0, s[8:11], s72 offen offset:2048 sc1\n"
    "v_mul_f32 v102, v58, v58\n"
    "v_mul_f32 v103, v59, v59\n"
    "v_mfma_f32_16x16x32_bf16 v[40:43], v[10:13], v[236:239], v[40:43]\n"
    "v_fma_f32 v100, v100, v123, v121\n"
    "v_fma_f32 v101, v101, v123, v121\n"
    "v_fma_f32 v102, v102, v123, v121\n"
    "v_mfma_f32_16x16x32_bf16 v[44:47], v[80:83], v[236:239], v[44:47]\n"
    "v_fma_f32 v103, v103, v123, v121\n"
    "v_mul_f32 v60, v56, v60\n"
    "v_mul_f32 v56, v100, v56\n"
    "v_mfma_f32_16x16x32_bf16 v[36:39], v[80:83], v[232:235], v[36:39]\n"
    "buffer_load_dwordx4 v[76:79], v0, s[8:11], s72 offen offset:3072 sc1\n"
    "v_mul_f32 v61, v57, v61\n"
    "v_mfma_f32_16x16x32_bf16 v[32:35], v[84:87], v[240:243], v[32:35]\n"
    "s_add_u32 s71, s87, 23076864\n"
    "v_mul_f32 v57, v101, v57\n"
    "v_mul_f32 v62, v58, v62\n"
    "v_mfma_f32_16x16x32_bf16 v[40:43], v[84:87], v[244:247], v[40:43]\n"
    "v_mul_f32 v58, v102, v58\n"
    "v_mul_f32 v63, v59, v63\n"
    "s_waitcnt lgkmcnt(0)\n"
    "v_mfma_f32_16x16x32_bf16 v[44:47], v[88:91], v[244:247], v[44:47]\n"
    "v_mul_f32 v59, v103, v59\n"
    "v_exp_f32 v56, v56\n"
    "v_mfma_f32_16x16x32_bf16 v[36:39], v[88:91], v[240:243], v[36:39]\n"
    "buffer_load_dwordx4 v[144:147], v0, s[8:11], s71 offen offset:0 sc1\n"
    "v_mfma_f32_16x16x32_bf16 v[32:35], v[92:95], v[248:251], v[32:35]\n"
    "v_exp_f32 v57, v57\n"
    "v_exp_f32 v58, v58\n"
    "v_mfma_f32_16x16x32_bf16 v[40:43], v[92:95], v[252:255], v[40:43]\n"
    "v_exp_f32 v59, v59\n"
    "v_fma_f32 v56, v56, v117, v117\n"
    "v_fma_f32 v57, v57, v117, v117\n"
    "v_mfma_f32_16x16x32_bf16 v[44:47], v[96:99], v[252:255], v[44:47]\n"
    "v_fma_f32 v58, v58, v117, v117\n"
    "v_fma_f32 v59, v59, v117, v117\n"
    "v_mfma_f32_16x16x32_bf16 v[36:39], v[96:99], v[248:251], v[36:39]\n"
    "buffer_load_dwordx4 v[148:151], v0, s[8:11], s71 offen offset:1024 sc1\n"
    "v_rcp_f32 v56, v56\n"
    "v_rcp_f32 v57, v57\n"
    "v_rcp_f32 v58, v58\n"
    "v_rcp_f32 v59, v59\n"
    "v_mul_f32 v60, v60, v56\n"
    "v_mul_f32 v61, v61, v57\n"
    "v_mul_f32 v62, v62, v58\n"
    "v_mul_f32 v63, v63, v59\n"
    "v_cvt_pk_bf16_f32 v110, v60, v61\n"
    "v_cvt_pk_bf16_f32 v111, v62, v63\n"
    "s_add_u32 s73, s85, 23085056\n"
    "v_permlane32_swap_b32 v108, v110\n"
    "v_permlane32_swap_b32_e64 v109, v111\n"
    "buffer_store_dwordx4 v[108:111], v15, s[8:11], s73 offen sc1\n"
    "v_pk_mul_f32 v[80:81], v[32:33], v[32:33] op_sel:[0,0] op_sel_hi:[1,1]\n"
    "v_pk_mul_f32 v[82:83], v[34:35], v[34:35] op_sel:[0,0] op_sel_hi:[1,1]\n"
    "v_pk_mul_f32 v[84:85], v[40:41], v[40:41] op_sel:[0,0] op_sel_hi:[1,1]\n"
    "v_pk_mul_f32 v[86:87], v[42:43], v[42:43] op_sel:[0,0] op_sel_hi:[1,1]\n"
    "v_pk_fma_f32 v[80:81], v[80:81], v[122:123], v[120:121] op_sel:[0,0,0] op_sel_hi:[1,0,0]\n"
    "v_pk_fma_f32 v[82:83], v[82:83], v[122:123], v[120:121] op_sel:[0,0,0] op_sel_hi:[1,0,0]\n"
    "s_add_u32 s72, s88, 23076864\n"
    "buffer_load_dwordx4 v[152:155], v0, s[8:11], s72 offen offset:0 sc1\n"
    "v_pk_fma_f32 v[84:85], v[84:85], v[122:123], v[120:121] op_sel:[0,1,1] op_sel_hi:[1,1,1]\n"
    "v_pk_fma_f32 v[86:87], v[86:87], v[122:123], v[120:121] op_sel:[0,1,1] op_sel_hi:[1,1,1]\n"
    "v_pk_mul_f32 v[80:81], v[80:81], v[32:33] op_sel:[0,0] op_sel_hi:[1,1]\n"
    "v_pk_mul_f32 v[82:83], v[82:83], v[34:35] op_sel:[0,0] op_sel_hi:[1,1]\n"
    "v_pk_mul_f32 v[84:85], v[84:85], v[40:41] op_sel:[0,0] op_sel_hi:[1,1]\n"
    "v_pk_mul_f32 v[86:87], v[86:87], v[42:43] op_sel:[0,0] op_sel_hi:[1,1]\n"
    "v_exp_f32 v80, v80\n"
    "v_exp_f32 v81, v81\n"
    "buffer_load_dwordx4 v[156:159], v0, s[8:11], s72 offen offset:1024 sc1\n"
    "v_exp_f32 v82, v82\n"
    "v_exp_f32 v83, v83\n"
    "v_exp_f32 v84, v84\n"
    "v_exp_f32 v85, v85\n"
    "v_exp_f32 v86, v86\n"
    "v_exp_f32 v87, v87\n"
    "v_pk_mul_f32 v[36:37], v[32:33], v[36:37] op_sel:[0,0] op_sel_hi:[1,1]\n"
    "v_pk_mul_f32 v[38:39], v[34:35], v[38:39] op_sel:[0,0] op_sel_hi:[1,1]\n"
    "v_pk_mul_f32 v[44:45], v[40:41], v[44:45] op_sel:[0,0] op_sel_hi:[1,1]\n"
    "v_pk_mul_f32 v[46:47], v[42:43], v[46:47] op_sel:[0,0] op_sel_hi:[1,1]\n"
    "buffer_load_dwordx4 v[160:163], v0, s[8:11], s71 offen offset:2048 sc1\n"
    "v_pk_fma_f32 v[80:81], v[80:81], v[116:117], v[116:117] op_sel:[0,0,0] op_sel_hi:[1,0,0]\n"
    "v_pk_fma_f32 v[82:83], v[82:83], v[116:117], v[116:117] op_sel:[0,0,0] op_sel_hi:[1,0,0]\n"
    "v_pk_fma_f32 v[84:85], v[84:85], v[116:117], v[116:117] op_sel:[0,1,1] op_sel_hi:[1,1,1]\n"
    "v_pk_fma_f32 v[86:87], v[86:87], v[116:117], v[116:117] op_sel:[0,1,1] op_sel_hi:[1,1,1]\n"
    "v_rcp_f32 v80, v80\n"
    "v_rcp_f32 v81, v81\n"
    "v_rcp_f32 v82, v82\n"
    "v_rcp_f32 v83, v83\n"
    "buffer_load_dwordx4 v[164:167], v0, s[8:11], s71 offen offset:3072 sc1\n"
    "v_rcp_f32 v84, v84\n"
    "v_rcp_f32 v85, v85\n"
    "v_rcp_f32 v86, v86\n"
    "v_rcp_f32 v87, v87\n"
    "v_pk_mul_f32 v[36:37], v[36:37], v[80:81] op_sel:[0,0] op_sel_hi:[1,1]\n"
    "v_pk_mul_f32 v[38:39], v[38:39], v[82:83] op_sel:[0,0] op_sel_hi:[1,1]\n"
    "v_pk_mul_f32 v[44:45], v[44:45], v[84:85] op_sel:[0,0] op_sel_hi:[1,1]\n"
    "v_pk_mul_f32 v[46:47], v[46:47], v[86:87] op_sel:[0,0] op_sel_hi:[1,1]\n"
    "v_cvt_pk_bf16_f32 v104, v36, v37\n"
    "v_cvt_pk_bf16_f32 v105, v38, v39\n"
    "v_cvt_pk_bf16_f32 v106, v44, v45\n"
    "v_cvt_pk_bf16_f32 v107, v46, v47\n"
    "s_add_u32 s73, s85, 23089152\n"
    "v_permlane32_swap_b32 v104, v106\n"
    "v_permlane32_swap_b32 v105, v107\n"
    "buffer_store_dwordx4 v[104:107], v15, s[8:11], s73 offen sc1\n"
    "s_waitcnt vmcnt(23)\n"
    "s_barrier\n"
    "Lredo24:\n"
    "s_waitcnt vmcnt(20)\n"
    "v_mfma_f32_16x16x32_bf16 v[32:35], a[160:163], v[20:23], 0\n"
    "buffer_load_dwordx4 v[168:171], v0, s[8:11], s72 offen offset:2048 sc1\n"
    "v_mfma_f32_16x16x32_bf16 v[36:39], a[164:167], v[20:23], 0\n"
    "v_accvgpr_read_b32 v208, a220\n"
    "v_accvgpr_read_b32 v209, a221\n"
    "v_mfma_f32_16x16x32_bf16 v[40:43], a[168:171], v[20:23], 0\n"
    "v_accvgpr_read_b32 v210, a222\n"
    "v_accvgpr_read_b32 v211, a223\n"
    "v_mfma_f32_16x16x32_bf16 v[44:47], a[172:175], v[20:23], 0\n"
    "v_accvgpr_read_b32 v212, a224\n"
    "v_accvgpr_read_b32 v213, a225\n"
    "s_waitcnt vmcnt(20)\n"
    "v_mfma_f32_16x16x32_bf16 v[236:239], a[172:175], v[24:27], 0\n"
    "v_accvgpr_read_b32 v214, a226\n"
    "v_accvgpr_read_b32 v215, a227\n"
    "v_mfma_f32_16x16x32_bf16 v[232:235], a[168:171], v[24:27], 0\n"
    "buffer_load_dwordx4 v[172:175], v0, s[8:11], s72 offen offset:3072 sc1\n"
    "v_mfma_f32_16x16x32_bf16 v[228:231], a[164:167], v[24:27], 0\n"
    "s_add_u32 s71, s87, 23080960\n"
    "v_accvgpr_read_b32 v216, a228\n"
    "v_mfma_f32_16x16x32_bf16 v[224:227], a[160:163], v[24:27], 0\n"
    "v_accvgpr_read_b32 v217, a229\n"
    "v_accvgpr_read_b32 v218, a230\n"
    "v_accvgpr_read_b32 v219, a231\n"
    "s_waitcnt vmcnt(20)\n"
    "v_mfma_f32_16x16x32_bf16 v[240:243], a[160:163], v[28:31], 0\n"
    "v_accvgpr_read_b32 v220, a232\n"
    "v_accvgpr_read_b32 v221, a233\n"
    "v_mfma_f32_16x16x32_bf16 v[244:247], a[164:167], v[28:31], 0\n"
    "buffer_load_dwordx4 v[176:179], v0, s[8:11], s71 offen offset:0 sc1\n"
    "v_mfma_f32_16x16x32_bf16 v[248:251], a[168:171], v[28:31], 0\n"
    "v_accvgpr_read_b32 v222, a234\n"
    "v_accvgpr_read_b32 v223, a235\n"
    "v_mfma_f32_16x16x32_bf16 v[252:255], a[172:175], v[28:31], 0\n"
    "s_xor_b32 s62, s58, 0\n"
    "s_mul_i32 s62, s62, 0x3000\n"
    "v_mfma_f32_16x16x32_bf16 v[220:223], a[172:175], v[16:19], v[220:223]\n"
    "s_add_u32 s62, s62, 0x18000\n"
    "v_add_u32 v10, s62, v0\n"
    "v_mfma_f32_16x16x32_bf16 v[216:219], a[168:171], v[16:19], v[216:219]\n"
    "s_xor_b32 s62, s58, 1\n"
    "s_mul_i32 s62, s62, 0x3000\n"
    "v_mfma_f32_16x16x32_bf16 v[212:215], a[164:167], v[16:19], v[212:215]\n"
    "buffer_load_dwordx4 v[180:183], v0, s[8:11], s71 offen offset:1024 sc1\n"
    "v_mfma_f32_16x16x32_bf16 v[208:211], a[160:163], v[16:19], v[208:211]\n"
    "s_add_u32 s72, s88, 23080960\n"
    "s_add_u32 s62, s62, 0x18000\n"
    "s_waitcnt vmcnt(19)\n"
    "v_mfma_f32_16x16x32_bf16 v[208:211], a[0:3], v[128:131], v[208:211]\n"
    "v_add_u32 v11, s62, v0\n"
    "s_xor_b32 s62, s58, 2\n"
    "v_mfma_f32_16x16x32_bf16 v[212:215], a[4:7], v[128:131], v[212:215]\n"
    "s_mul_i32 s62, s62, 0x3000\n"
    "s_add_u32 s62, s62, 0x19000\n"
    "v_mfma_f32_16x16x32_bf16 v[216:219], a[8:11], v[128:131], v[216:219]\n"
    "buffer_load_dwordx4 v[184:187], v0, s[8:11], s72 offen offset:0 sc1\n"
    "v_mfma_f32_16x16x32_bf16 v[220:223], a[12:15], v[128:131], v[220:223]\n"
    "v_add_u32 v12, s62, v0\n"
    "s_xor_b32 s62, s58, 3\n"
    "v_mfma_f32_16x16x32_bf16 v[44:47], a[12:15], v[132:135], v[44:47]\n"
    "s_mul_i32 s62, s62, 0x3000\n"
    "s_add_u32 s62, s62, 0x1a000\n"
    "v_mfma_f32_16x16x32_bf16 v[40:43], a[8:11], v[132:135], v[40:43]\n"
    "v_add_u32 v13, s62, v0\n"
    "v_mfma_f32_16x16x32_bf16 v[36:39], a[4:7], v[132:135], v[36:39]\n"
    "buffer_load_dwordx4 v[188:191], v0, s[8:11], s72 offen offset:1024 sc1\n"
    "v_mfma_f32_16x16x32_bf16 v[32:35], a[0:3], v[132:135], v[32:35]\n"
    "s_waitcnt vmcnt(18)\n"
    "v_mfma_f32_16x16x32_bf16 v[224:227], a[0:3], v[136:139], v[224:227]\n"
    "v_mfma_f32_16x16x32_bf16 v[228:231], a[4:7], v[136:139], v[228:231]\n"
    "v_mfma_f32_16x16x32_bf16 v[232:235], a[8:11], v[136:139], v[232:235]\n"
    "v_mfma_f32_16x16x32_bf16 v[236:239], a[12:15], v[136:139], v[236:239]\n"
    "buffer_load_dwordx4 v[192:195], v0, s[8:11], s71 offen offset:2048 sc1\n"
    "v_mfma_f32_16x16x32_bf16 v[252:255], a[12:15], v[140:143], v[252:255]\n"
    "v_mfma_f32_16x16x32_bf16 v[248:251], a[8:11], v[140:143], v[248:251]\n"
    "v_mfma_f32_16x16x32_bf16 v[244:247], a[4:7], v[140:143], v[244:247]\n"
    "v_mfma_f32_16x16x32_bf16 v[240:243], a[0:3], v[140:143], v[240:243]\n"
    "s_waitcnt vmcnt(17)\n"
    "v_mfma_f32_16x16x32_bf16 v[208:211], a[16:19], v[64:67], v[208:211]\n"
    "buffer_load_dwordx4 v[196:199], v0, s[8:11], s71 offen offset:3072 sc1\n"
    "v_mfma_f32_16x16x32_bf16 v[212:215], a[20:23], v[64:67], v[212:215]\n"
    "v_mfma_f32_16x16x32_bf16 v[216:219], a[24:27], v[64:67], v[216:219]\n"
    "v_mfma_f32_16x16x32_bf16 v[220:223], a[28:31], v[64:67], v[220:223]\n"
    "v_mfma_f32_16x16x32_bf16 v[44:47], a[28:31], v[68:71], v[44:47]\n"
    "buffer_load_dwordx4 v[200:203], v0, s[8:11], s72 offen offset:2048 sc1\n"
    "v_mfma_f32_16x16x32_bf16 v[40:43], a[24:27], v[68:71], v[40:43]\n"
    "v_mfma_f32_16x16x32_bf16 v[36:39], a[20:23], v[68:71], v[36:39]\n"
    "v_mfma_f32_16x16x32_bf16 v[32:35], a[16:19], v[68:71], v[32:35]\n"
    "s_waitcnt vmcnt(17)\n"
    "v_mfma_f32_16x16x32_bf16 v[224:227], a[16:19], v[72:75], v[224:227]\n"
    "buffer_load_dwordx4 v[204:207], v0, s[8:11], s72 offen offset:3072 sc1\n"
    "v_mfma_f32_16x16x32_bf16 v[228:231], a[20:23], v[72:75], v[228:231]\n"
    "s_add_u32 s71, s87, 23085056\n"
    "v_mfma_f32_16x16x32_bf16 v[232:235], a[24:27], v[72:75], v[232:235]\n"
    "v_mfma_f32_16x16x32_bf16 v[236:239], a[28:31], v[72:75], v[236:239]\n"
    "v_mfma_f32_16x16x32_bf16 v[252:255], a[28:31], v[76:79], v[252:255]\n"
    "buffer_load_dwordx4 v[48:51], v0, s[8:11], s71 offen offset:0 sc1\n"
    "v_mfma_f32_16x16x32_bf16 v[248:251], a[24:27], v[76:79], v[248:251]\n"
    "v_mfma_f32_16x16x32_bf16 v[244:247], a[20:23], v[76:79], v[244:247]\n"
    "v_mfma_f32_16x16x32_bf16 v[240:243], a[16:19], v[76:79], v[240:243]\n"
    "s_waitcnt vmcnt(17)\n"
    "v_mfma_f32_16x16x32_bf16 v[208:211], a[32:35], v[144:147], v[208:211]\n"
    "v_mfma_f32_16x16x32_bf16 v[212:215], a[36:39], v[144:147], v[212:215]\n"
    "buffer_load_dwordx4 v[52:55], v0, s[8:11], s71 offen offset:1024 sc1\n"
    "v_mfma_f32_16x16x32_bf16 v[216:219], a[40:43], v[144:147], v[216:219]\n"
    "s_add_u32 s72, s88, 23085056\n"
    "v_mfma_f32_16x16x32_bf16 v[220:223], a[44:47], v[144:147], v[220:223]\n"
    "v_mfma_f32_16x16x32_bf16 v[44:47], a[44:47], v[148:151], v[44:47]\n"
    "v_mfma_f32_16x16x32_bf16 v[40:43], a[40:43], v[148:151], v[40:43]\n"
    "buffer_load_dwordx4 v[56:59], v0, s[8:11], s72 offen offset:0 sc1\n"
    "v_mfma_f32_16x16x32_bf16 v[36:39], a[36:39], v[148:151], v[36:39]\n"
    "v_mfma_f32_16x16x32_bf16 v[32:35], a[32:35], v[148:151], v[32:35]\n"
    "s_waitcnt vmcnt(16)\n"
    "v_mfma_f32_16x16x32_bf16 v[224:227], a[32:35], v[152:155], v[224:227]\n"
    "v_mfma_f32_16x16x32_bf16 v[228:231], a[36:39], v[152:155], v[228:231]\n"
    "v_mfma_f32_16x16x32_bf16 v[232:235], a[40:43], v[152:155], v[232:235]\n"
    "buffer_load_dwordx4 v[60:63], v0, s[8:11], s72 offen offset:1024 sc1\n"
    "v_mfma_f32_16x16x32_bf16 v[236:239], a[44:47], v[152:155], v[236:239]\n"
    "s_add_u32 s70, s79, 32768\n"
    "v_mfma_f32_16x16x32_bf16 v[252:255], a[44:47], v[156:159], v[252:255]\n"
    "v_mfma_f32_16x16x32_bf16 v[248:251], a[40:43], v[156:159], v[248:251]\n"
    "v_mfma_f32_16x16x32_bf16 v[244:247], a[36:39], v[156:159], v[244:247]\n"
    "buffer_load_dwordx4 a[136:139], v0, s[8:11], s70 offen offset:2048 sc1\n"
    "v_mfma_f32_16x16x32_bf16 v[240:243], a[32:35], v[156:159], v[240:243]\n"
    "s_waitcnt vmcnt(16)\n"
    "v_mfma_f32_16x16x32_bf16 v[208:211], a[48:51], v[160:163], v[208:211]\n"
    "v_mfma_f32_16x16x32_bf16 v[212:215], a[52:55], v[160:163], v[212:215]\n"
    "v_mfma_f32_16x16x32_bf16 v[216:219], a[56:59], v[160:163], v[216:219]\n"
    "buffer_load_dwordx4 a[140:143], v0, s[8:11], s70 offen offset:3072 sc1\n"
    "v_mfma_f32_16x16x32_bf16 v[220:223], a[60:63], v[160:163], v[220:223]\n"
    "v_mfma_f32_16x16x32_bf16 v[44:47], a[60:63], v[164:167], v[44:47]\n"
    "s_add_u32 s70, s79, 36864\n"
    "v_mfma_f32_16x16x32_bf16 v[40:43], a[56:59], v[164:167], v[40:43]\n"
    "s_waitcnt vmcnt(13)\n"
    "v_max3_u32 v7, v176, v177, v178\n"
    "v_max_u32 v7, v7, v179\n"
    "s_waitcnt vmcnt(12)\n"
    "v_max3_u32 v7, v7, v180, v181\n"
    "v_max3_u32 v7, v7, v182, v183\n"
    "v_mfma_f32_16x16x32_bf16 v[36:39], a[52:55], v[164:167], v[36:39]\n"
    "s_waitcnt vmcnt(11)\n"
    "v_max3_u32 v7, v7, v184, v185\n"
    "v_max3_u32 v7, v7, v186, v187\n"
    "v_mfma_f32_16x16x32_bf16 v[32:35], a[48:51], v[164:167], v[32:35]\n"
    "s_waitcnt vmcnt(10)\n"
    "v_max3_u32 v7, v7, v188, v189\n"
    "v_mfma_f32_16x16x32_bf16 v[224:227], a[48:51], v[168:171], v[224:227]\n"
    "buffer_load_dwordx4 a[144:147], v0, s[8:11], s70 offen offset:0 sc1\n"
    "v_mfma_f32_16x16x32_bf16 v[228:231], a[52:55], v[168:171], v[228:231]\n"
    "v_max3_u32 v7, v7, v190, v191\n"
    "v_cmp_eq_u32 s[26:27], -1, v7\n"
    "v_mfma_f32_16x16x32_bf16 v[232:235], a[56:59], v[168:171], v[232:235]\n"
    "s_cmp_lg_u64 s[26:27], 0\n"
    "s_cbranch_scc1 Lgb25\n"
    "Lgo25:\n"
    "v_mfma_f32_16x16x32_bf16 v[236:239], a[60:63], v[168:171], v[236:239]\n"
    "buffer_load_dwordx4 a[148:151], v0, s[8:11], s70 offen offset:1024 sc1\n"
    "v_mfma_f32_16x16x32_bf16 v[252:255], a[60:63], v[172:175], v[252:255]\n"
    "v_mfma_f32_16x16x32_bf16 v[248:251], a[56:59], v[172:175], v[248:251]\n"
    "v_mfma_f32_16x16x32_bf16 v[244:247], a[52:55], v[172:175], v[244:247]\n"
    "v_mfma_f32_16x16x32_bf16 v[240:243], a[48:51], v[172:175], v[240:243]\n"
    "v_cmp_u_f32 s[30:31], v208, v32\n"
    "v_mfma_f32_16x16x32_bf16 v[208:211], a[64:67], v[176:179], v[208:211]\n"
    "buffer_load_dwordx4 a[152:155], v0, s[8:11], s70 offen offset:2048 sc1\n"
    "v_mfma_f32_16x16x32_bf16 v[212:215], a[68:71], v[176:179], v[212:215]\n"
    "v_mfma_f32_16x16x32_bf16 v[216:219], a[72:75], v[176:179], v[216:219]\n"
    "v_mfma_f32_16x16x32_bf16 v[220:223], a[76:79], v[176:179], v[220:223]\n"
    "v_mfma_f32_16x16x32_bf16 v[44:47], a[76:79], v[180:183], v[44:47]\n"
    "buffer_load_dwordx4 a[156:159], v0, s[8:11], s70 offen offset:3072 sc1\n"
    "v_mfma_f32_16x16x32_bf16 v[40:43], a[72:75], v[180:183], v[40:43]\n"
    "s_waitcnt vmcnt(13)\n"
    "v_max3_u32 v7, v192, v193, v194\n"
    "v_max_u32 v7, v7, v195\n"
    "s_waitcnt vmcnt(12)\n"
    "v_max3_u32 v7, v7, v196, v197\n"
    "v_max3_u32 v7, v7, v198, v199\n"
    "v_mfma_f32_16x16x32_bf16 v[36:39], a[68:71], v[180:183], v[36:39]\n"
    "s_waitcnt vmcnt(11)\n"
    "v_max3_u32 v7, v7, v200, v201\n"
    "v_max3_u32 v7, v7, v202, v203\n"
    "v_mfma_f32_16x16x32_bf16 v[32:35], a[64:67], v[180:183], v[32:35]\n"
    "s_waitcnt vmcnt(10)\n"
    "v_max3_u32 v7, v7, v204, v205\n"
    "v_cmp_u_f32 s[24:25], v224, v240\n"
    "v_mfma_f32_16x16x32_bf16 v[224:227], a[64:67], v[184:187], v[224:227]\n"
    "buffer_load_dwordx4 v[112:115], v0, s[8:11], s71 offen offset:3072 sc1\n"
    "v_mfma_f32_16x16x32_bf16 v[228:231], a[68:71], v[184:187], v[228:231]\n"
    "v_max3_u32 v7, v7, v206, v207\n"
    "v_cmp_eq_u32 s[26:27], -1, v7\n"
    "v_mfma_f32_16x16x32_bf16 v[232:235], a[72:75], v[184:187], v[232:235]\n"
    "s_add_u32 s71, s87, 23089152\n"
    "s_cmp_lg_u64 s[26:27], 0\n"
    "s_cbranch_scc1 Lgb26\n"
    "Lgo26:\n"
    "v_mfma_f32_16x16x32_bf16 v[236:239], a[76:79], v[184:187], v[236:239]\n"
    "s_or_b64 s[30:31], s[30:31], s[24:25]\n"
    "s_cbranch_scc1 Lbad32\n"
    "Lok32:\n"
    "v_mfma_f32_16x16x32_bf16 v[252:255], a[76:79], v[188:191], v[252:255]\n"
    "v_mfma_f32_16x16x32_bf16 v[248:251], a[72:75], v[188:191], v[248:251]\n"
    "buffer_load_dwordx4 v[88:91], v0, s[8:11], s71 offen offset:1024 sc1\n"
    "v_mfma_f32_16x16x32_bf16 v[244:247], a[68:71], v[188:191], v[244:247]\n"
    "v_mfma_f32_16x16x32_bf16 v[240:243], a[64:67], v[188:191], v[240:243]\n"
    "v_mfma_f32_16x16x32_bf16 v[208:211], a[80:83], v[192:195], v[208:211]\n"
    "v_mfma_f32_16x16x32_bf16 v[212:215], a[84:87], v[192:195], v[212:215]\n"
    "Lpad6:\n"
    "s_cbranch_execz Lpad10\n"
    "buffer_load_dwordx4 v[92:95], v0, s[8:11], s71 offen offset:3072 sc1\n"
    "v_mfma_f32_16x16x32_bf16 v[216:219], a[88:91], v[192:195], v[216:219]\n"
    "v_mfma_f32_16x16x32_bf16 v[220:223], a[92:95], v[192:195], v[220:223]\n"
    "v_mfma_f32_16x16x32_bf16 v[44:47], a[92:95], v[196:199], v[44:47]\n"
    "v_mfma_f32_16x16x32_bf16 v[40:43], a[88:91], v[196:199], v[40:43]\n"
    "buffer_load_dwordx4 v[96:99], v0, s[8:11], s72 offen offset:2048 sc1\n"
    "s_waitcnt vmcnt(13)\n"
    "v_max3_u32 v7, v48, v49, v50\n"
    "v_mfma_f32_16x16x32_bf16 v[36:39], a[84:87], v[196:199], v[36:39]\n"
    "v_max_u32 v7, v7, v51\n"
    "s_waitcnt vmcnt(12)\n"
    "v_max3_u32 v7, v7, v52, v53\n"
    "v_mfma_f32_16x16x32_bf16 v[32:35], a[80:83], v[196:199], v[32:35]\n"
    "s_add_u32 s72, s88, 23089152\n"
    "v_max3_u32 v7, v7, v54, v55\n"
    "v_mfma_f32_16x16x32_bf16 v[224:227], a[80:83], v[200:203], v[224:227]\n"
    "s_waitcnt vmcnt(11)\n"
    "v_max3_u32 v7, v7, v56, v57\n"
    "v_max3_u32 v7, v7, v58, v59\n"
    "v_mfma_f32_16x16x32_bf16 v[228:231], a[84:87], v[200:203], v[228:231]\n"
    "s_waitcnt vmcnt(10)\n"
    "v_max3_u32 v7, v7, v60, v61\n"
    "v_max3_u32 v7, v7, v62, v63\n"
    "v_mfma_f32_16x16x32_bf16 v[232:235], a[88:91], v[200:203], v[232:235]\n"
    "buffer_load_dwordx4 v[100:103], v0, s[8:11], s72 offen offset:0 sc1\n"
    "v_cmp_eq_u32 s[26:27], -1, v7\n"
    "v_mfma_f32_16x16x32_bf16 v[236:239], a[92:95], v[200:203], v[236:239]\n"
    "s_cmp_lg_u64 s[26:27], 0\n"
    "s_cbranch_scc1 Lgb27\n"
    "Lgo27:\n"
    "v_mfma_f32_16x16x32_bf16 v[252:255], a[92:95], v[204:207], v[252:255]\n"
    "v_mfma_f32_16x16x32_bf16 v[248:251], a[88:91], v[204:207], v[248:251]\n"
    "v_mfma_f32_16x16x32_bf16 v[244:247], a[84:87], v[204:207], v[244:247]\n"
    "buffer_load_dwordx4 v[108:111], v0, s[8:11], s72 offen offset:2048 sc1\n"
    "v_mfma_f32_16x16x32_bf16 v[240:243], a[80:83], v[204:207], v[240:243]\n"
    "v_mfma_f32_16x16x32_bf16 v[208:211], a[96:99], v[48:51], v[208:211]\n"
    "s_add_u32 s72, s88, 23085056\n"
    "v_mfma_f32_16x16x32_bf16 v[212:215], a[100:103], v[48:51], v[212:215]\n"
    "v_mfma_f32_16x16x32_bf16 v[216:219], a[104:107], v[48:51], v[216:219]\n"
    "v_mfma_f32_16x16x32_bf16 v[220:223], a[108:111], v[48:51], v[220:223]\n"
    "buffer_load_dwordx4 v[116:119], v0, s[8:11], s72 offen offset:3072 sc1\n"
    "v_mfma_f32_16x16x32_bf16 v[44:47], a[108:111], v[52:55], v[44:47]\n"
    "s_add_u32 s72, s88, 23089152\n"
    "v_mfma_f32_16x16x32_bf16 v[40:43], a[104:107], v[52:55], v[40:43]\n"
    "v_mfma_f32_16x16x32_bf16 v[36:39], a[100:103], v[52:55], v[36:39]\n"
    "s_waitcnt vmcnt(6)\n"
    "v_max3_u32 v7, v112, v113, v114\n"
    "v_max_u32 v7, v7, v115\n"
    "s_waitcnt vmcnt(5)\n"
    "v_max3_u32 v7, v7, v88, v89\n"
    "v_mfma_f32_16x16x32_bf16 v[32:35], a[96:99], v[52:55], v[32:35]\n"
    "buffer_load_dwordx4 v[120:123], v0, s[8:11], s72 offen offset:1024 sc1\n"
    "v_mfma_f32_16x16x32_bf16 v[224:227], a[96:99], v[56:59], v[224:227]\n"
    "v_max3_u32 v7, v7, v90, v91\n"
    "s_waitcnt vmcnt(5)\n"
    "v_max3_u32 v7, v7, v92, v93\n"
    "v_mfma_f32_16x16x32_bf16 v[228:231], a[100:103], v[56:59], v[228:231]\n"
    "v_max3_u32 v7, v7, v94, v95\n"
    "v_cmp_eq_u32 s[26:27], -1, v7\n"
    "v_mfma_f32_16x16x32_bf16 v[232:235], a[104:107], v[56:59], v[232:235]\n"
    "s_cmp_lg_u64 s[26:27], 0\n"
    "s_cbranch_scc1 Lgb28\n"
    "Lgo28:\n"
    "v_mfma_f32_16x16x32_bf16 v[236:239], a[108:111], v[56:59], v[236:239]\n"
    "buffer_load_dwordx4 v[124:127], v0, s[8:11], s72 offen offset:3072 sc1\n"
    "v_mfma_f32_16x16x32_bf16 v[252:255], a[108:111], v[60:63], v[252:255]\n"
    "v_mfma_f32_16x16x32_bf16 v[248:251], a[104:107], v[60:63], v[248:251]\n"
    "s_add_u32 s71, s87, 23085056\n"
    "v_mfma_f32_16x16x32_bf16 v[244:247], a[100:103], v[60:63], v[244:247]\n"
    "v_mfma_f32_16x16x32_bf16 v[240:243], a[96:99], v[60:63], v[240:243]\n"
    "v_mfma_f32_16x16x32_bf16 v[32:35], a[112:115], v[112:115], v[32:35]\n"
    "buffer_load_dwordx4 v[80:83], v0, s[8:11], s71 offen offset:2048 sc1\n"
    "v_mfma_f32_16x16x32_bf16 v[36:39], a[116:119], v[112:115], v[36:39]\n"
    "s_add_u32 s71, s87, 23089152\n"
    "v_mfma_f32_16x16x32_bf16 v[40:43], a[120:123], v[112:115], v[40:43]\n"
    "s_waitcnt vmcnt(6)\n"
    "v_max3_u32 v7, v96, v97, v98\n"
    "v_max_u32 v7, v7, v99\n"
    "s_waitcnt vmcnt(5)\n"
    "v_max3_u32 v7, v7, v100, v101\n"
    "v_max3_u32 v7, v7, v102, v103\n"
    "v_mfma_f32_16x16x32_bf16 v[44:47], a[124:127], v[112:115], v[44:47]\n"
    "s_waitcnt vmcnt(4)\n"
    "v_max3_u32 v7, v7, v108, v109\n"
    "v_max3_u32 v7, v7, v110, v111\n"
    "v_mfma_f32_16x16x32_bf16 v[44:47], a[140:143], v[88:91], v[44:47]\n"
    "buffer_load_dwordx4 v[84:87], v0, s[8:11], s71 offen offset:0 sc1\n"
    "v_mfma_f32_16x16x32_bf16 v[40:43], a[136:139], v[88:91], v[40:43]\n"
    "v_cmp_eq_u32 s[26:27], -1, v7\n"
    "v_mfma_f32_16x16x32_bf16 v[36:39], a[132:135], v[88:91], v[36:39]\n"
    "s_cmp_lg_u64 s[26:27], 0\n"
    "s_cbranch_scc1 Lgb29\n"
    "Lgo29:\n"
    "v_mfma_f32_16x16x32_bf16 v[32:35], a[128:131], v[88:91], v[32:35]\n"
    "v_mfma_f32_16x16x32_bf16 v[32:35], a[144:147], v[92:95], v[32:35]\n"
    "v_mfma_f32_16x16x32_bf16 v[36:39], a[148:151], v[92:95], v[36:39]\n"
    "buffer_load_dwordx4 v[104:107], v0, s[8:11], s71 offen offset:2048 sc1\n"
    "v_mfma_f32_16x16x32_bf16 v[40:43], a[152:155], v[92:95], v[40:43]\n"
    "s_add_u32 m0, s66, 32768\n"
    "s_add_u32 s69, s75, 32768\n"
    "v_mfma_f32_16x16x32_bf16 v[44:47], a[156:159], v[92:95], v[44:47]\n"
    "v_mfma_f32_16x16x32_bf16 v[224:227], a[112:115], v[96:99], v[224:227]\n"
    "s_nop 0\n"
    "ds_write_b128 v11, v[32:35] offset:0\n"
    "v_mfma_f32_16x16x32_bf16 v[228:231], a[116:119], v[96:99], v[228:231]\n"
    "buffer_load_dwordx4 v0, s[8:11], s69 offen offset:0 sc1 lds\n"
    "v_mfma_f32_16x16x32_bf16 v[232:235], a[120:123], v[96:99], v[232:235]\n"
    "s_waitcnt vmcnt(6)\n"
    "v_max3_u32 v7, v116, v117, v118\n"
    "v_max_u32 v7, v7, v119\n"
    "s_waitcnt vmcnt(5)\n"
    "v_max3_u32 v7, v7, v120, v121\n"
    "v_mfma_f32_16x16x32_bf16 v[236:239], a[124:127], v[96:99], v[236:239]\n"
    "ds_write_b128 v11, v[36:39] offset:1024\n"
    "v_mfma_f32_16x16x32_bf16 v[236:239], a[140:143], v[100:103], v[236:239]\n"
    "v_max3_u32 v7, v7, v122, v123\n"
    "s_waitcnt vmcnt(4)\n"
    "v_max3_u32 v7, v7, v124, v125\n"
    "v_mfma_f32_16x16x32_bf16 v[232:235], a[136:139], v[100:103], v[232:235]\n"
    "buffer_load_dwordx4 v0, s[8:11], s69 offen offset:1024 sc1 lds\n"
    "v_mfma_f32_16x16x32_bf16 v[228:231], a[132:135], v[100:103], v[228:231]\n"
    "ds_write_b128 v11, v[40:43] offset:2048\n"
    "v_max3_u32 v7, v7, v126, v127\n"
    "v_mfma_f32_16x16x32_bf16 v[224:227], a[128:131], v[100:103], v[224:227]\n"
    "v_cmp_eq_u32 s[26:27], -1, v7\n"
    "v_mfma_f32_16x16x32_bf16 v[224:227], a[144:147], v[108:111], v[224:227]\n"
    "s_cmp_lg_u64 s[26:27], 0\n"
    "s_cbranch_scc1 Lgb30\n"
    "Lgo30:\n"
    "v_mfma_f32_16x16x32_bf16 v[228:231], a[148:151], v[108:111], v[228:231]\n"
    "ds_write_b128 v11, v[44:47] offset:3072\n"
    "v_mfma_f32_16x16x32_bf16 v[232:235], a[152:155], v[108:111], v[232:235]\n"
    "buffer_load_dwordx4 v0, s[8:11], s69 offen offset:2048 sc1 lds\n"
    "v_mfma_f32_16x16x32_bf16 v[236:239], a[156:159], v[108:111], v[236:239]\n"
    "v_mfma_f32_16x16x32_bf16 v[240:243], a[112:115], v[116:119], v[240:243]\n"
    "ds_write_b128 v12, v[224:227] offset:0\n"
    "v_mfma_f32_16x16x32_bf16 v[244:247], a[116:119], v[116:119], v[244:247]\n"
    "v_mfma_f32_16x16x32_bf16 v[248:251], a[120:123], v[116:119], v[248:251]\n"
    "buffer_load_dwordx4 v0, s[8:11], s69 offen offset:3072 sc1 lds\n"
    "s_waitcnt vmcnt(6)\n"
    "v_max3_u32 v7, v80, v81, v82\n"
    "v_mfma_f32_16x16x32_bf16 v[252:255], a[124:127], v[116:119], v[252:255]\n"
    "ds_write_b128 v12, v[228:231] offset:1024\n"
    "v_mfma_f32_16x16x32_bf16 v[252:255], a[140:143], v[120:123], v[252:255]\n"
    "s_add_u32 m0, s66, 0\n"
    "s_add_u32 s69, s75, 0\n"
    "v_mfma_f32_16x16x32_bf16 v[248:251], a[136:139], v[120:123], v[248:251]\n"
    "v_max_u32 v7, v7, v83\n"
    "s_waitcnt vmcnt(5)\n"
    "v_max3_u32 v7, v7, v84, v85\n"
    "v_mfma_f32_16x16x32_bf16 v[244:247], a[132:135], v[120:123], v[244:247]\n"
    "ds_write_b128 v12, v[232:235] offset:2048\n"
    "v_mfma_f32_16x16x32_bf16 v[240:243], a[128:131], v[120:123], v[240:243]\n"
    "buffer_load_dwordx4 v0, s[8:11], s69 offen offset:0 sc1 lds\n"
    "v_mfma_f32_16x16x32_bf16 v[240:243], a[144:147], v[124:127], v[240:243]\n"
    "v_max3_u32 v7, v7, v86, v87\n"
    "s_waitcnt vmcnt(5)\n"
    "v_max3_u32 v7, v7, v104, v105\n"
    "v_mfma_f32_16x16x32_bf16 v[244:247], a[148:151], v[124:127], v[244:247]\n"
    "ds_write_b128 v12, v[236:239] offset:3072\n"
    "v_mfma_f32_16x16x32_bf16 v[248:251], a[152:155], v[124:127], v[248:251]\n"
    "v_max3_u32 v7, v7, v106, v107\n"
    "v_cmp_eq_u32 s[26:27], -1, v7\n"
    "v_mfma_f32_16x16x32_bf16 v[252:255], a[156:159], v[124:127], v[252:255]\n"
    "buffer_load_dwordx4 v0, s[8:11], s69 offen offset:1024 sc1 lds\n"
    "s_cmp_lg_u64 s[26:27], 0\n"
    "s_cbranch_scc1 Lgb31\n"
    "Lgo31:\n"
    "v_mfma_f32_16x16x32_bf16 v[208:211], a[112:115], v[80:83], v[208:211]\n"
    "ds_write_b128 v13, v[240:243] offset:0\n"
    "v_mfma_f32_16x16x32_bf16 v[212:215], a[116:119], v[80:83], v[212:215]\n"
    "v_mfma_f32_16x16x32_bf16 v[216:219], a[120:123], v[80:83], v[216:219]\n"
    "v_mfma_f32_16x16x32_bf16 v[220:223], a[124:127], v[80:83], v[220:223]\n"
    "ds_write_b128 v13, v[244:247] offset:1024\n"
    "v_mfma_f32_16x16x32_bf16 v[220:223], a[140:143], v[84:87], v[220:223]\n"
    "buffer_load_dwordx4 v0, s[8:11], s69 offen offset:2048 sc1 lds\n"
    "v_mfma_f32_16x16x32_bf16 v[216:219], a[136:139], v[84:87], v[216:219]\n"
    "v_mfma_f32_16x16x32_bf16 v[212:215], a[132:135], v[84:87], v[212:215]\n"
    "ds_write_b128 v13, v[248:251] offset:2048\n"
    "v_mfma_f32_16x16x32_bf16 v[208:211], a[128:131], v[84:87], v[208:211]\n"
    "v_mfma_f32_16x16x32_bf16 v[208:211], a[144:147], v[104:107], v[208:211]\n"
    "buffer_load_dwordx4 v0, s[8:11], s69 offen offset:3072 sc1 lds\n"
    "v_mfma_f32_16x16x32_bf16 v[212:215], a[148:151], v[104:107], v[212:215]\n"
    "ds_write_b128 v13, v[252:255] offset:3072\n"
    "v_mfma_f32_16x16x32_bf16 v[216:219], a[152:155], v[104:107], v[216:219]\n"
    "s_add_u32 m0, s66, 4096\n"
    "s_add_u32 s69, s75, 4096\n"
    "v_mfma_f32_16x16x32_bf16 v[220:223], a[156:159], v[104:107], v[220:223]\n"
    "buffer_load_dwordx4 v0, s[8:11], s69 offen offset:0 sc1 lds\n"
    "buffer_load_dwordx4 v0, s[8:11], s69 offen offset:1024 sc1 lds\n"
    "buffer_load_dwordx4 v0, s[8:11], s69 offen offset:2048 sc1 lds\n"
    "buffer_load_dwordx4 v0, s[8:11], s69 offen offset:3072 sc1 lds\n"
    "s_mov_b32 s65, 266240\n"
    "buffer_atomic_add a255, v3, s[8:11], s65 offen\n"
    "s_waitcnt lgkmcnt(0)\n"
    "s_nop 0\n"
    "s_barrier\n"
    "ds_read_b128 v[16:19], v10 offset:0\n"
    "ds_read_b128 v[20:23], v10 offset:1024\n"
    "ds_read_b128 v[24:27], v10 offset:2048\n"
    "ds_read_b128 v[28:31], v10 offset:3072\n"
    "ds_read_b128 v[64:67], v10 offset:4096\n"
    "ds_read_b128 v[68:71], v10 offset:5120\n"
    "ds_read_b128 v[72:75], v10 offset:6144\n"
    "ds_read_b128 v[76:79], v10 offset:7168\n"
    "ds_read_b128 v[48:51], v10 offset:8192\n"
    "ds_read_b128 v[52:55], v10 offset:9216\n"
    "ds_read_b128 v[56:59], v10 offset:10240\n"
    "ds_read_b128 v[60:63], v10 offset:11264\n"
    "s_waitcnt lgkmcnt(11)\n"
    "v_pk_add_f32 v[208:209], v[208:209], v[16:17] op_sel:[0,0] op_sel_hi:[1,1]\n"
    "v_pk_add_f32 v[210:211], v[210:211], v[18:19] op_sel:[0,0] op_sel_hi:[1,1]\n"
    "s_waitcnt lgkmcnt(10)\n"
    "v_pk_add_f32 v[212:213], v[212:213], v[20:21] op_sel:[0,0] op_sel_hi:[1,1]\n"
    "v_pk_add_f32 v[214:215], v[214:215], v[22:23] op_sel:[0,0] op_sel_hi:[1,1]\n"
    "s_waitcnt lgkmcnt(9)\n"
    "v_pk_add_f32 v[216:217], v[216:217], v[24:25] op_sel:[0,0] op_sel_hi:[1,1]\n"
    "v_pk_add_f32 v[218:219], v[218:219], v[26:27] op_sel:[0,0] op_sel_hi:[1,1]\n"
    "s_waitcnt lgkmcnt(8)\n"
    "v_pk_add_f32 v[220:221], v[220:221], v[28:29] op_sel:[0,0] op_sel_hi:[1,1]\n"
    "v_pk_add_f32 v[222:223], v[222:223], v[30:31] op_sel:[0,0] op_sel_hi:[1,1]\n"
    "s_waitcnt lgkmcnt(7)\n"
    "v_pk_add_f32 v[208:209], v[208:209], v[64:65] op_sel:[0,0] op_sel_hi:[1,1]\n"
    "v_pk_add_f32 v[210:211], v[210:211], v[66:67] op_sel:[0,0] op_sel_hi:[1,1]\n"
    "s_waitcnt lgkmcnt(6)\n"
    "v_pk_add_f32 v[212:213], v[212:213], v[68:69] op_sel:[0,0] op_sel_hi:[1,1]\n"
    "v_pk_add_f32 v[214:215], v[214:215], v[70:71] op_sel:[0,0] op_sel_hi:[1,1]\n"
    "s_waitcnt lgkmcnt(5)\n"
    "v_pk_add_f32 v[216:217], v[216:217], v[72:73] op_sel:[0,0] op_sel_hi:[1,1]\n"
    "v_pk_add_f32 v[218:219], v[218:219], v[74:75] op_sel:[0,0] op_sel_hi:[1,1]\n"
    "s_waitcnt lgkmcnt(4)\n"
    "v_pk_add_f32 v[220:221], v[220:221], v[76:77] op_sel:[0,0] op_sel_hi:[1,1]\n"
    "v_pk_add_f32 v[222:223], v[222:223], v[78:79] op_sel:[0,0] op_sel_hi:[1,1]\n"
    "s_waitcnt lgkmcnt(3)\n"
    "v_pk_add_f32 v[208:209], v[208:209], v[48:49] op_sel:[0,0] op_sel_hi:[1,1]\n"
    "v_pk_add_f32 v[210:211], v[210:211], v[50:51] op_sel:[0,0] op_sel_hi:[1,1]\n"
    "s_waitcnt lgkmcnt(2)\n"
    "v_pk_add_f32 v[212:213], v[212:213], v[52:53] op_sel:[0,0] op_sel_hi:[1,1]\n"
    "v_pk_add_f32 v[214:215], v[214:215], v[54:55] op_sel:[0,0] op_sel_hi:[1,1]\n"
    "s_waitcnt lgkmcnt(1)\n"
    "v_pk_add_f32 v[216:217], v[216:217], v[56:57] op_sel:[0,0] op_sel_hi:[1,1]\n"
    "v_pk_add_f32 v[218:219], v[218:219], v[58:59] op_sel:[0,0] op_sel_hi:[1,1]\n"
    "s_waitcnt lgkmcnt(0)\n"
    "v_pk_add_f32 v[220:221], v[220:221], v[60:61] op_sel:[0,0] op_sel_hi:[1,1]\n"
    "v_pk_add_f32 v[222:223], v[222:223], v[62:63] op_sel:[0,0] op_sel_hi:[1,1]\n"
    "v_cvt_pk_bf16_f32 v80, v208, v209\n"
    "v_cvt_pk_bf16_f32 v81, v210, v211\n"
    "v_cvt_pk_bf16_f32 v82, v212, v213\n"
    "v_cvt_pk_bf16_f32 v83, v214, v215\n"
    "v_cvt_pk_bf16_f32 v84, v216, v217\n"
    "v_cvt_pk_bf16_f32 v85, v218, v219\n"
    "v_cvt_pk_bf16_f32 v86, v220, v221\n"
    "v_cvt_pk_bf16_f32 v87, v222, v223\n"
    "s_add_u32 s73, s81, 14422016\n"
    "buffer_store_dwordx4 v[80:83], v0, s[8:11], s73 offen sc1\n"
    "buffer_store_dwordx4 v[84:87], v0, s[8:11], s73 offen offset:2048 sc1\n"
    "s_add_u32 s68, s94, 262144\n"
    "v_mov_b32_e64 v8, 0\n"
    "buffer_atomic_add v8, v6, s[8:11], s68 offen sc0\n"
    "v_mov_b32 v88, 0x3f803f80\n"
    "s_add_u32 m0, s66, 36864\n"
    "s_add_u32 s69, s75, 36864\n"
    "buffer_load_dwordx4 v0, s[8:11], s69 offen offset:0 sc1 lds\n"
    "v_mov_b32 v92, 0\n"
    "v_mov_b32 v93, 0\n"
    "v_dot2c_f32_bf16 v92, v88, v80\n"
    "v_dot2c_f32_bf16 v93, v80, v80\n"
    "buffer_load_dwordx4 v0, s[8:11], s69 offen offset:1024 sc1 lds\n"
    "v_dot2c_f32_bf16 v92, v88, v81\n"
    "v_dot2c_f32_bf16 v93, v81, v81\n"
    "v_dot2c_f32_bf16 v92, v88, v82\n"
    "v_dot2c_f32_bf16 v93, v82, v82\n"
    "buffer_load_dwordx4 v0, s[8:11], s69 offen offset:2048 sc1 lds\n"
    "v_dot2c_f32_bf16 v92, v88, v83\n"
    "v_dot2c_f32_bf16 v93, v83, v83\n"
    "v_dot2c_f32_bf16 v92, v88, v84\n"
    "v_dot2c_f32_bf16 v93, v84, v84\n"
    "buffer_load_dwordx4 v0, s[8:11], s69 offen offset:3072 sc1 lds\n"
    "v_dot2c_f32_bf16 v92, v88, v85\n"
    "v_dot2c_f32_bf16 v93, v85, v85\n"
    "v_dot2c_f32_bf16 v92, v88, v86\n"
    "v_dot2c_f32_bf16 v93, v86, v86\n"
    "v_dot2c_f32_bf16 v92, v88, v87\n"
    "v_dot2c_f32_bf16 v93, v87, v87\n"
    "v_mul_u32_u24 v7, 17, v0\n"
    "v_cmp_gt_u32 vcc, 0x100, v0\n"
    "s_nop 0\n"
    "v_permlane32_swap_b32 v92, v93\n"
    "v_add_f32 v92, v92, v93\n"
    "v_mov_b32 v93, v92\n"
    "v_bfe_u32 v7, v7, 7, 4\n"
    "v_lshlrev_b32 v7, 3, v7\n"
    "v_permlane16_swap_b32 v92, v93\n"
    "v_add_f32 v92, v92, v93\n"
    "v_mov_b32 v93, v92\n"
    "v_cndmask_b32 v7, 2.0, v7, vcc\n"
    "s_add_u32 s73, s82, 14422016\n"
    "v_permlane32_swap_b32 v92, v93\n"
    "buffer_store_dwordx2 v[92:93], v7, s[8:11], s73 offen sc1\n"
    "s_barrier\n"
    "v_accvgpr_read_b32 v15, a252\n"
    "v_lshrrev_b32 v10, 1, v0\n"
    "v_and_b32 v11, 0xe0, v0\n"
    "v_cmp_eq_u32 vcc, 0, v11\n"
    "v_lshrrev_b32 v11, 4, v0\n"
    "v_and_b32 v11, 0x31, v11\n"
    "v_mul_u32_u24 v11, 65, v11\n"
    "v_and_b32 v11, 0x70, v11\n"
    "v_cndmask_b32 v11, 2.0, v11, vcc\n"
    "s_lshl_b32 s62, s59, 9\n"
    "s_add_u32 s62, s62, s90\n"
    "s_lshr_b32 s63, s62, 2\n"
    "s_add_u32 s62, s62, 1183744\n"
    "s_add_u32 s63, s63, 1839104\n"
    "buffer_store_dwordx2 a[236:237], v10, s[8:11], s62 offen sc1\n"
    "v_accvgpr_write_b32 a220, v208\n"
    "v_accvgpr_write_b32 a221, v209\n"
    "v_accvgpr_write_b32 a222, v210\n"
    "v_accvgpr_write_b32 a223, v211\n"
    "v_accvgpr_write_b32 a224, v212\n"
    "v_accvgpr_write_b32 a225, v213\n"
    "v_accvgpr_write_b32 a226, v214\n"
    "v_accvgpr_write_b32 a227, v215\n"
    "buffer_store_dwordx4 a[236:239], v11, s[8:11], s63 offen sc1\n"
    "v_accvgpr_write_b32 a228, v216\n"
    "v_accvgpr_write_b32 a229, v217\n"
    "v_accvgpr_write_b32 a230, v218\n"
    "v_accvgpr_write_b32 a231, v219\n"
    "v_accvgpr_write_b32 a232, v220\n"
    "v_accvgpr_write_b32 a233, v221\n"
    "v_accvgpr_write_b32 a234, v222\n"
    "v_accvgpr_write_b32 a235, v223\n"
    "s_waitcnt vmcnt(7)\n"
    "v_readfirstlane_b32 s29, v8\n"
    "s_nop 0\n"
    "s_cmp_lt_u32 s29, s95\n"
    "s_cbranch_scc1 Lslow34\n"
    "Lback34:\n"
    "s_barrier\n"
    "s_add_u32 s71, s80, 14422016\n"
    "buffer_load_dwordx4 v[128:131], v0, s[8:11], s71 offen offset:0 sc1\n"
    "buffer_load_dwordx4 v[132:135], v0, s[8:11], s71 offen offset:1024 sc1\n"
    "buffer_load_dwordx4 v[136:139], v0, s[8:11], s71 offen offset:2048 sc1\n"
    "buffer_load_dwordx4 v[140:143], v0, s[8:11], s71 offen offset:3072 sc1\n"
    "s_add_u32 s71, s80, 14426112\n"
    "buffer_load_dwordx4 v[144:147], v0, s[8:11], s71 offen offset:0 sc1\n"
    "buffer_load_dwordx4 v[148:151], v0, s[8:11], s71 offen offset:1024 sc1\n"
    "buffer_load_dwordx4 v[152:155], v0, s[8:11], s71 offen offset:2048 sc1\n"
    "buffer_load_dwordx4 v[156:159], v0, s[8:11], s71 offen offset:3072 sc1\n"
    "s_add_u32 s71, s80, 14430208\n"
    "buffer_load_dwordx4 v[160:163], v0, s[8:11], s71 offen offset:0 sc1\n"
    "buffer_load_dwordx4 v[164:167], v0, s[8:11], s71 offen offset:1024 sc1\n"
    "s_waitcnt vmcnt(13)\n"
    "s_barrier\n"
    "Lredo37:\n"
    "ds_read_b128 v[80:83], v0 offset:0\n"
    "ds_read_b128 v[84:87], v0 offset:1024\n"
    "ds_read_b128 v[88:91], v0 offset:32768\n"
    "ds_read_b128 v[92:95], v0 offset:33792\n"
    "ds_read_b128 v[96:99], v0 offset:2048\n"
    "ds_read_b128 v[64:67], v0 offset:3072\n"
    "ds_read_b128 v[68:71], v0 offset:34816\n"
    "ds_read_b128 v[72:75], v0 offset:35840\n"
    "buffer_load_dwordx4 v[168:171], v0, s[8:11], s71 offen offset:2048 sc1\n"
    "buffer_load_dwordx4 v[172:175], v0, s[8:11], s71 offen offset:3072 sc1\n"
    "s_waitcnt vmcnt(11)\n"
    "v_max3_u32 v7, v128, v129, v130\n"
    "v_max_u32 v7, v7, v131\n"
    "s_waitcnt vmcnt(10)\n"
    "v_max3_u32 v7, v7, v132, v133\n"
    "v_max3_u32 v7, v7, v134, v135\n"
    "v_cmp_eq_u32 s[26:27], -1, v7\n"
    "s_nop 0\n"
    "s_cmp_lg_u64 s[26:27], 0\n"
    "s_cbranch_scc1 Lgb39\n"
    "Lgo39:\n"
    "s_waitcnt lgkmcnt(4)\n"
    "v_mfma_f32_16x16x32_bf16 v[16:19], v[80:83], v[128:131], 0\n"
    "ds_read_b128 v[76:79], v0 offset:4096\n"
    "s_add_u32 s71, s80, 14434304\n"
    "v_mfma_f32_16x16x32_bf16 v[20:23], v[80:83], v[132:135], 0\n"
    "buffer_load_dwordx4 v[176:179], v0, s[8:11], s71 offen offset:0 sc1\n"
    "v_mfma_f32_16x16x32_bf16 v[28:31], v[84:87], v[132:135], 0\n"
    "ds_read_b128 v[80:83], v0 offset:5120\n"
    "s_waitcnt vmcnt(10)\n"
    "v_max3_u32 v7, v136, v137, v138\n"
    "v_mfma_f32_16x16x32_bf16 v[24:27], v[84:87], v[128:131], 0\n"
    "buffer_load_dwordx4 v[180:183], v0, s[8:11], s71 offen offset:1024 sc1\n"
    "v_mfma_f32_16x16x32_bf16 v[32:35], v[88:91], v[128:131], 0\n"
    "ds_read_b128 v[84:87], v0 offset:36864\n"
    "v_max_u32 v7, v7, v139\n"
    "v_mfma_f32_16x16x32_bf16 v[40:43], v[88:91], v[132:135], 0\n"
    "s_waitcnt vmcnt(10)\n"
    "v_max3_u32 v7, v7, v140, v141\n"
    "v_max3_u32 v7, v7, v142, v143\n"
    "v_mfma_f32_16x16x32_bf16 v[44:47], v[92:95], v[132:135], 0\n"
    "ds_read_b128 v[88:91], v0 offset:37888\n"
    "v_cmp_eq_u32 s[26:27], -1, v7\n"
    "v_mfma_f32_16x16x32_bf16 v[36:39], v[92:95], v[128:131], 0\n"
    "s_cmp_lg_u64 s[26:27], 0\n"
    "s_cbranch_scc1 Lgb40\n"
    "Lgo40:\n"
    "s_waitcnt lgkmcnt(4)\n"
    "v_mfma_f32_16x16x32_bf16 v[16:19], v[96:99], v[136:139], v[16:19]\n"
    "ds_read_b128 v[92:95], v0 offset:6144\n"
    "v_mfma_f32_16x16x32_bf16 v[20:23], v[96:99], v[140:143], v[20:23]\n"
    "buffer_load_dwordx4 v[184:187], v0, s[8:11], s71 offen offset:2048 sc1\n"
    "v_mfma_f32_16x16x32_bf16 v[28:31], v[64:67], v[140:143], v[28:31]\n"
    "ds_read_b128 v[96:99], v0 offset:7168\n"
    "v_mfma_f32_16x16x32_bf16 v[24:27], v[64:67], v[136:139], v[24:27]\n"
    "buffer_load_dwordx4 v[188:191], v0, s[8:11], s71 offen offset:3072 sc1\n"
    "v_mfma_f32_16x16x32_bf16 v[32:35], v[68:71], v[136:139], v[32:35]\n"
    "ds_read_b128 v[64:67], v0 offset:38912\n"
    "s_add_u32 s72, s80, 14454784\n"
    "v_mfma_f32_16x16x32_bf16 v[40:43], v[68:71], v[140:143], v[40:43]\n"
    "v_mfma_f32_16x16x32_bf16 v[44:47], v[72:75], v[140:143], v[44:47]\n"
    "ds_read_b128 v[68:71], v0 offset:39936\n"
    "v_mfma_f32_16x16x32_bf16 v[36:39], v[72:75], v[136:139], v[36:39]\n"
    "s_waitcnt vmcnt(10) lgkmcnt(4)\n"
    "v_mfma_f32_16x16x32_bf16 v[16:19], v[76:79], v[144:147], v[16:19]\n"
    "ds_read_b128 v[72:75], v0 offset:8192\n"
    "buffer_load_dwordx4 v[100:103], v0, s[8:11], s72 offen offset:0 sc1\n"
    "v_mfma_f32_16x16x32_bf16 v[20:23], v[76:79], v[148:151], v[20:23]\n"
    "s_add_u32 s71, s80, 14438400\n"
    "v_mfma_f32_16x16x32_bf16 v[28:31], v[80:83], v[148:151], v[28:31]\n"
    "ds_read_b128 v[76:79], v0 offset:9216\n"
    "v_mfma_f32_16x16x32_bf16 v[24:27], v[80:83], v[144:147], v[24:27]\n"
    "buffer_load_dwordx4 v[192:195], v0, s[8:11], s71 offen offset:0 sc1\n"
    "v_mfma_f32_16x16x32_bf16 v[32:35], v[84:87], v[144:147], v[32:35]\n"
    "ds_read_b128 v[80:83], v0 offset:40960\n"
    "v_mfma_f32_16x16x32_bf16 v[40:43], v[84:87], v[148:151], v[40:43]\n"
    "buffer_load_dwordx4 v[196:199], v0, s[8:11], s71 offen offset:1024 sc1\n"
    "v_mfma_f32_16x16x32_bf16 v[44:47], v[88:91], v[148:151], v[44:47]\n"
    "ds_read_b128 v[84:87], v0 offset:41984\n"
    "v_mfma_f32_16x16x32_bf16 v[36:39], v[88:91], v[144:147], v[36:39]\n"
    "s_waitcnt vmcnt(11) lgkmcnt(4)\n"
    "v_mfma_f32_16x16x32_bf16 v[16:19], v[92:95], v[152:155], v[16:19]\n"
    "ds_read_b128 v[88:91], v0 offset:10240\n"
    "v_mfma_f32_16x16x32_bf16 v[20:23], v[92:95], v[156:159], v[20:23]\n"
    "buffer_load_dwordx4 v[104:107], v0, s[8:11], s72 offen offset:1024 sc1\n"
    "v_mfma_f32_16x16x32_bf16 v[28:31], v[96:99], v[156:159], v[28:31]\n"
    "ds_read_b128 v[92:95], v0 offset:11264\n"
    "v_mfma_f32_16x16x32_bf16 v[24:27], v[96:99], v[152:155], v[24:27]\n"
    "v_mfma_f32_16x16x32_bf16 v[32:35], v[64:67], v[152:155], v[32:35]\n"
    "ds_read_b128 v[96:99], v0 offset:43008\n"
    "v_mfma_f32_16x16x32_bf16 v[40:43], v[64:67], v[156:159], v[40:43]\n"
    "buffer_load_dwordx4 v[200:203], v0, s[8:11], s71 offen offset:2048 sc1\n"
    "v_mfma_f32_16x16x32_bf16 v[44:47], v[68:71], v[156:159], v[44:47]\n"
    "ds_read_b128 v[64:67], v0 offset:44032\n"
    "v_mfma_f32_16x16x32_bf16 v[36:39], v[68:71], v[152:155], v[36:39]\n"
    "s_waitcnt vmcnt(11) lgkmcnt(4)\n"
    "v_mfma_f32_16x16x32_bf16 v[16:19], v[72:75], v[160:163], v[16:19]\n"
    "ds_read_b128 v[68:71], v0 offset:12288\n"
    "v_mfma_f32_16x16x32_bf16 v[20:23], v[72:75], v[164:167], v[20:23]\n"
    "buffer_load_dwordx4 v[204:207], v0, s[8:11], s71 offen offset:3072 sc1\n"
    "v_mfma_f32_16x16x32_bf16 v[28:31], v[76:79], v[164:167], v[28:31]\n"
    "ds_read_b128 v[72:75], v0 offset:13312\n"
    "s_add_u32 s71, s80, 14442496\n"
    "v_mfma_f32_16x16x32_bf16 v[24:27], v[76:79], v[160:163], v[24:27]\n"
    "buffer_load_dwordx4 v[208:211], v0, s[8:11], s71 offen offset:0 sc1\n"
    "v_mfma_f32_16x16x32_bf16 v[32:35], v[80:83], v[160:163], v[32:35]\n"
    "ds_read_b128 v[76:79], v0 offset:45056\n"
    "v_mfma_f32_16x16x32_bf16 v[40:43], v[80:83], v[164:167], v[40:43]\n"
    "v_mfma_f32_16x16x32_bf16 v[44:47], v[84:87], v[164:167], v[44:47]\n"
    "ds_read_b128 v[80:83], v0 offset:46080\n"
    "v_mfma_f32_16x16x32_bf16 v[36:39], v[84:87], v[160:163], v[36:39]\n"
    "v_cmp_u_f32 s[30:31], v16, v20\n"
    "s_waitcnt vmcnt(11) lgkmcnt(4)\n"
    "v_mfma_f32_16x16x32_bf16 v[16:19], v[88:91], v[168:171], v[16:19]\n"
    "ds_read_b128 v[84:87], v0 offset:14336\n"
    "v_mfma_f32_16x16x32_bf16 v[20:23], v[88:91], v[172:175], v[20:23]\n"
    "s_or_b64 s[30:31], s[30:31], s[30:31]\n"
    "s_cbranch_scc1 Lbad38\n"
    "Lok38:\n"
    "buffer_load_dwordx4 v[212:215], v0, s[8:11], s71 offen offset:1024 sc1\n"
    "v_mfma_f32_16x16x32_bf16 v[28:31], v[92:95], v[172:175], v[28:31]\n"
    "ds_read_b128 v[88:91], v0 offset:15360\n"
    "v_mfma_f32_16x16x32_bf16 v[24:27], v[92:95], v[168:171], v[24:27]\n"
    "buffer_load_dwordx4 v[216:219], v0, s[8:11], s71 offen offset:2048 sc1\n"
    "v_mfma_f32_16x16x32_bf16 v[32:35], v[96:99], v[168:171], v[32:35]\n"
    "ds_read_b128 v[92:95], v0 offset:47104\n"
    "v_mfma_f32_16x16x32_bf16 v[40:43], v[96:99], v[172:175], v[40:43]\n"
    "v_mfma_f32_16x16x32_bf16 v[44:47], v[64:67], v[172:175], v[44:47]\n"
    "ds_read_b128 v[96:99], v0 offset:48128\n"
    "v_mfma_f32_16x16x32_bf16 v[36:39], v[64:67], v[168:171], v[36:39]\n"
    "s_waitcnt vmcnt(11) lgkmcnt(4)\n"
    "v_mfma_f32_16x16x32_bf16 v[16:19], v[68:71], v[176:179], v[16:19]\n"
    "ds_read_b128 v[64:67], v0 offset:16384\n"
    "buffer_load_dwordx4 v[220:223], v0, s[8:11], s71 offen offset:3072 sc1\n"
    "v_mfma_f32_16x16x32_bf16 v[20:23], v[68:71], v[180:183], v[20:23]\n"
    "s_add_u32 s71, s80, 14446592\n"
    "v_mfma_f32_16x16x32_bf16 v[28:31], v[72:75], v[180:183], v[28:31]\n"
    "ds_read_b128 v[68:71], v0 offset:17408\n"
    "v_mfma_f32_16x16x32_bf16 v[24:27], v[72:75], v[176:179], v[24:27]\n"
    "buffer_load_dwordx4 v[224:227], v0, s[8:11], s71 offen offset:0 sc1\n"
    "v_mfma_f32_16x16x32_bf16 v[32:35], v[76:79], v[176:179], v[32:35]\n"
    "ds_read_b128 v[72:75], v0 offset:49152\n"
    "v_mfma_f32_16x16x32_bf16 v[40:43], v[76:79], v[180:183], v[40:43]\n"
    "buffer_load_dwordx4 v[228:231], v0, s[8:11], s71 offen offset:1024 sc1\n"
    "v_mfma_f32_16x16x32_bf16 v[44:47], v[80:83], v[180:183], v[44:47]\n"
    "ds_read_b128 v[76:79], v0 offset:50176\n"
    "v_mfma_f32_16x16x32_bf16 v[36:39], v[80:83], v[176:179], v[36:39]\n"
    "s_waitcnt vmcnt(12) lgkmcnt(4)\n"
    "v_mfma_f32_16x16x32_bf16 v[16:19], v[84:87], v[184:187], v[16:19]\n"
    "ds_read_b128 v[80:83], v0 offset:18432\n"
    "v_mfma_f32_16x16x32_bf16 v[20:23], v[84:87], v[188:191], v[20:23]\n"
    "buffer_load_dwordx4 v[232:235], v0, s[8:11], s71 offen offset:2048 sc1\n"
    "v_mfma_f32_16x16x32_bf16 v[28:31], v[88:91], v[188:191], v[28:31]\n"
    "ds_read_b128 v[84:87], v0 offset:19456\n"
    "v_mfma_f32_16x16x32_bf16 v[24:27], v[88:91], v[184:187], v[24:27]\n"
    "v_mfma_f32_16x16x32_bf16 v[32:35], v[92:95], v[184:187], v[32:35]\n"
    "ds_read_b128 v[88:91], v0 offset:51200\n"
    "v_mfma_f32_16x16x32_bf16 v[40:43], v[92:95], v[188:191], v[40:43]\n"
    "buffer_load_dwordx4 v[236:239], v0, s[8:11], s71 offen offset:3072 sc1\n"
    "v_mfma_f32_16x16x32_bf16 v[44:47], v[96:99], v[188:191], v[44:47]\n"
    "ds_read_b128 v[92:95], v0 offset:52224\n"
    "s_add_u32 s71, s80, 14450688\n"
    "v_mfma_f32_16x16x32_bf16 v[36:39], v[96:99], v[184:187], v[36:39]\n"
    "s_waitcnt vmcnt(11) lgkmcnt(4)\n"
    "v_mfma_f32_16x16x32_bf16 v[16:19], v[64:67], v[192:195], v[16:19]\n"
    "ds_read_b128 v[96:99], v0 offset:20480\n"
    "v_mfma_f32_16x16x32_bf16 v[20:23], v[64:67], v[196:199], v[20:23]\n"
    "buffer_load_dwordx4 v[240:243], v0, s[8:11], s71 offen offset:0 sc1\n"
    "v_mfma_f32_16x16x32_bf16 v[28:31], v[68:71], v[196:199], v[28:31]\n"
    "ds_read_b128 v[64:67], v0 offset:21504\n"
    "v_mfma_f32_16x16x32_bf16 v[24:27], v[68:71], v[192:195], v[24:27]\n"
    "buffer_load_dwordx4 v[244:247], v0, s[8:11], s71 offen offset:1024 sc1\n"
    "v_mfma_f32_16x16x32_bf16 v[32:35], v[72:75], v[192:195], v[32:35]\n"
    "ds_read_b128 v[68:71], v0 offset:53248\n"
    "v_mfma_f32_16x16x32_bf16 v[40:43], v[72:75], v[196:199], v[40:43]\n"
    "v_mfma_f32_16x16x32_bf16 v[44:47], v[76:79], v[196:199], v[44:47]\n"
    "ds_read_b128 v[72:75], v0 offset:54272\n"
    "v_mfma_f32_16x16x32_bf16 v[36:39], v[76:79], v[192:195], v[36:39]\n"
    "s_waitcnt vmcnt(10) lgkmcnt(4)\n"
    "v_mfma_f32_16x16x32_bf16 v[16:19], v[80:83], v[200:203], v[16:19]\n"
    "ds_read_b128 v[76:79], v0 offset:22528\n"
    "v_max3_u32 v110, v100, v101, v102\n"
    "v_max_u32 v110, v110, v103\n"
    "v_max3_u32 v110, v110, v104, v105\n"
    "v_mfma_f32_16x16x32_bf16 v[20:23], v[80:83], v[204:207], v[20:23]\n"
    "buffer_load_dwordx4 v[248:251], v0, s[8:11], s71 offen offset:2048 sc1\n"
    "v_mfma_f32_16x16x32_bf16 v[28:31], v[84:87], v[204:207], v[28:31]\n"
    "ds_read_b128 v[80:83], v0 offset:23552\n"
    "v_max3_u32 v110, v110, v106, v107\n"
    "v_mfma_f32_16x16x32_bf16 v[24:27], v[84:87], v[200:203], v[24:27]\n"
    "buffer_load_dwordx4 v[252:255], v0, s[8:11], s71 offen offset:3072 sc1\n"
    "v_mfma_f32_16x16x32_bf16 v[32:35], v[88:91], v[200:203], v[32:35]\n"
    "ds_read_b128 v[84:87], v0 offset:55296\n"
    "s_add_u32 m0, s66, 65536\n"
    "v_mfma_f32_16x16x32_bf16 v[40:43], v[88:91], v[204:207], v[40:43]\n"
    "s_add_u32 s69, s75, 65536\n"
    "v_cmp_eq_u32 s[26:27], -1, v110\n"
    "v_mfma_f32_16x16x32_bf16 v[44:47], v[92:95], v[204:207], v[44:47]\n"
    "ds_read_b128 v[88:91], v0 offset:56320\n"
    "s_cmp_lg_u64 s[26:27], 0\n"
    "s_cbranch_scc1 Lgb41\n"
    "Lgo41:\n"
    "v_mfma_f32_16x16x32_bf16 v[36:39], v[92:95], v[200:203], v[36:39]\n"
    "v_add_f32_dpp v112, v100, v100 row_ror:8 row_mask:0xf bank_mask:3\n"
    "s_waitcnt vmcnt(10) lgkmcnt(4)\n"
    "v_mfma_f32_16x16x32_bf16 v[16:19], v[96:99], v[208:211], v[16:19]\n"
    "ds_read_b128 v[92:95], v0 offset:24576\n"
    "buffer_load_dwordx4 v0, s[8:11], s69 offen offset:0 sc1 lds\n"
    "v_mfma_f32_16x16x32_bf16 v[20:23], v[96:99], v[212:215], v[20:23]\n"
    "v_add_f32_dpp v113, v101, v101 row_ror:8 row_mask:0xf bank_mask:3\n"
    "v_add_f32_dpp v114, v104, v104 row_ror:8 row_mask:0xf bank_mask:3\n"
    "v_mfma_f32_16x16x32_bf16 v[28:31], v[64:67], v[212:215], v[28:31]\n"
    "ds_read_b128 v[96:99], v0 offset:25600\n"
    "v_add_f32_dpp v115, v105, v105 row_ror:8 row_mask:0xf bank_mask:3\n"
    "v_mfma_f32_16x16x32_bf16 v[24:27], v[64:67], v[208:211], v[24:27]\n"
    "buffer_load_dwordx4 v0, s[8:11], s69 offen offset:1024 sc1 lds\n"
    "v_mfma_f32_16x16x32_bf16 v[32:35], v[68:71], v[208:211], v[32:35]\n"
    "ds_read_b128 v[64:67], v0 offset:57344\n"
    "v_add_f32_dpp v112, v102, v102 row_ror:8 row_mask:0xf bank_mask:12\n"
    "v_mfma_f32_16x16x32_bf16 v[40:43], v[68:71], v[212:215], v[40:43]\n"
    "v_add_f32_dpp v113, v103, v103 row_ror:8 row_mask:0xf bank_mask:12\n"
    "v_add_f32_dpp v114, v106, v106 row_ror:8 row_mask:0xf bank_mask:12\n"
    "v_mfma_f32_16x16x32_bf16 v[44:47], v[72:75], v[212:215], v[44:47]\n"
    "ds_read_b128 v[68:71], v0 offset:58368\n"
    "v_add_f32_dpp v115, v107, v107 row_ror:8 row_mask:0xf bank_mask:12\n"
    "v_mfma_f32_16x16x32_bf16 v[36:39], v[72:75], v[208:211], v[36:39]\n"
    "v_permlane32_swap_b32 v112, v113\n"
    "s_waitcnt vmcnt(10) lgkmcnt(4)\n"
    "v_mfma_f32_16x16x32_bf16 v[16:19], v[76:79], v[216:219], v[16:19]\n"
    "ds_read_b128 v[72:75], v0 offset:26624\n"
    "Lpad7:\n"
    "s_cbranch_execz Lpad11\n"
    "buffer_load_dwordx4 v0, s[8:11], s69 offen offset:2048 sc1 lds\n"
    "v_mfma_f32_16x16x32_bf16 v[20:23], v[76:79], v[220:223], v[20:23]\n"
    "v_permlane32_swap_b32 v114, v115\n"
    "v_mfma_f32_16x16x32_bf16 v[28:31], v[80:83], v[220:223], v[28:31]\n"
    "ds_read_b128 v[76:79], v0 offset:27648\n"
    "v_add_f32 v112, v112, v113\n"
    "v_mfma_f32_16x16x32_bf16 v[24:27], v[80:83], v[216:219], v[24:27]\n"
    "buffer_load_dwordx4 v0, s[8:11], s69 offen offset:3072 sc1 lds\n"
    "v_mfma_f32_16x16x32_bf16 v[32:35], v[84:87], v[216:219], v[32:35]\n"
    "ds_read_b128 v[80:83], v0 offset:59392\n"
    "s_add_u32 m0, s66, 69632\n"
    "v_mfma_f32_16x16x32_bf16 v[40:43], v[84:87], v[220:223], v[40:43]\n"
    "s_add_u32 s69, s75, 69632\n"
    "v_add_f32 v114, v114, v115\n"
    "v_mfma_f32_16x16x32_bf16 v[44:47], v[88:91], v[220:223], v[44:47]\n"
    "ds_read_b128 v[84:87], v0 offset:60416\n"
    "v_permlane16_swap_b32 v112, v114\n"
    "v_mfma_f32_16x16x32_bf16 v[36:39], v[88:91], v[216:219], v[36:39]\n"
    "v_add_f32 v112, v112, v114\n"
    "s_waitcnt vmcnt(10) lgkmcnt(4)\n"
    "v_mfma_f32_16x16x32_bf16 v[16:19], v[92:95], v[224:227], v[16:19]\n"
    "ds_read_b128 v[88:91], v0 offset:28672\n"
    "buffer_load_dwordx4 v0, s[8:11], s69 offen offset:0 sc1 lds\n"
    "v_mfma_f32_16x16x32_bf16 v[20:23], v[92:95], v[228:231], v[20:23]\n"
    "v_mul_f32 v108, 0xbb000000, v112\n"
    "v_mul_f32 v116, 0xbb000000, v112\n"
    "v_mfma_f32_16x16x32_bf16 v[28:31], v[96:99], v[228:231], v[28:31]\n"
    "ds_read_b128 v[92:95], v0 offset:29696\n"
    "v_mfma_f32_16x16x32_bf16 v[24:27], v[96:99], v[224:227], v[24:27]\n"
    "buffer_load_dwordx4 v0, s[8:11], s69 offen offset:1024 sc1 lds\n"
    "v_mfma_f32_16x16x32_bf16 v[32:35], v[64:67], v[224:227], v[32:35]\n"
    "ds_read_b128 v[96:99], v0 offset:61440\n"
    "v_permlane32_swap_b32 v108, v116\n"
    "v_mfma_f32_16x16x32_bf16 v[40:43], v[64:67], v[228:231], v[40:43]\n"
    "v_fma_f32 v109, -v108, v108, -v116\n"
    "v_cvt_pk_bf16_f32 v124, v108, v108\n"
    "v_mfma_f32_16x16x32_bf16 v[44:47], v[68:71], v[228:231], v[44:47]\n"
    "ds_read_b128 v[64:67], v0 offset:62464\n"
    "v_max_f32 v116, 0, v109\n"
    "v_mfma_f32_16x16x32_bf16 v[36:39], v[68:71], v[224:227], v[36:39]\n"
    "v_lshlrev_b32 v112, 16, v124\n"
    "s_waitcnt vmcnt(10) lgkmcnt(4)\n"
    "v_mfma_f32_16x16x32_bf16 v[16:19], v[72:75], v[232:235], v[16:19]\n"
    "ds_read_b128 v[68:71], v0 offset:30720\n"
    "buffer_load_dwordx4 v0, s[8:11], s69 offen offset:2048 sc1 lds\n"
    "v_mfma_f32_16x16x32_bf16 v[20:23], v[72:75], v[236:239], v[20:23]\n"
    "v_add_f32 v116, 0x3727c5ac, v116\n"
    "v_sub_f32 v112, v108, v112\n"
    "v_mfma_f32_16x16x32_bf16 v[28:31], v[76:79], v[236:239], v[28:31]\n"
    "ds_read_b128 v[72:75], v0 offset:31744\n"
    "v_fmac_f32 v116, 0, v109\n"
    "v_mfma_f32_16x16x32_bf16 v[24:27], v[76:79], v[232:235], v[24:27]\n"
    "buffer_load_dwordx4 v0, s[8:11], s69 offen offset:3072 sc1 lds\n"
    "v_mfma_f32_16x16x32_bf16 v[32:35], v[80:83], v[232:235], v[32:35]\n"
    "ds_read_b128 v[76:79], v0 offset:63488\n"
    "s_add_u32 m0, s66, 98304\n"
    "v_cvt_pk_bf16_f32 v125, v108, v112\n"
    "v_mfma_f32_16x16x32_bf16 v[40:43], v[80:83], v[236:239], v[40:43]\n"
    "s_add_u32 s69, s76, 0\n"
    "v_mov_b32 v126, v124\n"
    "v_mfma_f32_16x16x32_bf16 v[44:47], v[84:87], v[236:239], v[44:47]\n"
    "ds_read_b128 v[80:83], v0 offset:64512\n"
    "v_mfma_f32_16x16x32_bf16 v[36:39], v[84:87], v[232:235], v[36:39]\n"
    "s_waitcnt vmcnt(10) lgkmcnt(4)\n"
    "v_mfma_f32_16x16x32_bf16 v[16:19], v[88:91], v[240:243], v[16:19]\n"
    "buffer_load_dwordx4 v15, s[20:23], s69 offen offset:0 lds\n"
    "v_rsq_f32 v118, v116\n"
    "v_mfma_f32_16x16x32_bf16 v[20:23], v[88:91], v[244:247], v[20:23]\n"
    "s_add_u32 s69, s76, 7168\n"
    "v_mov_b32 v127, v125\n"
    "v_mfma_f32_16x16x32_bf16 v[28:31], v[92:95], v[244:247], v[28:31]\n"
    "buffer_load_dwordx4 v15, s[20:23], s69 offen offset:1024 lds\n"
    "v_mfma_f32_16x16x32_bf16 v[24:27], v[92:95], v[240:243], v[24:27]\n"
    "s_add_u32 s69, s76, 14336\n"
    "v_mov_b32 v117, v116\n"
    "v_mfma_f32_16x16x32_bf16 v[32:35], v[96:99], v[240:243], v[32:35]\n"
    "v_permlane16_swap_b32 v124, v126\n"
    "v_mfma_f32_16x16x32_bf16 v[40:43], v[96:99], v[244:247], v[40:43]\n"
    "v_permlane16_swap_b32 v125, v127\n"
    "v_mfma_f32_16x16x32_bf16 v[44:47], v[64:67], v[244:247], v[44:47]\n"
    "buffer_load_dwordx4 v15, s[20:23], s69 offen offset:2048 lds\n"
    "v_mfma_f32_16x16x32_bf16 v[36:39], v[64:67], v[240:243], v[36:39]\n"
    "s_add_u32 s69, s76, 21504\n"
    "v_mul_f32 v120, 0x3fb8aa3b, v118\n"
    "v_mfma_f32_16x16x16_bf16 v[16:19], a[176:177], v[124:125], v[16:19]\n"
    "v_mfma_f32_16x16x16_bf16 v[20:23], a[176:177], v[126:127], v[20:23]\n"
    "v_mfma_f32_16x16x16_bf16 v[24:27], a[178:179], v[124:125], v[24:27]\n"
    "v_mfma_f32_16x16x16_bf16 v[28:31], a[178:179], v[126:127], v[28:31]\n"
    "v_mfma_f32_16x16x16_bf16 v[32:35], a[180:181], v[124:125], v[32:35]\n"
    "v_mfma_f32_16x16x16_bf16 v[40:43], a[180:181], v[126:127], v[40:43]\n"
    "v_mfma_f32_16x16x16_bf16 v[36:39], a[182:183], v[124:125], v[36:39]\n"
    "v_mfma_f32_16x16x16_bf16 v[44:47], a[182:183], v[126:127], v[44:47]\n"
    "s_waitcnt vmcnt(11) lgkmcnt(0)\n"
    "v_mfma_f32_16x16x32_bf16 v[16:19], v[68:71], v[248:251], v[16:19]\n"
    "v_mov_b32 v119, v118\n"
    "s_nop 1\n"
    "v_permlane16_swap_b32 v118, v119\n"
    "v_mfma_f32_16x16x32_bf16 v[20:23], v[68:71], v[252:255], v[20:23]\n"
    "v_mfma_f32_16x16x32_bf16 v[28:31], v[72:75], v[252:255], v[28:31]\n"
    "buffer_load_dwordx4 v15, s[20:23], s69 offen offset:3072 lds\n"
    "v_mfma_f32_16x16x32_bf16 v[24:27], v[72:75], v[248:251], v[24:27]\n"
    "v_mfma_f32_16x16x32_bf16 v[32:35], v[76:79], v[248:251], v[32:35]\n"
    "v_mfma_f32_16x16x32_bf16 v[40:43], v[76:79], v[252:255], v[40:43]\n"
    "v_mfma_f32_16x16x32_bf16 v[44:47], v[80:83], v[252:255], v[44:47]\n"
    "v_mfma_f32_16x16x32_bf16 v[36:39], v[80:83], v[248:251], v[36:39]\n"
    "s_nop 0\n"
    "v_cmp_u_f32 s[30:31], v16, v20\n"
    "v_cmp_u_f32 s[24:25], v116, v117\n"
    "v_permlane16_swap_b32 v16, v20\n"
    "v_permlane16_swap_b32 v17, v21\n"
    "v_permlane16_swap_b32 v18, v22\n"
    "v_permlane16_swap_b32 v19, v23\n"
    "s_or_b64 s[30:31], s[30:31], s[24:25]\n"
    "s_cbranch_scc1 Lbad42\n"
    "Lok42:\n"
    "s_barrier\n"
    "v_permlane16_swap_b32 v24, v28\n"
    "v_permlane16_swap_b32 v25, v29\n"
    "s_add_u32 m0, s66, 131072\n"
    "s_add_u32 s69, s76, 262144\n"
    "buffer_load_dwordx4 v15, s[20:23], s69 offen offset:0 lds\n"
    "v_permlane16_swap_b32 v26, v30\n"
    "v_permlane16_swap_b32 v27, v31\n"
    "v_accvgpr_read_b32 v10, a244\n"
    "v_accvgpr_read_b32 v14, a245\n"
    "v_xor_b32_e64 v11, 16, v10\n"
    "v_pk_mul_f32 v[32:33], v[32:33], v[118:119] op_sel:[0,0] op_sel_hi:[1,0]\n"
    "v_pk_mul_f32 v[34:35], v[34:35], v[118:119] op_sel:[0,0] op_sel_hi:[1,0]\n"
    "v_pk_mul_f32 v[36:37], v[36:37], v[118:119] op_sel:[0,0] op_sel_hi:[1,0]\n"
    "v_pk_mul_f32 v[38:39], v[38:39], v[118:119] op_sel:[0,0] op_sel_hi:[1,0]\n"
    "v_pk_mul_f32 v[40:41], v[40:41], v[118:119] op_sel:[0,1] op_sel_hi:[1,1]\n"
    "v_pk_mul_f32 v[42:43], v[42:43], v[118:119] op_sel:[0,1] op_sel_hi:[1,1]\n"
    "v_pk_mul_f32 v[44:45], v[44:45], v[118:119] op_sel:[0,1] op_sel_hi:[1,1]\n"
    "v_pk_mul_f32 v[46:47], v[46:47], v[118:119] op_sel:[0,1] op_sel_hi:[1,1]\n"
    "v_max3_f32 v80, v16, v17, v18\n"
    "v_max3_f32 v81, v24, v25, v26\n"
    "v_min3_f32 v82, v16, v17, v18\n"
    "s_add_u32 s69, s76, 269312\n"
    "buffer_load_dwordx4 v15, s[20:23], s69 offen offset:1024 lds\n"
    "v_min3_f32 v83, v24, v25, v26\n"
    "v_max3_f32 v80, v80, v19, v20\n"
    "v_max3_f32 v81, v81, v27, v28\n"
    "v_min3_f32 v82, v82, v19, v20\n"
    "v_min3_f32 v83, v83, v27, v28\n"
    "v_max3_f32 v80, v80, v21, v22\n"
    "v_max3_f32 v81, v81, v29, v30\n"
    "v_min3_f32 v82, v82, v21, v22\n"
    "v_min3_f32 v83, v83, v29, v30\n"
    "v_max_f32 v80, v80, v23\n"
    "v_max_f32 v81, v81, v31\n"
    "v_min_f32 v82, v82, v23\n"
    "v_min_f32 v83, v83, v31\n"
    "v_permlane32_swap_b32 v80, v81\n"
    "s_nop 0\n"
    "v_permlane32_swap_b32 v82, v83\n"
    "v_max_f32 v80, v80, v81\n"
    "s_add_u32 s69, s76, 276480\n"
    "buffer_load_dwordx4 v15, s[20:23], s69 offen offset:2048 lds\n"
    "v_min_f32 v82, v82, v83\n"
    "v_mul_f32 v80, v80, v120\n"
    "v_mul_f32 v82, v82, v120\n"
    "v_mov_b32 v81, v80\n"
    "v_mov_b32 v83, v82\n"
    "s_nop 0\n"
    "v_permlane32_swap_b32 v80, v81\n"
    "v_permlane32_swap_b32 v82, v83\n"
    "v_pk_fma_f32 v[64:65], v[16:17], v[120:121], v[82:83] op_sel:[0,0,0] op_sel_hi:[1,0,0] neg_lo:[1,0,0] neg_hi:[1,0,0]\n"
    "v_pk_fma_f32 v[66:67], v[18:19], v[120:121], v[82:83] op_sel:[0,0,0] op_sel_hi:[1,0,0] neg_lo:[1,0,0] neg_hi:[1,0,0]\n"
    "v_pk_fma_f32 v[68:69], v[20:21], v[120:121], v[82:83] op_sel:[0,0,0] op_sel_hi:[1,0,0] neg_lo:[1,0,0] neg_hi:[1,0,0]\n"
    "v_pk_fma_f32 v[70:71], v[22:23], v[120:121], v[82:83] op_sel:[0,0,0] op_sel_hi:[1,0,0] neg_lo:[1,0,0] neg_hi:[1,0,0]\n"
    "v_exp_f32 v64, v64\n"
    "v_pk_fma_f32 v[16:17], v[16:17], v[120:121], v[80:81] op_sel:[0,0,0] op_sel_hi:[1,0,0] neg_lo:[0,0,1] neg_hi:[0,0,1]\n"
    "v_exp_f32 v65, v65\n"
    "v_pk_fma_f32 v[18:19], v[18:19], v[120:121], v[80:81] op_sel:[0,0,0] op_sel_hi:[1,0,0] neg_lo:[0,0,1] neg_hi:[0,0,1]\n"
    "v_exp_f32 v66, v66\n"
    "s_add_u32 s69, s76, 283648\n"
    "buffer_load_dwordx4 v15, s[20:23], s69 offen offset:3072 lds\n"
    "v_pk_fma_f32 v[20:21], v[20:21], v[120:121], v[80:81] op_sel:[0,0,0] op_sel_hi:[1,0,0] neg_lo:[0,0,1] neg_hi:[0,0,1]\n"
    "v_exp_f32 v67, v67\n"
    "v_pk_fma_f32 v[22:23], v[22:23], v[120:121], v[80:81] op_sel:[0,0,0] op_sel_hi:[1,0,0] neg_lo:[0,0,1] neg_hi:[0,0,1]\n"
    "v_exp_f32 v68, v68\n"
    "v_cvt_pk_bf16_f32 v88, v32, v33\n"
    "v_exp_f32 v69, v69\n"
    "v_cvt_pk_bf16_f32 v89, v34, v35\n"
    "v_exp_f32 v70, v70\n"
    "ds_write_b64 v14, v[88:89] offset:16384\n"
    "v_exp_f32 v71, v71\n"
    "v_cvt_pk_bf16_f32 v90, v36, v37\n"
    "v_cvt_pk_bf16_f32 v91, v38, v39\n"
    "ds_write_b64 v14, v[90:91] offset:20480\n"
    "v_exp_f32 v16, v16\n"
    "v_pk_fma_f32 v[72:73], v[24:25], v[120:121], v[82:83] op_sel:[0,0,1] op_sel_hi:[1,0,1] neg_lo:[1,0,0] neg_hi:[1,0,0]\n"
    "v_exp_f32 v17, v17\n"
    "v_pk_fma_f32 v[74:75], v[26:27], v[120:121], v[82:83] op_sel:[0,0,1] op_sel_hi:[1,0,1] neg_lo:[1,0,0] neg_hi:[1,0,0]\n"
    "v_exp_f32_e64 v18, v18\n"
    "v_accvgpr_read_b32 v7, a251\n"
    "s_add_u32 s74, s83, 20975616\n"
    "buffer_store_dwordx4 a[236:239], v7, s[8:11], s74 offen sc1\n"
    "v_pk_fma_f32 v[76:77], v[28:29], v[120:121], v[82:83] op_sel:[0,0,1] op_sel_hi:[1,0,1] neg_lo:[1,0,0] neg_hi:[1,0,0]\n"
    "v_exp_f32 v19, v19\n"
    "v_pk_fma_f32 v[78:79], v[30:31], v[120:121], v[82:83] op_sel:[0,0,1] op_sel_hi:[1,0,1] neg_lo:[1,0,0] neg_hi:[1,0,0]\n"
    "v_exp_f32 v20, v20\n"
    "v_cvt_pk_bf16_f32 v92, v40, v41\n"
    "v_exp_f32 v21, v21\n"
    "v_cvt_pk_bf16_f32 v93, v42, v43\n"
    "v_exp_f32 v22, v22\n"
    "ds_write_b64 v14, v[92:93] offset:16896\n"
    "v_exp_f32 v23, v23\n"
    "v_cvt_pk_bf16_f32 v94, v44, v45\n"
    "v_cvt_pk_bf16_f32 v95, v46, v47\n"
    "ds_write_b64 v14, v[94:95] offset:20992\n"
    "v_exp_f32 v72, v72\n"
    "v_pk_fma_f32 v[24:25], v[24:25], v[120:121], v[80:81] op_sel:[0,0,1] op_sel_hi:[1,0,1] neg_lo:[0,0,1] neg_hi:[0,0,1]\n"
    "v_exp_f32 v73, v73\n"
    "v_pk_fma_f32 v[26:27], v[26:27], v[120:121], v[80:81] op_sel:[0,0,1] op_sel_hi:[1,0,1] neg_lo:[0,0,1] neg_hi:[0,0,1]\n"
    "v_exp_f32 v74, v74\n"
    "s_add_u32 s74, s83, 20976128\n"
    "buffer_store_dwordx4 a[236:239], v7, s[8:11], s74 offen sc1\n"
    "v_pk_fma_f32 v[28:29], v[28:29], v[120:121], v[80:81] op_sel:[0,0,1] op_sel_hi:[1,0,1] neg_lo:[0,0,1] neg_hi:[0,0,1]\n"
    "v_exp_f32 v75, v75\n"
    "v_pk_fma_f32 v[30:31], v[30:31], v[120:121], v[80:81] op_sel:[0,0,1] op_sel_hi:[1,0,1] neg_lo:[0,0,1] neg_hi:[0,0,1]\n"
    "v_exp_f32 v76, v76\n"
    "v_accvgpr_read_b32 v32, a246\n"
    "v_exp_f32 v77, v77\n"
    "v_accvgpr_read_b32 v33, a247\n"
    "v_exp_f32 v78, v78\n"
    "v_accvgpr_read_b32 v34, a250\n"
    "v_exp_f32 v79, v79\n"
    "v_exp_f32 v24, v24\n"
    "v_pk_add_f32 v[92:93], v[64:65], v[66:67] op_sel:[0,0] op_sel_hi:[1,1]\n"
    "v_exp_f32 v25, v25\n"
    "v_pk_add_f32 v[94:95], v[68:69], v[70:71] op_sel:[0,0] op_sel_hi:[1,1]\n"
    "v_exp_f32 v26, v26\n"
    "v_pk_add_f32 v[84:85], v[16:17], v[18:19] op_sel:[0,0] op_sel_hi:[1,1]\n"
    "s_add_u32 s74, s81, 16650240\n"
    "buffer_store_dwordx4 a[236:239], v0, s[8:11], s74 offen sc1\n"
    "v_exp_f32 v27, v27\n"
    "v_pk_add_f32 v[86:87], v[20:21], v[22:23] op_sel:[0,0] op_sel_hi:[1,1]\n"
    "v_exp_f32 v28, v28\n"
    "v_pk_add_f32 v[96:97], v[72:73], v[74:75] op_sel:[0,0] op_sel_hi:[1,1]\n"
    "v_exp_f32 v29, v29\n"
    "v_pk_add_f32 v[98:99], v[76:77], v[78:79] op_sel:[0,0] op_sel_hi:[1,1]\n"
    "v_exp_f32 v30, v30\n"
    "v_exp_f32_e64 v31, v31\n"
    "v_pk_add_f32 v[88:89], v[24:25], v[26:27] op_sel:[0,0] op_sel_hi:[1,1]\n"
    "v_pk_add_f32 v[90:91], v[28:29], v[30:31] op_sel:[0,0] op_sel_hi:[1,1]\n"
    "v_pk_add_f32 v[84:85], v[84:85], v[86:87] op_sel:[0,0] op_sel_hi:[1,1]\n"
    "v_pk_add_f32 v[88:89], v[88:89], v[90:91] op_sel:[0,0] op_sel_hi:[1,1]\n"
    "v_pk_add_f32 v[92:93], v[92:93], v[94:95] op_sel:[0,0] op_sel_hi:[1,1]\n"
    "v_pk_add_f32 v[96:97], v[96:97], v[98:99] op_sel:[0,0] op_sel_hi:[1,1]\n"
    "v_add_f32 v80, v84, v85\n"
    "v_add_f32 v81, v88, v89\n"
    "s_add_u32 s74, s81, 16652288\n"
    "buffer_store_dwordx4 a[236:239], v0, s[8:11], s74 offen sc1\n"
    "v_add_f32 v82, v92, v93\n"
    "v_add_f32 v83, v96, v97\n"
    "v_permlane32_swap_b32 v80, v81\n"
    "s_nop 0\n"
    "v_permlane32_swap_b32 v82, v83\n"
    "v_add_f32 v80, v80, v81\n"
    "v_add_f32 v82, v82, v83\n"
    "v_rcp_f32 v80, v80\n"
    "v_rcp_f32 v82, v82\n"
    "v_mov_b32 v81, v80\n"
    "v_mov_b32 v83, v82\n"
    "s_nop 0\n"
    "v_permlane32_swap_b32 v80, v81\n"
    "v_permlane32_swap_b32 v82, v83\n"
    "v_pk_mul_f32 v[16:17], v[16:17], v[80:81] op_sel:[0,0] op_sel_hi:[1,0]\n"
    "v_pk_mul_f32 v[18:19], v[18:19], v[80:81] op_sel:[0,0] op_sel_hi:[1,0]\n"
    "v_pk_mul_f32 v[20:21], v[20:21], v[80:81] op_sel:[0,0] op_sel_hi:[1,0]\n"
    "v_pk_mul_f32 v[22:23], v[22:23], v[80:81] op_sel:[0,0] op_sel_hi:[1,0]\n"
    "v_mul_u32_u24 v7, 17, v0\n"
    "v_cmp_gt_u32 vcc, 0x100, v0\n"
    "v_bfe_u32 v7, v7, 7, 4\n"
    "v_lshlrev_b32 v7, 3, v7\n"
    "s_nop 0\n"
    "v_cndmask_b32 v7, 2.0, v7, vcc\n"
    "s_add_u32 s74, s82, 16650240\n"
    "buffer_store_dwordx2 a[236:237], v7, s[8:11], s74 offen sc1\n"
    "v_cvt_pk_bf16_f32 v100, v16, v17\n"
    "v_cvt_pk_bf16_f32 v101, v18, v19\n"
    "v_cvt_pk_bf16_f32 v102, v20, v21\n"
    "v_cvt_pk_bf16_f32 v103, v22, v23\n"
    "ds_write_b64 v10, v[100:101] offset:0\n"
    "ds_write_b64 v11, v[102:103] offset:0\n"
    "v_pk_mul_f32 v[64:65], v[64:65], v[82:83] op_sel:[0,0] op_sel_hi:[1,0]\n"
    "v_pk_mul_f32 v[66:67], v[66:67], v[82:83] op_sel:[0,0] op_sel_hi:[1,0]\n"
    "v_pk_mul_f32 v[68:69], v[68:69], v[82:83] op_sel:[0,0] op_sel_hi:[1,0]\n"
    "v_pk_mul_f32 v[70:71], v[70:71], v[82:83] op_sel:[0,0] op_sel_hi:[1,0]\n"
    "v_cvt_pk_bf16_f32 v104, v64, v65\n"
    "v_cvt_pk_bf16_f32 v105, v66, v67\n"
    "v_cvt_pk_bf16_f32 v106, v68, v69\n"
    "v_cvt_pk_bf16_f32 v107, v70, v71\n"
    "ds_write_b64 v10, v[104:105] offset:4096\n"
    "ds_write_b64 v11, v[106:107] offset:4096\n"
    "v_pk_mul_f32 v[24:25], v[24:25], v[80:81] op_sel:[0,1] op_sel_hi:[1,1]\n"
    "v_pk_mul_f32 v[26:27], v[26:27], v[80:81] op_sel:[0,1] op_sel_hi:[1,1]\n"
    "v_pk_mul_f32 v[28:29], v[28:29], v[80:81] op_sel:[0,1] op_sel_hi:[1,1]\n"
    "v_pk_mul_f32 v[30:31], v[30:31], v[80:81] op_sel:[0,1] op_sel_hi:[1,1]\n"
    "v_cvt_pk_bf16_f32 v108, v24, v25\n"
    "v_cvt_pk_bf16_f32 v109, v26, v27\n"
    "v_cvt_pk_bf16_f32 v110, v28, v29\n"
    "v_cvt_pk_bf16_f32 v111, v30, v31\n"
    "ds_write_b64 v10, v[108:109] offset:8192\n"
    "ds_write_b64 v11, v[110:111] offset:8192\n"
    "v_pk_mul_f32 v[72:73], v[72:73], v[82:83] op_sel:[0,1] op_sel_hi:[1,1]\n"
    "v_pk_mul_f32 v[74:75], v[74:75], v[82:83] op_sel:[0,1] op_sel_hi:[1,1]\n"
    "v_pk_mul_f32 v[76:77], v[76:77], v[82:83] op_sel:[0,1] op_sel_hi:[1,1]\n"
    "v_pk_mul_f32 v[78:79], v[78:79], v[82:83] op_sel:[0,1] op_sel_hi:[1,1]\n"
    "v_cvt_pk_bf16_f32 v112, v72, v73\n"
    "v_cvt_pk_bf16_f32 v113, v74, v75\n"
    "v_cvt_pk_bf16_f32 v114, v76, v77\n"
    "v_cvt_pk_bf16_f32 v115, v78, v79\n"
    "ds_write_b64 v10, v[112:113] offset:12288\n"
    "ds_write_b64 v11, v[114:115] offset:12288\n"
    "s_waitcnt vmcnt(13) lgkmcnt(0)\n"
    "s_barrier\n"
    "s_mov_b32 s65, 299008\n"
    "buffer_atomic_add a255, v34, s[8:11], s65 offen\n"
    "ds_read_b64_tr_b16 v[16:17], v32 offset:0\n"
    "ds_read_b64_tr_b16 v[18:19], v32 offset:512\n"
    "ds_read_b64_tr_b16 v[64:65], v33 offset:0\n"
    "ds_read_b64_tr_b16 v[66:67], v33 offset:512\n"
    "ds_read_b64_tr_b16 v[20:21], v32 offset:1024\n"
    "ds_read_b64_tr_b16 v[22:23], v32 offset:1536\n"
    "ds_read_b64_tr_b16 v[68:69], v33 offset:1024\n"
    "ds_read_b64_tr_b16 v[70:71], v33 offset:1536\n"
    "ds_read_b64_tr_b16 v[24:25], v32 offset:2048\n"
    "ds_read_b64_tr_b16 v[26:27], v32 offset:2560\n"
    "ds_read_b64_tr_b16 v[72:73], v33 offset:2048\n"
    "ds_read_b64_tr_b16 v[74:75], v33 offset:2560\n"
    "ds_read_b64_tr_b16 v[28:29], v32 offset:3072\n"
    "ds_read_b64_tr_b16 v[30:31], v32 offset:3584\n"
    "ds_read_b64_tr_b16 v[76:77], v33 offset:3072\n"
    "ds_read_b64_tr_b16 v[78:79], v33 offset:3584\n"
    "v_lshrrev_b32 v10, 1, v0\n"
    "v_and_b32 v11, 0xe0, v0\n"
    "v_cmp_eq_u32 vcc, 0, v11\n"
    "v_lshrrev_b32 v11, 4, v0\n"
    "v_and_b32 v11, 0x31, v11\n"
    "v_mul_u32_u24 v11, 65, v11\n"
    "v_and_b32 v11, 0x70, v11\n"
    "v_cndmask_b32 v11, 2.0, v11, vcc\n"
    "s_lshl_b32 s62, s59, 9\n"
    "s_add_u32 s62, s62, s90\n"
    "s_lshr_b32 s63, s62, 2\n"
    "s_add_u32 s62, s62, 1183744\n"
    "s_add_u32 s63, s63, 1839104\n"
    "s_waitcnt lgkmcnt(12)\n"
    "v_mfma_f32_16x16x32_bf16 v[36:39], v[16:19], v[64:67], 0\n"
    "v_mfma_f32_16x16x32_bf16 v[40:43], v[16:19], a[240:243], 0\n"
    "s_waitcnt lgkmcnt(8)\n"
    "v_mfma_f32_16x16x32_bf16 v[36:39], v[20:23], v[68:71], v[36:39]\n"
    "v_mfma_f32_16x16x32_bf16 v[40:43], v[20:23], a[240:243], v[40:43]\n"
    "s_waitcnt lgkmcnt(4)\n"
    "v_mfma_f32_16x16x32_bf16 v[36:39], v[24:27], v[72:75], v[36:39]\n"
    "v_mfma_f32_16x16x32_bf16 v[40:43], v[24:27], a[240:243], v[40:43]\n"
    "s_waitcnt lgkmcnt(0)\n"
    "v_mfma_f32_16x16x32_bf16 v[36:39], v[28:31], v[76:79], v[36:39]\n"
    "v_mfma_f32_16x16x32_bf16 v[40:43], v[28:31], a[240:243], v[40:43]\n"
    "s_nop 6\n"
    "v_cvt_pk_bf16_f32 v36, v36, v37\n"
    "v_cvt_pk_bf16_f32 v37, v38, v39\n"
    "buffer_store_dwordx2 v[36:37], v10, s[8:11], s62 offen sc1\n"
    "buffer_store_dwordx4 v[40:43], v11, s[8:11], s63 offen sc1\n"
    "v_and_b32 v14, 0x3c0, v0\n"
    "v_add_u32 v14, v14, v0\n"
    "s_nop 0\n"
    "s_lshr_b32 s64, s90, 2\n"
    "v_accvgpr_read_b32 v11, a251\n"
    "ds_read_b128 v[80:83], v1 offset:0\n"
    "ds_read_b128 v[84:87], v1 offset:1024\n"
    "ds_read_b128 v[88:91], v1 offset:2048\n"
    "ds_read_b128 v[92:95], v1 offset:3072\n"
    "ds_read_b128 v[96:99], v1 offset:4096\n"
    "ds_read_b128 v[64:67], v1 offset:5120\n"
    "ds_read_b128 v[68:71], v1 offset:6144\n"
    "ds_read_b128 v[72:75], v1 offset:7168\n"
    "v_mfma_f32_16x16x16_bf16 v[48:51], a[184:185], v[124:125], 0\n"
    "v_mfma_f32_16x16x16_bf16 v[52:55], a[184:185], v[126:127], 0\n"
    "v_mfma_f32_16x16x16_bf16 v[56:59], a[186:187], v[124:125], 0\n"
    "s_add_u32 m0, s66, 135168\n"
    "s_add_u32 s69, s76, 294912\n"
    "v_mfma_f32_16x16x16_bf16 v[60:63], a[186:187], v[126:127], 0\n"
    "s_add_u32 s68, s94, 294912\n"
    "v_mov_b32 v8, 0\n"
    "buffer_atomic_add v8, v6, s[8:11], s68 offen sc0\n"
    "s_waitcnt lgkmcnt(4)\n"
    "v_mfma_f32_16x16x32_bf16 v[48:51], v[80:83], v[128:131], v[48:51]\n"
    "ds_read_b128 v[76:79], v1 offset:8192\n"
    "v_mfma_f32_16x16x32_bf16 v[52:55], v[80:83], v[132:135], v[52:55]\n"
    "buffer_load_dwordx4 v15, s[20:23], s69 offen offset:0 lds\n"
    "v_mfma_f32_16x16x32_bf16 v[60:63], v[84:87], v[132:135], v[60:63]\n"
    "ds_read_b128 v[80:83], v1 offset:9216\n"
    "v_mfma_f32_16x16x32_bf16 v[56:59], v[84:87], v[128:131], v[56:59]\n"
    "v_mfma_f32_16x16x32_bf16 v[48:51], v[88:91], v[136:139], v[48:51]\n"
    "ds_read_b128 v[84:87], v1 offset:10240\n"
    "v_mfma_f32_16x16x32_bf16 v[52:55], v[88:91], v[140:143], v[52:55]\n"
    "s_add_u32 s69, s76, 302080\n"
    "v_mfma_f32_16x16x32_bf16 v[60:63], v[92:95], v[140:143], v[60:63]\n"
    "ds_read_b128 v[88:91], v1 offset:11264\n"
    "v_mfma_f32_16x16x32_bf16 v[56:59], v[92:95], v[136:139], v[56:59]\n"
    "s_waitcnt lgkmcnt(4)\n"
    "v_mfma_f32_16x16x32_bf16 v[48:51], v[96:99], v[144:147], v[48:51]\n"
    "ds_read_b128 v[92:95], v1 offset:12288\n"
    "v_mfma_f32_16x16x32_bf16 v[52:55], v[96:99], v[148:151], v[52:55]\n"
    "buffer_load_dwordx4 v15, s[20:23], s69 offen offset:1024 lds\n"
    "v_mfma_f32_16x16x32_bf16 v[60:63], v[64:67], v[148:151], v[60:63]\n"
    "ds_read_b128 v[96:99], v1 offset:13312\n"
    "v_mfma_f32_16x16x32_bf16 v[56:59], v[64:67], v[144:147], v[56:59]\n"
    "v_mfma_f32_16x16x32_bf16 v[48:51], v[68:71], v[152:155], v[48:51]\n"
    "ds_read_b128 v[64:67], v1 offset:14336\n"
    "s_add_u32 s69, s76, 309248\n"
    "v_mfma_f32_16x16x32_bf16 v[52:55], v[68:71], v[156:159], v[52:55]\n"
    "v_mfma_f32_16x16x32_bf16 v[60:63], v[72:75], v[156:159], v[60:63]\n"
    "ds_read_b128 v[68:71], v1 offset:15360\n"
    "v_mfma_f32_16x16x32_bf16 v[56:59], v[72:75], v[152:155], v[56:59]\n"
    "s_waitcnt lgkmcnt(4)\n"
    "v_mfma_f32_16x16x32_bf16 v[48:51], v[76:79], v[160:163], v[48:51]\n"
    "ds_read_b128 v[72:75], v1 offset:16384\n"
    "v_mfma_f32_16x16x32_bf16 v[52:55], v[76:79], v[164:167], v[52:55]\n"
    "buffer_load_dwordx4 v15, s[20:23], s69 offen offset:2048 lds\n"
    "v_mfma_f32_16x16x32_bf16 v[60:63], v[80:83], v[164:167], v[60:63]\n"
    "ds_read_b128 v[76:79], v1 offset:17408\n"
    "v_mfma_f32_16x16x32_bf16 v[56:59], v[80:83], v[160:163], v[56:59]\n"
    "s_add_u32 s69, s76, 316416\n"
    "v_mfma_f32_16x16x32_bf16 v[48:51], v[84:87], v[168:171], v[48:51]\n"
    "ds_read_b128 v[80:83], v1 offset:18432\n"
    "v_mfma_f32_16x16x32_bf16 v[52:55], v[84:87], v[172:175], v[52:55]\n"
    "v_mfma_f32_16x16x32_bf16 v[60:63], v[88:91], v[172:175], v[60:63]\n"
    "ds_read_b128 v[84:87], v1 offset:19456\n"
    "v_mfma_f32_16x16x32_bf16 v[56:59], v[88:91], v[168:171], v[56:59]\n"
    "s_waitcnt lgkmcnt(4)\n"
    "v_mfma_f32_16x16x32_bf16 v[48:51], v[92:95], v[176:179], v[48:51]\n"
    "ds_read_b128 v[88:91], v1 offset:20480\n"
    "buffer_load_dwordx4 v15, s[20:23], s69 offen offset:3072 lds\n"
    "v_mfma_f32_16x16x32_bf16 v[52:55], v[92:95], v[180:183], v[52:55]\n"
    "v_mfma_f32_16x16x32_bf16 v[60:63], v[96:99], v[180:183], v[60:63]\n"
    "ds_read_b128 v[92:95], v1 offset:21504\n"
    "s_add_u32 m0, s66, 102400\n"
    "v_mfma_f32_16x16x32_bf16 v[56:59], v[96:99], v[176:179], v[56:59]\n"
    "s_add_u32 s69, s76, 32768\n"
    "v_mfma_f32_16x16x32_bf16 v[48:51], v[64:67], v[184:187], v[48:51]\n"
    "ds_read_b128 v[96:99], v1 offset:22528\n"
    "v_mfma_f32_16x16x32_bf16 v[52:55], v[64:67], v[188:191], v[52:55]\n"
    "buffer_load_dwordx4 v15, s[20:23], s69 offen offset:0 lds\n"
    "v_mfma_f32_16x16x32_bf16 v[60:63], v[68:71], v[188:191], v[60:63]\n"
    "ds_read_b128 v[64:67], v1 offset:23552\n"
    "v_mfma_f32_16x16x32_bf16 v[56:59], v[68:71], v[184:187], v[56:59]\n"
    "v_mov_b32 v9, 0\n"
    "buffer_atomic_add v9, v6, s[8:11], s68 offen sc0\n"
    "s_waitcnt lgkmcnt(4)\n"
    "v_mfma_f32_16x16x32_bf16 v[48:51], v[72:75], v[192:195], v[48:51]\n"
    "ds_read_b128 v[68:71], v1 offset:24576\n"
    "v_mfma_f32_16x16x32_bf16 v[52:55], v[72:75], v[196:199], v[52:55]\n"
    "s_add_u32 s69, s76, 39936\n"
    "v_mfma_f32_16x16x32_bf16 v[60:63], v[76:79], v[196:199], v[60:63]\n"
    "ds_read_b128 v[72:75], v1 offset:25600\n"
    "v_mfma_f32_16x16x32_bf16 v[56:59], v[76:79], v[192:195], v[56:59]\n"
    "v_mfma_f32_16x16x32_bf16 v[48:51], v[80:83], v[200:203], v[48:51]\n"
    "ds_read_b128 v[76:79], v1 offset:26624\n"
    "v_mfma_f32_16x16x32_bf16 v[52:55], v[80:83], v[204:207], v[52:55]\n"
    "buffer_load_dwordx4 v15, s[20:23], s69 offen offset:1024 lds\n"
    "v_mfma_f32_16x16x32_bf16 v[60:63], v[84:87], v[204:207], v[60:63]\n"
    "ds_read_b128 v[80:83], v1 offset:27648\n"
    "v_mfma_f32_16x16x32_bf16 v[56:59], v[84:87], v[200:203], v[56:59]\n"
    "s_waitcnt lgkmcnt(4)\n"
    "v_mfma_f32_16x16x32_bf16 v[48:51], v[88:91], v[208:211], v[48:51]\n"
    "ds_read_b128 v[84:87], v1 offset:28672\n"
    "s_add_u32 s69, s76, 47104\n"
    "v_mfma_f32_16x16x32_bf16 v[52:55], v[88:91], v[212:215], v[52:55]\n"
    "v_mfma_f32_16x16x32_bf16 v[60:63], v[92:95], v[212:215], v[60:63]\n"
    "ds_read_b128 v[88:91], v1 offset:29696\n"
    "v_mfma_f32_16x16x32_bf16 v[56:59], v[92:95], v[208:211], v[56:59]\n"
    "buffer_load_dwordx4 v15, s[20:23], s69 offen offset:2048 lds\n"
    "v_mfma_f32_16x16x32_bf16 v[48:51], v[96:99], v[216:219], v[48:51]\n"
    "ds_read_b128 v[92:95], v1 offset:30720\n"
    "v_mfma_f32_16x16x32_bf16 v[52:55], v[96:99], v[220:223], v[52:55]\n"
    "v_mfma_f32_16x16x32_bf16 v[60:63], v[64:67], v[220:223], v[60:63]\n"
    "ds_read_b128 v[96:99], v1 offset:31744\n"
    "v_mfma_f32_16x16x32_bf16 v[56:59], v[64:67], v[216:219], v[56:59]\n"
    "s_add_u32 s69, s76, 54272\n"
    "s_waitcnt lgkmcnt(4)\n"
    "v_mfma_f32_16x16x32_bf16 v[48:51], v[68:71], v[224:227], v[48:51]\n"
    "v_mfma_f32_16x16x32_bf16 v[52:55], v[68:71], v[228:231], v[52:55]\n"
    "v_mfma_f32_16x16x32_bf16 v[60:63], v[72:75], v[228:231], v[60:63]\n"
    "buffer_load_dwordx4 v15, s[20:23], s69 offen offset:3072 lds\n"
    "v_mfma_f32_16x16x32_bf16 v[56:59], v[72:75], v[224:227], v[56:59]\n"
    "v_mfma_f32_16x16x32_bf16 v[48:51], v[76:79], v[232:235], v[48:51]\n"
    "v_mfma_f32_16x16x32_bf16 v[52:55], v[76:79], v[236:239], v[52:55]\n"
    "v_mfma_f32_16x16x32_bf16 v[60:63], v[80:83], v[236:239], v[60:63]\n"
    "v_mfma_f32_16x16x32_bf16 v[56:59], v[80:83], v[232:235], v[56:59]\n"
    "s_waitcnt lgkmcnt(0)\n"
    "v_mfma_f32_16x16x32_bf16 v[48:51], v[84:87], v[240:243], v[48:51]\n"
    "v_mfma_f32_16x16x32_bf16 v[52:55], v[84:87], v[244:247], v[52:55]\n"
    "v_mfma_f32_16x16x32_bf16 v[60:63], v[88:91], v[244:247], v[60:63]\n"
    "v_mfma_f32_16x16x32_bf16 v[56:59], v[88:91], v[240:243], v[56:59]\n"
    "s_add_u32 s72, s64, 1839104\n"
    "v_mfma_f32_16x16x32_bf16 v[48:51], v[92:95], v[248:251], v[48:51]\n"
    "v_mfma_f32_16x16x32_bf16 v[52:55], v[92:95], v[252:255], v[52:55]\n"
    "s_waitcnt vmcnt(9)\n"
    "v_readfirstlane_b32 s29, v8\n"
    "s_nop 0\n"
    "s_cmp_lt_u32 s29, s95\n"
    "s_cbranch_scc1 Lslow43\n"
    "Lback43:\n"
    "s_barrier\n"
    "v_mfma_f32_16x16x32_bf16 v[60:63], v[96:99], v[252:255], v[60:63]\n"
    "buffer_load_dwordx4 v[160:163], v14, s[8:11], s72 offen sc1\n"
    "v_mfma_f32_16x16x32_bf16 v[56:59], v[96:99], v[248:251], v[56:59]\n"
    "s_add_u32 s71, s90, 1183744\n"
    "buffer_load_dwordx4 v[128:131], v0, s[8:11], s71 offen offset:0 sc1\n"
    "v_permlane16_swap_b32 v48, v52\n"
    "v_permlane16_swap_b32 v49, v53\n"
    "v_permlane16_swap_b32 v50, v54\n"
    "v_permlane16_swap_b32 v51, v55\n"
    "s_nop 1\n"
    "v_permlane16_swap_b32 v56, v60\n"
    "v_permlane16_swap_b32 v57, v61\n"
    "buffer_load_dwordx4 v[132:135], v0, s[8:11], s71 offen offset:1024 sc1\n"
    "v_permlane16_swap_b32 v58, v62\n"
    "v_permlane16_swap_b32_e64 v59, v63\n"
    "v_max3_f32 v80, v48, v49, v50\n"
    "v_max3_f32 v81, v56, v57, v58\n"
    "v_min3_f32 v82, v48, v49, v50\n"
    "v_min3_f32 v83, v56, v57, v58\n"
    "buffer_load_dwordx4 v[136:139], v0, s[8:11], s71 offen offset:2048 sc1\n"
    "v_max3_f32 v80, v80, v51, v52\n"
    "v_max3_f32 v81, v81, v59, v60\n"
    "v_min3_f32 v82, v82, v51, v52\n"
    "v_min3_f32 v83, v83, v59, v60\n"
    "v_max3_f32 v80, v80, v53, v54\n"
    "v_max3_f32 v81, v81, v61, v62\n"
    "buffer_load_dwordx4 v[140:143], v0, s[8:11], s71 offen offset:3072 sc1\n"
    "v_min3_f32 v82, v82, v53, v54\n"
    "v_min3_f32 v83, v83, v61, v62\n"
    "v_max_f32 v80, v80, v55\n"
    "v_max_f32 v81, v81, v63\n"
    "v_min_f32 v82, v82, v55\n"
    "v_min_f32 v83, v83, v63\n"
    "s_add_u32 s71, s90, 1187840\n"
    "buffer_load_dwordx4 v[144:147], v0, s[8:11], s71 offen offset:0 sc1\n"
    "v_permlane32_swap_b32 v80, v81\n"
    "v_permlane32_swap_b32 v82, v83\n"
    "v_max_f32 v80, v80, v81\n"
    "v_min_f32 v82, v82, v83\n"
    "v_mul_f32 v80, v80, v120\n"
    "v_mul_f32 v82, v82, v120\n"
    "buffer_load_dwordx4 v[148:151], v0, s[8:11], s71 offen offset:1024 sc1\n"
    "v_mov_b32 v81, v80\n"
    "v_mov_b32 v83, v82\n"
    "s_nop 0\n"
    "v_permlane32_swap_b32 v80, v81\n"
    "v_permlane32_swap_b32_e64 v82, v83\n"
    "v_pk_fma_f32 v[64:65], v[48:49], v[120:121], v[82:83] op_sel:[0,0,0] op_sel_hi:[1,0,0] neg_lo:[1,0,0] neg_hi:[1,0,0]\n"
    "v_pk_fma_f32 v[66:67], v[50:51], v[120:121], v[82:83] op_sel:[0,0,0] op_sel_hi:[1,0,0] neg_lo:[1,0,0] neg_hi:[1,0,0]\n"
    "buffer_load_dwordx4 v[152:155], v0, s[8:11], s71 offen offset:2048 sc1\n"
    "v_pk_fma_f32 v[68:69], v[52:53], v[120:121], v[82:83] op_sel:[0,0,0] op_sel_hi:[1,0,0] neg_lo:[1,0,0] neg_hi:[1,0,0]\n"
    "v_pk_fma_f32 v[70:71], v[54:55], v[120:121], v[82:83] op_sel:[0,0,0] op_sel_hi:[1,0,0] neg_lo:[1,0,0] neg_hi:[1,0,0]\n"
    "v_exp_f32 v64, v64\n"
    "v_pk_fma_f32 v[48:49], v[48:49], v[120:121], v[80:81] op_sel:[0,0,0] op_sel_hi:[1,0,0] neg_lo:[0,0,1] neg_hi:[0,0,1]\n"
    "v_exp_f32 v65, v65\n"
    "v_pk_fma_f32 v[50:51], v[50:51], v[120:121], v[80:81] op_sel:[0,0,0] op_sel_hi:[1,0,0] neg_lo:[0,0,1] neg_hi:[0,0,1]\n"
    "buffer_load_dwordx4 v[156:159], v0, s[8:11], s71 offen offset:3072 sc1\n"
    "v_exp_f32 v66, v66\n"
    "v_pk_fma_f32 v[52:53], v[52:53], v[120:121], v[80:81] op_sel:[0,0,0] op_sel_hi:[1,0,0] neg_lo:[0,0,1] neg_hi:[0,0,1]\n"
    "v_exp_f32 v67, v67\n"
    "v_pk_fma_f32 v[54:55], v[54:55], v[120:121], v[80:81] op_sel:[0,0,0] op_sel_hi:[1,0,0] neg_lo:[0,0,1] neg_hi:[0,0,1]\n"
    "v_exp_f32 v68, v68\n"
    "v_exp_f32 v69, v69\n"
    "s_add_u32 m0, s66, 32768\n"
    "s_add_u32 s69, s77, 0\n"
    "buffer_load_dwordx4 v0, s[8:11], s69 offen offset:0 sc1 lds\n"
    "v_exp_f32 v70, v70\n"
    "v_exp_f32 v71, v71\n"
    "v_exp_f32 v48, v48\n"
    "v_pk_fma_f32 v[72:73], v[56:57], v[120:121], v[82:83] op_sel:[0,0,1] op_sel_hi:[1,0,1] neg_lo:[1,0,0] neg_hi:[1,0,0]\n"
    "v_exp_f32 v49, v49\n"
    "v_pk_fma_f32 v[74:75], v[58:59], v[120:121], v[82:83] op_sel:[0,0,1] op_sel_hi:[1,0,1] neg_lo:[1,0,0] neg_hi:[1,0,0]\n"
    "v_exp_f32 v50, v50\n"
    "v_pk_fma_f32 v[76:77], v[60:61], v[120:121], v[82:83] op_sel:[0,0,1] op_sel_hi:[1,0,1] neg_lo:[1,0,0] neg_hi:[1,0,0]\n"
    "v_exp_f32 v51, v51\n"
    "v_pk_fma_f32 v[78:79], v[62:63], v[120:121], v[82:83] op_sel:[0,0,1] op_sel_hi:[1,0,1] neg_lo:[1,0,0] neg_hi:[1,0,0]\n"
    "v_exp_f32 v52, v52\n"
    "v_exp_f32 v53, v53\n"
    "v_exp_f32 v54, v54\n"
    "v_exp_f32 v55, v55\n"
    "v_exp_f32 v72, v72\n"
    "buffer_load_dwordx4 v0, s[8:11], s69 offen offset:1024 sc1 lds\n"
    "v_pk_fma_f32 v[56:57], v[56:57], v[120:121], v[80:81] op_sel:[0,0,1] op_sel_hi:[1,0,1] neg_lo:[0,0,1] neg_hi:[0,0,1]\n"
    "v_exp_f32 v73, v73\n"
    "v_pk_fma_f32 v[58:59], v[58:59], v[120:121], v[80:81] op_sel:[0,0,1] op_sel_hi:[1,0,1] neg_lo:[0,0,1] neg_hi:[0,0,1]\n"
    "v_exp_f32 v74, v74\n"
    "v_pk_fma_f32 v[60:61], v[60:61], v[120:121], v[80:81] op_sel:[0,0,1] op_sel_hi:[1,0,1] neg_lo:[0,0,1] neg_hi:[0,0,1]\n"
    "v_exp_f32 v75, v75\n"
    "v_pk_fma_f32 v[62:63], v[62:63], v[120:121], v[80:81] op_sel:[0,0,1] op_sel_hi:[1,0,1] neg_lo:[0,0,1] neg_hi:[0,0,1]\n"
    "v_exp_f32 v76, v76\n"
    "v_exp_f32 v77, v77\n"
    "v_exp_f32 v78, v78\n"
    "v_exp_f32 v79, v79\n"
    "v_exp_f32 v56, v56\n"
    "v_pk_add_f32 v[92:93], v[64:65], v[66:67] op_sel:[0,0] op_sel_hi:[1,1]\n"
    "v_exp_f32 v57, v57\n"
    "v_pk_add_f32 v[94:95], v[68:69], v[70:71] op_sel:[0,0] op_sel_hi:[1,1]\n"
    "buffer_load_dwordx4 v0, s[8:11], s69 offen offset:2048 sc1 lds\n"
    "Lpad8:\n"
    "s_cbranch_execz Lwarm0\n"
    "v_exp_f32 v58, v58\n"
    "v_pk_add_f32 v[84:85], v[48:49], v[50:51] op_sel:[0,0] op_sel_hi:[1,1]\n"
    "v_exp_f32 v59, v59\n"
    "v_pk_add_f32 v[86:87], v[52:53], v[54:55] op_sel:[0,0] op_sel_hi:[1,1]\n"
    "v_exp_f32 v60, v60\n"
    "v_pk_add_f32 v[96:97], v[72:73], v[74:75] op_sel:[0,0] op_sel_hi:[1,1]\n"
    "v_exp_f32 v61, v61\n"
    "v_pk_add_f32 v[98:99], v[76:77], v[78:79] op_sel:[0,0] op_sel_hi:[1,1]\n"
    "v_exp_f32 v62, v62\n"
    "v_exp_f32 v63, v63\n"
    "v_pk_add_f32 v[88:89], v[56:57], v[58:59] op_sel:[0,0] op_sel_hi:[1,1]\n"
    "v_pk_add_f32 v[90:91], v[60:61], v[62:63] op_sel:[0,0] op_sel_hi:[1,1]\n"
    "v_pk_add_f32 v[84:85], v[84:85], v[86:87] op_sel:[0,0] op_sel_hi:[1,1]\n"
    "v_pk_add_f32 v[88:89], v[88:89], v[90:91] op_sel:[0,0] op_sel_hi:[1,1]\n"
    "v_pk_add_f32 v[92:93], v[92:93], v[94:95] op_sel:[0,0] op_sel_hi:[1,1]\n"
    "buffer_load_dwordx4 v0, s[8:11], s69 offen offset:3072 sc1 lds\n"
    "v_pk_add_f32 v[96:97], v[96:97], v[98:99] op_sel:[0,0] op_sel_hi:[1,1]\n"
    "v_add_f32 v80, v84, v85\n"
    "v_add_f32 v81, v88, v89\n"
    "v_add_f32 v82, v92, v93\n"
    "v_add_f32 v83, v96, v97\n"
    "v_permlane32_swap_b32 v80, v81\n"
    "s_nop 0\n"
    "v_permlane32_swap_b32 v82, v83\n"
    "v_add_f32 v80, v80, v81\n"
    "v_add_f32 v82, v82, v83\n"
    "v_rcp_f32 v80, v80\n"
    "v_rcp_f32 v82, v82\n"
    "v_mov_b32 v81, v80\n"
    "v_mov_b32 v83, v82\n"
    "s_nop 0\n"
    "v_permlane32_swap_b32 v80, v81\n"
    "v_permlane32_swap_b32 v82, v83\n"
    "s_add_u32 m0, s66, 36864\n"
    "s_add_u32 s69, s77, 4096\n"
    "buffer_load_dwordx4 v0, s[8:11], s69 offen offset:0 sc1 lds\n"
    "v_pk_mul_f32 v[48:49], v[48:49], v[80:81] op_sel:[0,0] op_sel_hi:[1,0]\n"
    "v_pk_mul_f32 v[50:51], v[50:51], v[80:81] op_sel:[0,0] op_sel_hi:[1,0]\n"
    "v_pk_mul_f32 v[52:53], v[52:53], v[80:81] op_sel:[0,0] op_sel_hi:[1,0]\n"
    "v_pk_mul_f32 v[54:55], v[54:55], v[80:81] op_sel:[0,0] op_sel_hi:[1,0]\n"
    "v_cvt_pk_bf16_f32 v100, v48, v49\n"
    "v_cvt_pk_bf16_f32 v101, v50, v51\n"
    "v_cvt_pk_bf16_f32 v102, v52, v53\n"
    "v_cvt_pk_bf16_f32 v103, v54, v55\n"
    "v_pk_mul_f32 v[64:65], v[64:65], v[82:83] op_sel:[0,0] op_sel_hi:[1,0]\n"
    "v_pk_mul_f32 v[66:67], v[66:67], v[82:83] op_sel:[0,0] op_sel_hi:[1,0]\n"
    "v_pk_mul_f32 v[68:69], v[68:69], v[82:83] op_sel:[0,0] op_sel_hi:[1,0]\n"
    "v_pk_mul_f32 v[70:71], v[70:71], v[82:83] op_sel:[0,0] op_sel_hi:[1,0]\n"
    "v_cvt_pk_bf16_f32 v104, v64, v65\n"
    "v_cvt_pk_bf16_f32 v105, v66, v67\n"
    "v_cvt_pk_bf16_f32 v106, v68, v69\n"
    "buffer_load_dwordx4 v0, s[8:11], s69 offen offset:1024 sc1 lds\n"
    "v_cvt_pk_bf16_f32 v107, v70, v71\n"
    "v_pk_mul_f32 v[56:57], v[56:57], v[80:81] op_sel:[0,1] op_sel_hi:[1,1]\n"
    "v_pk_mul_f32 v[58:59], v[58:59], v[80:81] op_sel:[0,1] op_sel_hi:[1,1]\n"
    "v_pk_mul_f32 v[60:61], v[60:61], v[80:81] op_sel:[0,1] op_sel_hi:[1,1]\n"
    "v_pk_mul_f32 v[62:63], v[62:63], v[80:81] op_sel:[0,1] op_sel_hi:[1,1]\n"
    "v_cvt_pk_bf16_f32 v108, v56, v57\n"
    "v_cvt_pk_bf16_f32 v109, v58, v59\n"
    "v_cvt_pk_bf16_f32 v110, v60, v61\n"
    "v_cvt_pk_bf16_f32 v111, v62, v63\n"
    "v_pk_mul_f32 v[72:73], v[72:73], v[82:83] op_sel:[0,1] op_sel_hi:[1,1]\n"
    "v_pk_mul_f32 v[74:75], v[74:75], v[82:83] op_sel:[0,1] op_sel_hi:[1,1]\n"
    "v_pk_mul_f32 v[76:77], v[76:77], v[82:83] op_sel:[0,1] op_sel_hi:[1,1]\n"
    "v_pk_mul_f32 v[78:79], v[78:79], v[82:83] op_sel:[0,1] op_sel_hi:[1,1]\n"
    "v_cvt_pk_bf16_f32 v112, v72, v73\n"
    "v_cvt_pk_bf16_f32 v113, v74, v75\n"
    "v_cvt_pk_bf16_f32 v114, v76, v77\n"
    "v_cvt_pk_bf16_f32 v115, v78, v79\n"
    "v_accvgpr_read_b32 v80, a249\n"
    "v_accvgpr_read_b32 v81, a248\n"
    "v_mov_b32 v82, 0x3f80\n"
    "v_mov_b32 v83, 1.0\n"
    "Lredo45:\n"
    "s_waitcnt vmcnt(14)\n"
    "v_permlane32_swap_b32 v160, v161\n"
    "v_permlane32_swap_b32 v162, v163\n"
    "v_add_f32 v160, v160, v161\n"
    "v_add_f32 v162, v162, v163\n"
    "s_waitcnt vmcnt(13)\n"
    "v_lshlrev_b32 v16, 16, v128\n"
    "v_and_b32 v17, 0xffff0000, v128\n"
    "v_permlane16_swap_b32 v160, v162\n"
    "v_lshlrev_b32 v18, 16, v129\n"
    "v_and_b32 v19, 0xffff0000, v129\n"
    "v_add_f32 v160, v160, v162\n"
    "v_lshlrev_b32 v20, 16, v130\n"
    "v_and_b32 v21, 0xffff0000, v130\n"
    "v_add_f32_dpp v160, v160, v160 row_ror:4 row_mask:0xf bank_mask:0xf\n"
    "v_lshlrev_b32 v22, 16, v131\n"
    "v_and_b32 v23, 0xffff0000, v131\n"
    "v_add_f32_dpp v160, v160, v160 row_ror:8 row_mask:0xf bank_mask:0xf\n"
    "s_waitcnt vmcnt(12)\n"
    "v_dot2c_f32_bf16 v16, v82, v132\n"
    "v_dot2c_f32_bf16 v17, v83, v132\n"
    "v_cvt_pk_bf16_f32 v161, v160, v160\n"
    "v_dot2c_f32_bf16 v18, v82, v133\n"
    "v_dot2c_f32_bf16 v19, v83, v133\n"
    "ds_write_b16 v80, v161\n"
    "v_dot2c_f32_bf16 v20, v82, v134\n"
    "v_dot2c_f32_bf16 v21, v83, v134\n"
    "v_dot2c_f32_bf16 v22, v82, v135\n"
    "v_dot2c_f32_bf16 v23, v83, v135\n"
    "s_waitcnt vmcnt(11)\n"
    "v_dot2c_f32_bf16 v16, v82, v136\n"
    "v_dot2c_f32_bf16 v17, v83, v136\n"
    "v_dot2c_f32_bf16 v18, v82, v137\n"
    "v_dot2c_f32_bf16 v19, v83, v137\n"
    "v_dot2c_f32_bf16 v20, v82, v138\n"
    "v_dot2c_f32_bf16 v21, v83, v138\n"
    "v_dot2c_f32_bf16 v22, v82, v139\n"
    "v_dot2c_f32_bf16 v23, v83, v139\n"
    "s_waitcnt vmcnt(10)\n"
    "v_dot2c_f32_bf16 v16, v82, v140\n"
    "v_dot2c_f32_bf16 v17, v83, v140\n"
    "v_dot2c_f32_bf16 v18, v82, v141\n"
    "v_dot2c_f32_bf16 v19, v83, v141\n"
    "v_dot2c_f32_bf16 v20, v82, v142\n"
    "v_dot2c_f32_bf16 v21, v83, v142\n"
    "v_dot2c_f32_bf16 v22, v82, v143\n"
    "v_dot2c_f32_bf16 v23, v83, v143\n"
    "s_waitcnt vmcnt(9)\n"
    "v_dot2c_f32_bf16 v16, v82, v144\n"
    "v_dot2c_f32_bf16 v17, v83, v144\n"
    "v_dot2c_f32_bf16 v18, v82, v145\n"
    "v_dot2c_f32_bf16 v19, v83, v145\n"
    "v_dot2c_f32_bf16 v20, v82, v146\n"
    "v_dot2c_f32_bf16 v21, v83, v146\n"
    "v_dot2c_f32_bf16 v22, v82, v147\n"
    "v_dot2c_f32_bf16 v23, v83, v147\n"
    "s_waitcnt vmcnt(8)\n"
    "v_dot2c_f32_bf16 v16, v82, v148\n"
    "v_dot2c_f32_bf16 v17, v83, v148\n"
    "v_dot2c_f32_bf16 v18, v82, v149\n"
    "v_dot2c_f32_bf16 v19, v83, v149\n"
    "v_dot2c_f32_bf16 v20, v82, v150\n"
    "v_dot2c_f32_bf16 v21, v83, v150\n"
    "v_dot2c_f32_bf16 v22, v82, v151\n"
    "v_dot2c_f32_bf16 v23, v83, v151\n"
    "s_waitcnt vmcnt(7)\n"
    "v_dot2c_f32_bf16 v16, v82, v152\n"
    "v_dot2c_f32_bf16 v17, v83, v152\n"
    "v_dot2c_f32_bf16 v18, v82, v153\n"
    "v_dot2c_f32_bf16 v19, v83, v153\n"
    "v_dot2c_f32_bf16 v20, v82, v154\n"
    "v_dot2c_f32_bf16 v21, v83, v154\n"
    "v_dot2c_f32_bf16 v22, v82, v155\n"
    "v_dot2c_f32_bf16 v23, v83, v155\n"
    "s_waitcnt vmcnt(6)\n"
    "v_dot2c_f32_bf16 v16, v82, v156\n"
    "v_dot2c_f32_bf16 v17, v83, v156\n"
    "v_dot2c_f32_bf16 v18, v82, v157\n"
    "v_dot2c_f32_bf16 v19, v83, v157\n"
    "v_dot2c_f32_bf16 v20, v82, v158\n"
    "v_dot2c_f32_bf16 v21, v83, v158\n"
    "v_dot2c_f32_bf16 v22, v82, v159\n"
    "v_dot2c_f32_bf16 v23, v83, v159\n"
    "s_nop 2\n"
    "v_permlane32_swap_b32 v16, v20\n"
    "v_permlane32_swap_b32 v17, v21\n"
    "v_permlane32_swap_b32 v18, v22\n"
    "v_permlane32_swap_b32_e64 v19, v23\n"
    "v_pk_add_f32 v[16:17], v[16:17], v[20:21] op_sel:[0,0] op_sel_hi:[1,1]\n"
    "v_pk_add_f32 v[18:19], v[18:19], v[22:23] op_sel:[0,0] op_sel_hi:[1,1]\n"
    "v_cmp_u_f32 s[30:31], v16, v18\n"
    "v_cmp_u_f32 s[24:25], v160, v160\n"
    "v_cvt_pk_bf16_f32 v20, v16, v17\n"
    "v_cvt_pk_bf16_f32 v21, v18, v19\n"
    "ds_write_b64 v81, v[20:21]\n"
    "s_or_b64 s[30:31], s[30:31], s[24:25]\n"
    "s_cbranch_scc1 Lbad46\n"
    "Lok46:\n"
    "s_waitcnt lgkmcnt(0)\n"
    "s_barrier\n"
    "ds_read_b128 v[80:83], v0 offset:24576\n"
    "ds_read_b128 v[84:87], v0 offset:25600\n"
    "ds_read_b128 v[88:91], v0 offset:26624\n"
    "ds_read_b128 v[92:95], v0 offset:27648\n"
    "s_mov_b32 s65, 331776\n"
    "buffer_atomic_add a255, v3, s[8:11], s65 offen\n"
    "s_waitcnt lgkmcnt(3)\n"
    "v_mfma_f32_32x32x16_bf16 v[16:31], v[80:83], v[100:103], 0\n"
    "s_add_u32 m0, s66, 36864\n"
    "s_add_u32 s69, s77, 4096\n"
    "buffer_load_dwordx4 v0, s[8:11], s69 offen offset:2048 sc1 lds\n"
    "s_waitcnt lgkmcnt(2)\n"
    "v_mfma_f32_32x32x16_bf16 v[16:31], v[84:87], v[104:107], v[16:31]\n"
    "s_add_u32 m0, s66, 65536\n"
    "s_add_u32 s69, s77, 32768\n"
    "buffer_load_dwordx4 v0, s[8:11], s69 offen offset:0 sc1 lds\n"
    "s_waitcnt lgkmcnt(1)\n"
    "v_mfma_f32_32x32x16_bf16 v[32:47], v[88:91], v[108:111], 0\n"
    "buffer_load_dwordx4 v0, s[8:11], s69 offen offset:1024 sc1 lds\n"
    "s_waitcnt lgkmcnt(0)\n"
    "v_mfma_f32_32x32x16_bf16 v[32:47], v[92:95], v[112:115], v[32:47]\n"
    "buffer_load_dwordx4 v0, s[8:11], s69 offen offset:2048 sc1 lds\n"
    "s_add_u32 s73, s83, 20975616\n"
    "s_nop 1\n"
    "v_add_f32 v24, 0x358637bd, v24\n"
    "v_rcp_f32 v24, v24\n"
    "s_nop 0\n"
    "s_nop 0\n"
    "v_pk_mul_f32 v[16:17], v[16:17], v[24:25] op_sel:[0,0] op_sel_hi:[1,0]\n"
    "v_pk_mul_f32 v[18:19], v[18:19], v[24:25] op_sel:[0,0] op_sel_hi:[1,0]\n"
    "v_pk_mul_f32 v[20:21], v[20:21], v[24:25] op_sel:[0,0] op_sel_hi:[1,0]\n"
    "v_pk_mul_f32 v[22:23], v[22:23], v[24:25] op_sel:[0,0] op_sel_hi:[1,0]\n"
    "v_cvt_pk_bf16_f32 v28, v16, v17\n"
    "v_cvt_pk_bf16_f32 v29, v18, v19\n"
    "v_cvt_pk_bf16_f32 v30, v20, v21\n"
    "v_cvt_pk_bf16_f32 v31, v22, v23\n"
    "buffer_store_dwordx4 v[28:31], v11, s[8:11], s73 offen offset:0 sc1\n"
    "v_add_f32 v40, 0x358637bd, v40\n"
    "v_rcp_f32 v40, v40\n"
    "s_nop 0\n"
    "v_pk_mul_f32 v[32:33], v[32:33], v[40:41] op_sel:[0,0] op_sel_hi:[1,0]\n"
    "v_pk_mul_f32 v[34:35], v[34:35], v[40:41] op_sel:[0,0] op_sel_hi:[1,0]\n"
    "v_pk_mul_f32 v[36:37], v[36:37], v[40:41] op_sel:[0,0] op_sel_hi:[1,0]\n"
    "v_pk_mul_f32 v[38:39], v[38:39], v[40:41] op_sel:[0,0] op_sel_hi:[1,0]\n"
    "v_cvt_pk_bf16_f32 v44, v32, v33\n"
    "v_cvt_pk_bf16_f32 v45, v34, v35\n"
    "v_cvt_pk_bf16_f32 v46, v36, v37\n"
    "v_cvt_pk_bf16_f32 v47, v38, v39\n"
    "buffer_store_dwordx4 v[44:47], v11, s[8:11], s73 offen offset:512 sc1\n"
    "s_add_u32 s68, s94, 327680\n"
    "v_mov_b32 v8, 0\n"
    "buffer_atomic_add v8, v6, s[8:11], s68 offen sc0\n"
    "v_accvgpr_read_b32 v12, a253\n"
    "v_xor_b32 v13, 32, v12\n"
    "v_xor_b32_e64 v14, 64, v12\n"
    "v_xor_b32 v15, 0x60, v12\n"
    "s_add_u32 m0, s66, 36864\n"
    "s_add_u32 s69, s77, 4096\n"
    "buffer_load_dwordx4 v0, s[8:11], s69 offen offset:3072 sc1 lds\n"
    "v_accvgpr_read_b32 v16, a220\n"
    "v_accvgpr_read_b32 v17, a221\n"
    "v_accvgpr_read_b32 v18, a222\n"
    "v_accvgpr_read_b32 v19, a223\n"
    "v_accvgpr_read_b32 v20, a224\n"
    "v_accvgpr_read_b32 v21, a225\n"
    "v_accvgpr_read_b32 v22, a226\n"
    "v_accvgpr_read_b32 v23, a227\n"
    "v_accvgpr_read_b32 v24, a228\n"
    "v_accvgpr_read_b32 v25, a229\n"
    "v_accvgpr_read_b32 v26, a230\n"
    "v_accvgpr_read_b32 v27, a231\n"
    "v_accvgpr_read_b32 v28, a232\n"
    "v_accvgpr_read_b32 v29, a233\n"
    "v_accvgpr_read_b32 v30, a234\n"
    "v_accvgpr_read_b32 v31, a235\n"
    "ds_read_b64_tr_b16 v[48:49], v12 offset:0\n"
    "ds_read_b64_tr_b16 v[50:51], v12 offset:1024\n"
    "ds_read_b64_tr_b16 v[52:53], v13 offset:0\n"
    "ds_read_b64_tr_b16 v[54:55], v13 offset:1024\n"
    "ds_read_b64_tr_b16 v[56:57], v14 offset:0\n"
    "ds_read_b64_tr_b16 v[58:59], v14 offset:1024\n"
    "ds_read_b64_tr_b16 v[60:61], v15 offset:0\n"
    "ds_read_b64_tr_b16 v[62:63], v15 offset:1024\n"
    "ds_read_b64_tr_b16 v[64:65], v12 offset:4096\n"
    "ds_read_b64_tr_b16 v[66:67], v12 offset:5120\n"
    "ds_read_b64_tr_b16 v[68:69], v13 offset:4096\n"
    "v_mov_b32_e64 v9, 0\n"
    "buffer_atomic_add v9, v6, s[8:11], s68 offen sc0\n"
    "ds_read_b64_tr_b16 v[70:71], v13 offset:5120\n"
    "ds_read_b64_tr_b16 v[72:73], v14 offset:4096\n"
    "ds_read_b64_tr_b16 v[74:75], v14 offset:5120\n"
    "ds_read_b64_tr_b16 v[76:77], v15 offset:4096\n"
    "ds_read_b64_tr_b16 v[78:79], v15 offset:5120\n"
    "ds_read_b64_tr_b16 v[80:81], v12 offset:8192\n"
    "ds_read_b64_tr_b16 v[82:83], v12 offset:9216\n"
    "ds_read_b64_tr_b16 v[84:85], v13 offset:8192\n"
    "ds_read_b64_tr_b16 v[86:87], v13 offset:9216\n"
    "ds_read_b64_tr_b16 v[88:89], v14 offset:8192\n"
    "ds_read_b64_tr_b16 v[90:91], v14 offset:9216\n"
    "ds_read_b64_tr_b16 v[92:93], v15 offset:8192\n"
    "ds_read_b64_tr_b16 v[94:95], v15 offset:9216\n"
    "ds_read_b64_tr_b16 v[96:97], v12 offset:12288\n"
    "ds_read_b64_tr_b16 v[98:99], v12 offset:13312\n"
    "ds_read_b64_tr_b16 v[100:101], v13 offset:12288\n"
    "ds_read_b64_tr_b16 v[102:103], v13 offset:13312\n"
    "ds_read_b64_tr_b16 v[104:105], v14 offset:12288\n"
    "ds_read_b64_tr_b16 v[106:107], v14 offset:13312\n"
    "ds_read_b64_tr_b16 v[108:109], v15 offset:12288\n"
    "ds_read_b64_tr_b16 v[110:111], v15 offset:13312\n"
    "ds_read_b64_tr_b16 v[112:113], v12 offset:16384\n"
    "ds_read_b64_tr_b16 v[114:115], v12 offset:17408\n"
    "ds_read_b64_tr_b16 v[116:117], v13 offset:16384\n"
    "ds_read_b64_tr_b16 v[118:119], v13 offset:17408\n"
    "ds_read_b64_tr_b16 v[120:121], v14 offset:16384\n"
    "ds_read_b64_tr_b16 v[122:123], v14 offset:17408\n"
    "ds_read_b64_tr_b16 v[124:125], v15 offset:16384\n"
    "ds_read_b64_tr_b16 v[126:127], v15 offset:17408\n"
    "ds_read_b64_tr_b16 v[192:193], v12 offset:20480\n"
    "ds_read_b64_tr_b16 v[194:195], v12 offset:21504\n"
    "ds_read_b64_tr_b16 v[196:197], v13 offset:20480\n"
    "ds_read_b64_tr_b16 v[198:199], v13 offset:21504\n"
    "ds_read_b64_tr_b16 v[200:201], v14 offset:20480\n"
    "ds_read_b64_tr_b16 v[202:203], v14 offset:21504\n"
    "ds_read_b64_tr_b16 v[204:205], v15 offset:20480\n"
    "ds_read_b64_tr_b16 v[206:207], v15 offset:21504\n"
    "s_waitcnt vmcnt(2)\n"
    "v_readfirstlane_b32 s29, v8\n"
    "s_nop 0\n"
    "s_cmp_lt_u32 s29, s95\n"
    "s_cbranch_scc1 Lslow48\n"
    "Lback48:\n"
    "s_barrier\n"
    "s_add_u32 s71, s84, 20975616\n"
    "buffer_load_dwordx4 v[128:131], v0, s[8:11], s71 offen offset:0 sc1\n"
    "ds_read_b64_tr_b16 v[208:209], v12 offset:24576\n"
    "ds_read_b64_tr_b16 v[210:211], v12 offset:25600\n"
    "ds_read_b64_tr_b16 v[212:213], v13 offset:24576\n"
    "buffer_load_dwordx4 v[132:135], v0, s[8:11], s71 offen offset:1024 sc1\n"
    "ds_read_b64_tr_b16 v[214:215], v13 offset:25600\n"
    "ds_read_b64_tr_b16 v[216:217], v14 offset:24576\n"
    "ds_read_b64_tr_b16 v[218:219], v14 offset:25600\n"
    "buffer_load_dwordx4 v[136:139], v0, s[8:11], s71 offen offset:2048 sc1\n"
    "ds_read_b64_tr_b16 v[220:221], v15 offset:24576\n"
    "ds_read_b64_tr_b16 v[222:223], v15 offset:25600\n"
    "ds_read_b64_tr_b16 v[224:225], v12 offset:28672\n"
    "buffer_load_dwordx4 v[140:143], v0, s[8:11], s71 offen offset:3072 sc1\n"
    "ds_read_b64_tr_b16 v[226:227], v12 offset:29696\n"
    "ds_read_b64_tr_b16 v[228:229], v13 offset:28672\n"
    "ds_read_b64_tr_b16 v[230:231], v13 offset:29696\n"
    "s_add_u32 s71, s84, 20979712\n"
    "buffer_load_dwordx4 v[144:147], v0, s[8:11], s71 offen offset:0 sc1\n"
    "ds_read_b64_tr_b16 v[232:233], v14 offset:28672\n"
    "ds_read_b64_tr_b16 v[234:235], v14 offset:29696\n"
    "ds_read_b64_tr_b16 v[236:237], v15 offset:28672\n"
    "buffer_load_dwordx4 v[148:151], v0, s[8:11], s71 offen offset:1024 sc1\n"
    "ds_read_b64_tr_b16 v[238:239], v15 offset:29696\n"
    "ds_read_b64_tr_b16 v[240:241], v12 offset:32768\n"
    "ds_read_b64_tr_b16 v[242:243], v12 offset:33792\n"
    "buffer_load_dwordx4 v[152:155], v0, s[8:11], s71 offen offset:2048 sc1\n"
    "ds_read_b64_tr_b16 v[244:245], v13 offset:32768\n"
    "ds_read_b64_tr_b16 v[246:247], v13 offset:33792\n"
    "ds_read_b64_tr_b16 v[248:249], v14 offset:32768\n"
    "buffer_load_dwordx4 v[156:159], v0, s[8:11], s71 offen offset:3072 sc1\n"
    "ds_read_b64_tr_b16 v[250:251], v14 offset:33792\n"
    "ds_read_b64_tr_b16 v[252:253], v15 offset:32768\n"
    "ds_read_b64_tr_b16 v[254:255], v15 offset:33792\n"
    "s_add_u32 s71, s84, 20983808\n"
    "buffer_load_dwordx4 v[160:163], v0, s[8:11], s71 offen offset:0 sc1\n"
    "ds_read_b64_tr_b16 v[32:33], v12 offset:36864\n"
    "ds_read_b64_tr_b16 v[34:35], v12 offset:37888\n"
    "ds_read_b64_tr_b16 v[36:37], v13 offset:36864\n"
    "buffer_load_dwordx4 v[164:167], v0, s[8:11], s71 offen offset:1024 sc1\n"
    "ds_read_b64_tr_b16 v[38:39], v13 offset:37888\n"
    "ds_read_b64_tr_b16 v[40:41], v14 offset:36864\n"
    "ds_read_b64_tr_b16 v[42:43], v14 offset:37888\n"
    "s_mov_b32 s65, 364544\n"
    "Lredo49:\n"
    "s_waitcnt lgkmcnt(15) vmcnt(9)\n"
    "v_mfma_f32_16x16x32_bf16 v[16:19], v[48:51], v[128:131], v[16:19]\n"
    "ds_read_b64_tr_b16 v[44:45], v15 offset:36864\n"
    "ds_read_b64_tr_b16 v[46:47], v15 offset:37888\n"
    "v_mfma_f32_16x16x32_bf16 v[20:23], v[52:55], v[128:131], v[20:23]\n"
    "ds_read_b64_tr_b16 v[48:49], v12 offset:40960\n"
    "ds_read_b64_tr_b16 v[50:51], v12 offset:41984\n"
    "v_mfma_f32_16x16x32_bf16 v[24:27], v[56:59], v[128:131], v[24:27]\n"
    "ds_read_b64_tr_b16 v[52:53], v13 offset:40960\n"
    "ds_read_b64_tr_b16 v[54:55], v13 offset:41984\n"
    "buffer_load_dwordx4 v[168:171], v0, s[8:11], s71 offen offset:2048 sc1\n"
    "v_mfma_f32_16x16x32_bf16 v[28:31], v[60:63], v[128:131], v[28:31]\n"
    "ds_read_b64_tr_b16 v[56:57], v14 offset:40960\n"
    "ds_read_b64_tr_b16 v[58:59], v14 offset:41984\n"
    "s_waitcnt vmcnt(9)\n"
    "v_mfma_f32_16x16x32_bf16 v[16:19], v[64:67], v[132:135], v[16:19]\n"
    "ds_read_b64_tr_b16 v[60:61], v15 offset:40960\n"
    "ds_read_b64_tr_b16 v[62:63], v15 offset:41984\n"
    "v_mfma_f32_16x16x32_bf16 v[20:23], v[68:71], v[132:135], v[20:23]\n"
    "ds_read_b64_tr_b16 v[64:65], v12 offset:45056\n"
    "ds_read_b64_tr_b16 v[66:67], v12 offset:46080\n"
    "v_mfma_f32_16x16x32_bf16 v[24:27], v[72:75], v[132:135], v[24:27]\n"
    "ds_read_b64_tr_b16 v[68:69], v13 offset:45056\n"
    "ds_read_b64_tr_b16 v[70:71], v13 offset:46080\n"
    "buffer_load_dwordx4 v[172:175], v0, s[8:11], s71 offen offset:3072 sc1\n"
    "v_mfma_f32_16x16x32_bf16 v[28:31], v[76:79], v[132:135], v[28:31]\n"
    "ds_read_b64_tr_b16 v[72:73], v14 offset:45056\n"
    "ds_read_b64_tr_b16 v[74:75], v14 offset:46080\n"
    "s_waitcnt vmcnt(9)\n"
    "v_mfma_f32_16x16x32_bf16 v[16:19], v[80:83], v[136:139], v[16:19]\n"
    "ds_read_b64_tr_b16 v[76:77], v15 offset:45056\n"
    "ds_read_b64_tr_b16 v[78:79], v15 offset:46080\n"
    "v_mfma_f32_16x16x32_bf16 v[20:23], v[84:87], v[136:139], v[20:23]\n"
    "ds_read_b64_tr_b16 v[80:81], v12 offset:49152\n"
    "ds_read_b64_tr_b16 v[82:83], v12 offset:50176\n"
    "s_add_u32 s71, s84, 20987904\n"
    "v_mfma_f32_16x16x32_bf16 v[24:27], v[88:91], v[136:139], v[24:27]\n"
    "ds_read_b64_tr_b16 v[84:85], v13 offset:49152\n"
    "ds_read_b64_tr_b16 v[86:87], v13 offset:50176\n"
    "buffer_load_dwordx4 v[176:179], v0, s[8:11], s71 offen offset:0 sc1\n"
    "v_mfma_f32_16x16x32_bf16 v[28:31], v[92:95], v[136:139], v[28:31]\n"
    "ds_read_b64_tr_b16 v[88:89], v14 offset:49152\n"
    "ds_read_b64_tr_b16 v[90:91], v14 offset:50176\n"
    "s_waitcnt vmcnt(9)\n"
    "v_mfma_f32_16x16x32_bf16 v[16:19], v[96:99], v[140:143], v[16:19]\n"
    "ds_read_b64_tr_b16 v[92:93], v15 offset:49152\n"
    "ds_read_b64_tr_b16 v[94:95], v15 offset:50176\n"
    "v_mfma_f32_16x16x32_bf16 v[20:23], v[100:103], v[140:143], v[20:23]\n"
    "ds_read_b64_tr_b16 v[96:97], v12 offset:53248\n"
    "ds_read_b64_tr_b16 v[98:99], v12 offset:54272\n"
    "v_mfma_f32_16x16x32_bf16 v[24:27], v[104:107], v[140:143], v[24:27]\n"
    "ds_read_b64_tr_b16 v[100:101], v13 offset:53248\n"
    "ds_read_b64_tr_b16 v[102:103], v13 offset:54272\n"
    "buffer_load_dwordx4 v[180:183], v0, s[8:11], s71 offen offset:1024 sc1\n"
    "v_mfma_f32_16x16x32_bf16 v[28:31], v[108:111], v[140:143], v[28:31]\n"
    "ds_read_b64_tr_b16 v[104:105], v14 offset:53248\n"
    "ds_read_b64_tr_b16 v[106:107], v14 offset:54272\n"
    "s_waitcnt vmcnt(9)\n"
    "v_mfma_f32_16x16x32_bf16 v[16:19], v[112:115], v[144:147], v[16:19]\n"
    "ds_read_b64_tr_b16 v[108:109], v15 offset:53248\n"
    "ds_read_b64_tr_b16 v[110:111], v15 offset:54272\n"
    "v_mfma_f32_16x16x32_bf16 v[20:23], v[116:119], v[144:147], v[20:23]\n"
    "ds_read_b64_tr_b16 v[112:113], v12 offset:57344\n"
    "ds_read_b64_tr_b16 v[114:115], v12 offset:58368\n"
    "v_mfma_f32_16x16x32_bf16 v[24:27], v[120:123], v[144:147], v[24:27]\n"
    "ds_read_b64_tr_b16 v[116:117], v13 offset:57344\n"
    "ds_read_b64_tr_b16 v[118:119], v13 offset:58368\n"
    "buffer_load_dwordx4 v[184:187], v0, s[8:11], s71 offen offset:2048 sc1\n"
    "v_mfma_f32_16x16x32_bf16 v[28:31], v[124:127], v[144:147], v[28:31]\n"
    "ds_read_b64_tr_b16 v[120:121], v14 offset:57344\n"
    "ds_read_b64_tr_b16 v[122:123], v14 offset:58368\n"
    "s_waitcnt vmcnt(9)\n"
    "v_mfma_f32_16x16x32_bf16 v[16:19], v[192:195], v[148:151], v[16:19]\n"
    "ds_read_b64_tr_b16 v[124:125], v15 offset:57344\n"
    "ds_read_b64_tr_b16 v[126:127], v15 offset:58368\n"
    "v_mfma_f32_16x16x32_bf16 v[20:23], v[196:199], v[148:151], v[20:23]\n"
    "ds_read_b64_tr_b16 v[192:193], v12 offset:61440\n"
    "ds_read_b64_tr_b16 v[194:195], v12 offset:62464\n"
    "v_mfma_f32_16x16x32_bf16 v[24:27], v[200:203], v[148:151], v[24:27]\n"
    "ds_read_b64_tr_b16 v[196:197], v13 offset:61440\n"
    "ds_read_b64_tr_b16 v[198:199], v13 offset:62464\n"
    "buffer_load_dwordx4 v[188:191], v0, s[8:11], s71 offen offset:3072 sc1\n"
    "v_mfma_f32_16x16x32_bf16 v[28:31], v[204:207], v[148:151], v[28:31]\n"
    "ds_read_b64_tr_b16 v[200:201], v14 offset:61440\n"
    "ds_read_b64_tr_b16 v[202:203], v14 offset:62464\n"
    "s_waitcnt lgkmcnt(15) vmcnt(9)\n"
    "v_mfma_f32_16x16x32_bf16 v[16:19], v[208:211], v[152:155], v[16:19]\n"
    "ds_read_b64_tr_b16 v[204:205], v15 offset:61440\n"
    "ds_read_b64_tr_b16 v[206:207], v15 offset:62464\n"
    "v_mfma_f32_16x16x32_bf16 v[20:23], v[212:215], v[152:155], v[20:23]\n"
    "s_add_u32 m0, s66, 65536\n"
    "s_add_u32 s69, s77, 32768\n"
    "v_mfma_f32_16x16x32_bf16 v[24:27], v[216:219], v[152:155], v[24:27]\n"
    "buffer_load_dwordx4 v0, s[8:11], s69 offen offset:3072 sc1 lds\n"
    "v_mfma_f32_16x16x32_bf16 v[28:31], v[220:223], v[152:155], v[28:31]\n"
    "s_add_u32 m0, s66, 69632\n"
    "s_add_u32 s69, s77, 36864\n"
    "s_waitcnt vmcnt(9)\n"
    "v_mfma_f32_16x16x32_bf16 v[16:19], v[224:227], v[156:159], v[16:19]\n"
    "buffer_load_dwordx4 v0, s[8:11], s69 offen offset:0 sc1 lds\n"
    "v_mfma_f32_16x16x32_bf16 v[20:23], v[228:231], v[156:159], v[20:23]\n"
    "v_mfma_f32_16x16x32_bf16 v[24:27], v[232:235], v[156:159], v[24:27]\n"
    "v_mfma_f32_16x16x32_bf16 v[28:31], v[236:239], v[156:159], v[28:31]\n"
    "s_waitcnt vmcnt(9)\n"
    "v_mfma_f32_16x16x32_bf16 v[16:19], v[240:243], v[160:163], v[16:19]\n"
    "buffer_load_dwordx4 v0, s[8:11], s69 offen offset:1024 sc1 lds\n"
    "v_mfma_f32_16x16x32_bf16 v[20:23], v[244:247], v[160:163], v[20:23]\n"
    "v_mfma_f32_16x16x32_bf16 v[24:27], v[248:251], v[160:163], v[24:27]\n"
    "v_mfma_f32_16x16x32_bf16 v[28:31], v[252:255], v[160:163], v[28:31]\n"
    "s_waitcnt vmcnt(9)\n"
    "v_mfma_f32_16x16x32_bf16 v[16:19], v[32:35], v[164:167], v[16:19]\n"
    "buffer_atomic_add a255, v3, s[8:11], s65 offen\n"
    "s_mov_b32 s65, 2.0\n"
    "v_mfma_f32_16x16x32_bf16 v[20:23], v[36:39], v[164:167], v[20:23]\n"
    "buffer_load_dwordx4 v0, s[8:11], s69 offen offset:2048 sc1 lds\n"
    "v_mfma_f32_16x16x32_bf16 v[24:27], v[40:43], v[164:167], v[24:27]\n"
    "s_add_u32 m0, s66, 0\n"
    "s_add_u32 s69, s78, 0\n"
    "v_mfma_f32_16x16x32_bf16 v[28:31], v[44:47], v[164:167], v[28:31]\n"
    "s_waitcnt vmcnt(10)\n"
    "v_mfma_f32_16x16x32_bf16 v[16:19], v[48:51], v[168:171], v[16:19]\n"
    "buffer_load_dwordx4 v0, s[8:11], s69 offen offset:0 sc1 lds\n"
    "v_mfma_f32_16x16x32_bf16 v[20:23], v[52:55], v[168:171], v[20:23]\n"
    "v_mfma_f32_16x16x32_bf16 v[24:27], v[56:59], v[168:171], v[24:27]\n"
    "v_mfma_f32_16x16x32_bf16 v[28:31], v[60:63], v[168:171], v[28:31]\n"
    "s_waitcnt vmcnt(10)\n"
    "v_mfma_f32_16x16x32_bf16 v[16:19], v[64:67], v[172:175], v[16:19]\n"
    "buffer_load_dwordx4 v0, s[8:11], s69 offen offset:1024 sc1 lds\n"
    "v_mfma_f32_16x16x32_bf16 v[20:23], v[68:71], v[172:175], v[20:23]\n"
    "v_mfma_f32_16x16x32_bf16 v[24:27], v[72:75], v[172:175], v[24:27]\n"
    "v_mfma_f32_16x16x32_bf16 v[28:31], v[76:79], v[172:175], v[28:31]\n"
    "s_waitcnt lgkmcnt(15) vmcnt(10)\n"
    "v_mfma_f32_16x16x32_bf16 v[16:19], v[80:83], v[176:179], v[16:19]\n"
    "buffer_load_dwordx4 v0, s[8:11], s69 offen offset:2048 sc1 lds\n"
    "v_mfma_f32_16x16x32_bf16 v[20:23], v[84:87], v[176:179], v[20:23]\n"
    "v_mfma_f32_16x16x32_bf16 v[24:27], v[88:91], v[176:179], v[24:27]\n"
    "v_mfma_f32_16x16x32_bf16 v[28:31], v[92:95], v[176:179], v[28:31]\n"
    "s_waitcnt vmcnt(10)\n"
    "v_mfma_f32_16x16x32_bf16 v[16:19], v[96:99], v[180:183], v[16:19]\n"
    "buffer_load_dwordx4 v0, s[8:11], s69 offen offset:3072 sc1 lds\n"
    "v_mfma_f32_16x16x32_bf16 v[20:23], v[100:103], v[180:183], v[20:23]\n"
    "v_mfma_f32_16x16x32_bf16 v[24:27], v[104:107], v[180:183], v[24:27]\n"
    "v_mfma_f32_16x16x32_bf16 v[28:31], v[108:111], v[180:183], v[28:31]\n"
    "s_waitcnt lgkmcnt(0) vmcnt(10)\n"
    "v_mfma_f32_16x16x32_bf16 v[16:19], v[112:115], v[184:187], v[16:19]\n"
    "v_mfma_f32_16x16x32_bf16 v[20:23], v[116:119], v[184:187], v[20:23]\n"
    "v_mfma_f32_16x16x32_bf16 v[24:27], v[120:123], v[184:187], v[24:27]\n"
    "v_mfma_f32_16x16x32_bf16 v[28:31], v[124:127], v[184:187], v[28:31]\n"
    "s_waitcnt vmcnt(9)\n"
    "v_mfma_f32_16x16x32_bf16 v[16:19], v[192:195], v[188:191], v[16:19]\n"
    "v_mfma_f32_16x16x32_bf16 v[20:23], v[196:199], v[188:191], v[20:23]\n"
    "v_mfma_f32_16x16x32_bf16 v[24:27], v[200:203], v[188:191], v[24:27]\n"
    "v_mfma_f32_16x16x32_bf16 v[28:31], v[204:207], v[188:191], v[28:31]\n"
    "s_nop 4\n"
    "v_cmp_u_f32 s[30:31], v16, v16\n"
    "v_cvt_pk_bf16_f32 v80, v16, v17\n"
    "v_cvt_pk_bf16_f32 v81, v18, v19\n"
    "v_cvt_pk_bf16_f32 v82, v20, v21\n"
    "v_cvt_pk_bf16_f32 v83, v22, v23\n"
    "v_cvt_pk_bf16_f32 v84, v24, v25\n"
    "v_cvt_pk_bf16_f32 v85, v26, v27\n"
    "v_cvt_pk_bf16_f32 v86, v28, v29\n"
    "v_cvt_pk_bf16_f32 v87, v30, v31\n"
    "s_or_b64 s[30:31], s[30:31], s[30:31]\n"
    "s_cbranch_scc1 Lbad50\n"
    "Lok50:\n"
    "s_add_u32 s73, s81, 16650240\n"
    "buffer_store_dwordx4 v[80:83], v0, s[8:11], s73 offen sc1\n"
    "buffer_store_dwordx4 v[84:87], v0, s[8:11], s73 offen offset:2048 sc1\n"
    "s_add_u32 s68, s94, 360448\n"
    "v_mov_b32 v8, 0\n"
    "buffer_atomic_add v8, v6, s[8:11], s68 offen sc0\n"
    "s_barrier\n"
    "v_mov_b32 v88, 0x3f803f80\n"
    "s_add_u32 m0, s66, 69632\n"
    "s_add_u32 s69, s77, 36864\n"
    "buffer_load_dwordx4 v0, s[8:11], s69 offen offset:3072 sc1 lds\n"
    "v_mov_b32 v92, 0\n"
    "v_mov_b32 v93, 0\n"
    "v_dot2c_f32_bf16 v92, v88, v80\n"
    "v_dot2c_f32_bf16 v93, v80, v80\n"
    "v_dot2c_f32_bf16 v92, v88, v81\n"
    "v_dot2c_f32_bf16 v93, v81, v81\n"
    "v_dot2c_f32_bf16 v92, v88, v82\n"
    "v_dot2c_f32_bf16 v93, v82, v82\n"
    "v_dot2c_f32_bf16 v92, v88, v83\n"
    "v_dot2c_f32_bf16 v93, v83, v83\n"
    "v_dot2c_f32_bf16 v92, v88, v84\n"
    "v_dot2c_f32_bf16 v93, v84, v84\n"
    "v_dot2c_f32_bf16 v92, v88, v85\n"
    "v_dot2c_f32_bf16 v93, v85, v85\n"
    "v_dot2c_f32_bf16 v92, v88, v86\n"
    "v_dot2c_f32_bf16 v93, v86, v86\n"
    "v_dot2c_f32_bf16 v92, v88, v87\n"
    "v_dot2c_f32_bf16 v93, v87, v87\n"
    "v_mul_u32_u24 v7, 17, v0\n"
    "v_cmp_gt_u32 vcc, 0x100, v0\n"
    "s_nop 0\n"
    "v_permlane32_swap_b32 v92, v93\n"
    "v_add_f32 v92, v92, v93\n"
    "v_mov_b32 v93, v92\n"
    "v_bfe_u32 v7, v7, 7, 4\n"
    "v_lshlrev_b32 v7, 3, v7\n"
    "v_permlane16_swap_b32 v92, v93\n"
    "v_add_f32 v92, v92, v93\n"
    "v_mov_b32 v93, v92\n"
    "v_cndmask_b32 v7, 2.0, v7, vcc\n"
    "s_add_u32 s73, s82, 16650240\n"
    "v_permlane32_swap_b32_e64 v92, v93\n"
    "buffer_store_dwordx2 v[92:93], v7, s[8:11], s73 offen sc1\n"
    "v_accvgpr_write_b32 a220, v16\n"
    "v_accvgpr_write_b32 a221, v17\n"
    "v_accvgpr_write_b32 a222, v18\n"
    "v_accvgpr_write_b32 a223, v19\n"
    "v_accvgpr_write_b32 a224, v20\n"
    "v_accvgpr_write_b32 a225, v21\n"
    "v_accvgpr_write_b32 a226, v22\n"
    "v_accvgpr_write_b32 a227, v23\n"
    "v_accvgpr_write_b32 a228, v24\n"
    "v_accvgpr_write_b32 a229, v25\n"
    "v_accvgpr_write_b32 a230, v26\n"
    "v_accvgpr_write_b32 a231, v27\n"
    "v_accvgpr_write_b32 a232, v28\n"
    "v_accvgpr_write_b32 a233, v29\n"
    "v_accvgpr_write_b32 a234, v30\n"
    "v_accvgpr_write_b32 a235, v31\n"
    "v_and_b32 v15, 0x200, v0\n"
    "v_add_u32 v15, v0, v15\n"
    "v_xor_b32 v15, s92, v15\n"
    "s_and_b32 s63, s60, 4\n"
    "s_lshl_b32 s63, s63, 8\n"
    "v_and_b32 v14, 0x200, v0\n"
    "v_add_u32 v14, v0, v14\n"
    "v_xor_b32 v14, s63, v14\n"
    "v_cndmask_b32 v14, v14, 2.0, s[54:55]\n"
    "s_waitcnt vmcnt(2)\n"
    "v_readfirstlane_b32 s29, v8\n"
    "s_nop 0\n"
    "s_cmp_lt_u32 s29, s95\n"
    "s_cbranch_scc1 Lslow51\n"
    "Lback51:\n"
    "s_barrier\n"
    "s_add_u32 s71, s80, 16650240\n"
    "buffer_load_dwordx4 v[128:131], v0, s[8:11], s71 offen offset:0 sc1\n"
    "buffer_load_dwordx4 v[132:135], v0, s[8:11], s71 offen offset:1024 sc1\n"
    "buffer_load_dwordx4 v[136:139], v0, s[8:11], s71 offen offset:2048 sc1\n"
    "buffer_load_dwordx4 v[140:143], v0, s[8:11], s71 offen offset:3072 sc1\n"
    "s_add_u32 s71, s80, 16654336\n"
    "buffer_load_dwordx4 v[144:147], v0, s[8:11], s71 offen offset:0 sc1\n"
    "buffer_load_dwordx4 v[148:151], v0, s[8:11], s71 offen offset:1024 sc1\n"
    "buffer_load_dwordx4 v[152:155], v0, s[8:11], s71 offen offset:2048 sc1\n"
    "buffer_load_dwordx4 v[156:159], v0, s[8:11], s71 offen offset:3072 sc1\n"
    "s_add_u32 s71, s80, 16658432\n"
    "buffer_load_dwordx4 v[160:163], v0, s[8:11], s71 offen offset:0 sc1\n"
    "buffer_load_dwordx4 v[164:167], v0, s[8:11], s71 offen offset:1024 sc1\n"
    "s_waitcnt vmcnt(11)\n"
    "s_barrier\n"
    "Lredo54:\n"
    "ds_read_b128 v[80:83], v0 offset:32768\n"
    "ds_read_b128 v[84:87], v0 offset:33792\n"
    "ds_read_b128 v[88:91], v1 offset:0\n"
    "ds_read_b128 v[92:95], v1 offset:1024\n"
    "Lpad9:\n"
    "s_cbranch_execz Lwarm0\n"
    "ds_read_b128 v[96:99], v0 offset:0\n"
    "ds_read_b128 v[64:67], v0 offset:34816\n"
    "ds_read_b128 v[68:71], v0 offset:35840\n"
    "ds_read_b128 v[72:75], v1 offset:2048\n"
    "buffer_load_dwordx4 v[168:171], v0, s[8:11], s71 offen offset:2048 sc1\n"
    "buffer_load_dwordx4 v[172:175], v0, s[8:11], s71 offen offset:3072 sc1\n"
    "s_waitcnt vmcnt(11)\n"
    "v_max3_u32 v7, v128, v129, v130\n"
    "v_max_u32 v7, v7, v131\n"
    "s_waitcnt vmcnt(10)\n"
    "v_max3_u32 v7, v7, v132, v133\n"
    "v_max3_u32 v7, v7, v134, v135\n"
    "v_cmp_eq_u32 s[26:27], -1, v7\n"
    "s_nop 0\n"
    "s_cmp_lg_u64 s[26:27], 0\n"
    "s_cbranch_scc1 Lgb56\n"
    "Lgo56:\n"
    "s_waitcnt lgkmcnt(3)\n"
    "v_mfma_f32_16x16x32_bf16 v[16:19], v[80:83], v[128:131], 0\n"
    "ds_read_b128 v[76:79], v1 offset:3072\n"
    "s_add_u32 s71, s80, 16662528\n"
    "v_mfma_f32_16x16x32_bf16 v[24:27], v[80:83], v[132:135], 0\n"
    "buffer_load_dwordx4 v[176:179], v0, s[8:11], s71 offen offset:0 sc1\n"
    "v_mfma_f32_16x16x32_bf16 v[28:31], v[84:87], v[132:135], 0\n"
    "ds_read_b128 v[80:83], v0 offset:1024\n"
    "v_mfma_f32_16x16x32_bf16 v[20:23], v[84:87], v[128:131], 0\n"
    "v_mfma_f32_16x16x32_bf16 v[32:35], v[88:91], v[128:131], 0\n"
    "ds_read_b128 v[84:87], v0 offset:36864\n"
    "s_waitcnt vmcnt(10)\n"
    "v_max3_u32 v7, v136, v137, v138\n"
    "v_mfma_f32_16x16x32_bf16 v[40:43], v[88:91], v[132:135], 0\n"
    "buffer_load_dwordx4 v[180:183], v0, s[8:11], s71 offen offset:1024 sc1\n"
    "v_max_u32 v7, v7, v139\n"
    "v_mfma_f32_16x16x32_bf16 v[44:47], v[92:95], v[132:135], 0\n"
    "ds_read_b128 v[88:91], v0 offset:37888\n"
    "s_waitcnt vmcnt(10)\n"
    "v_max3_u32 v7, v7, v140, v141\n"
    "v_mfma_f32_16x16x32_bf16 v[36:39], v[92:95], v[128:131], 0\n"
    "v_max3_u32 v7, v7, v142, v143\n"
    "v_cmp_eq_u32 s[26:27], -1, v7\n"
    "v_mfma_f32_16x16x32_bf16 v[48:51], v[96:99], v[128:131], 0\n"
    "ds_read_b128 v[92:95], v1 offset:4096\n"
    "s_cmp_lg_u64 s[26:27], 0\n"
    "s_cbranch_scc1 Lgb57\n"
    "Lgo57:\n"
    "v_mfma_f32_16x16x32_bf16 v[56:59], v[96:99], v[132:135], 0\n"
    "s_waitcnt lgkmcnt(3)\n"
    "v_mfma_f32_16x16x32_bf16 v[24:27], v[64:67], v[140:143], v[24:27]\n"
    "ds_read_b128 v[96:99], v1 offset:5120\n"
    "buffer_load_dwordx4 v[184:187], v0, s[8:11], s71 offen offset:2048 sc1\n"
    "v_mfma_f32_16x16x32_bf16 v[16:19], v[64:67], v[136:139], v[16:19]\n"
    "v_mfma_f32_16x16x32_bf16 v[20:23], v[68:71], v[136:139], v[20:23]\n"
    "ds_read_b128 v[64:67], v0 offset:2048\n"
    "v_mfma_f32_16x16x32_bf16 v[28:31], v[68:71], v[140:143], v[28:31]\n"
    "buffer_load_dwordx4 v[188:191], v0, s[8:11], s71 offen offset:3072 sc1\n"
    "v_mfma_f32_16x16x32_bf16 v[40:43], v[72:75], v[140:143], v[40:43]\n"
    "ds_read_b128 v[68:71], v0 offset:38912\n"
    "s_add_u32 s72, s80, 16683008\n"
    "v_mfma_f32_16x16x32_bf16 v[32:35], v[72:75], v[136:139], v[32:35]\n"
    "v_mfma_f32_16x16x32_bf16 v[36:39], v[76:79], v[136:139], v[36:39]\n"
    "ds_read_b128 v[72:75], v0 offset:39936\n"
    "v_mfma_f32_16x16x32_bf16 v[44:47], v[76:79], v[140:143], v[44:47]\n"
    "buffer_load_dwordx4 v[100:103], v0, s[8:11], s72 offen offset:0 sc1\n"
    "v_mfma_f32_16x16x32_bf16 v[56:59], v[80:83], v[140:143], v[56:59]\n"
    "ds_read_b128 v[76:79], v1 offset:6144\n"
    "v_mfma_f32_16x16x32_bf16 v[48:51], v[80:83], v[136:139], v[48:51]\n"
    "s_add_u32 s71, s80, 16666624\n"
    "s_waitcnt vmcnt(11) lgkmcnt(3)\n"
    "v_mfma_f32_16x16x32_bf16 v[16:19], v[84:87], v[144:147], v[16:19]\n"
    "ds_read_b128 v[80:83], v1 offset:7168\n"
    "v_mfma_f32_16x16x32_bf16 v[24:27], v[84:87], v[148:151], v[24:27]\n"
    "v_mfma_f32_16x16x32_bf16 v[28:31], v[88:91], v[148:151], v[28:31]\n"
    "ds_read_b128 v[84:87], v0 offset:3072\n"
    "v_mfma_f32_16x16x32_bf16 v[20:23], v[88:91], v[144:147], v[20:23]\n"
    "buffer_load_dwordx4 v[192:195], v0, s[8:11], s71 offen offset:0 sc1\n"
    "v_mfma_f32_16x16x32_bf16 v[32:35], v[92:95], v[144:147], v[32:35]\n"
    "ds_read_b128 v[88:91], v0 offset:40960\n"
    "v_mfma_f32_16x16x32_bf16 v[40:43], v[92:95], v[148:151], v[40:43]\n"
    "v_mfma_f32_16x16x32_bf16 v[44:47], v[96:99], v[148:151], v[44:47]\n"
    "ds_read_b128 v[92:95], v0 offset:41984\n"
    "v_mfma_f32_16x16x32_bf16 v[36:39], v[96:99], v[144:147], v[36:39]\n"
    "buffer_load_dwordx4 v[196:199], v0, s[8:11], s71 offen offset:1024 sc1\n"
    "v_mfma_f32_16x16x32_bf16 v[48:51], v[64:67], v[144:147], v[48:51]\n"
    "ds_read_b128 v[96:99], v1 offset:8192\n"
    "v_mfma_f32_16x16x32_bf16 v[56:59], v[64:67], v[148:151], v[56:59]\n"
    "s_waitcnt vmcnt(11) lgkmcnt(3)\n"
    "v_mfma_f32_16x16x32_bf16 v[24:27], v[68:71], v[156:159], v[24:27]\n"
    "ds_read_b128 v[64:67], v1 offset:9216\n"
    "v_mfma_f32_16x16x32_bf16 v[16:19], v[68:71], v[152:155], v[16:19]\n"
    "buffer_load_dwordx4 v[104:107], v0, s[8:11], s72 offen offset:1024 sc1\n"
    "v_mfma_f32_16x16x32_bf16 v[20:23], v[72:75], v[152:155], v[20:23]\n"
    "ds_read_b128 v[68:71], v0 offset:8192\n"
    "v_mfma_f32_16x16x32_bf16 v[28:31], v[72:75], v[156:159], v[28:31]\n"
    "v_mfma_f32_16x16x32_bf16 v[40:43], v[76:79], v[156:159], v[40:43]\n"
    "ds_read_b128 v[72:75], v0 offset:43008\n"
    "v_mfma_f32_16x16x32_bf16 v[32:35], v[76:79], v[152:155], v[32:35]\n"
    "buffer_load_dwordx4 v[200:203], v0, s[8:11], s71 offen offset:2048 sc1\n"
    "v_mfma_f32_16x16x32_bf16 v[36:39], v[80:83], v[152:155], v[36:39]\n"
    "ds_read_b128 v[76:79], v0 offset:44032\n"
    "v_mfma_f32_16x16x32_bf16 v[44:47], v[80:83], v[156:159], v[44:47]\n"
    "v_mfma_f32_16x16x32_bf16 v[56:59], v[84:87], v[156:159], v[56:59]\n"
    "ds_read_b128 v[80:83], v1 offset:10240\n"
    "v_mfma_f32_16x16x32_bf16 v[48:51], v[84:87], v[152:155], v[48:51]\n"
    "s_waitcnt vmcnt(11) lgkmcnt(3)\n"
    "v_mfma_f32_16x16x32_bf16 v[16:19], v[88:91], v[160:163], v[16:19]\n"
    "ds_read_b128 v[84:87], v1 offset:11264\n"
    "v_mfma_f32_16x16x32_bf16 v[24:27], v[88:91], v[164:167], v[24:27]\n"
    "buffer_load_dwordx4 v[204:207], v0, s[8:11], s71 offen offset:3072 sc1\n"
    "v_mfma_f32_16x16x32_bf16 v[28:31], v[92:95], v[164:167], v[28:31]\n"
    "ds_read_b128 v[88:91], v0 offset:9216\n"
    "s_add_u32 s71, s80, 16670720\n"
    "v_mfma_f32_16x16x32_bf16 v[20:23], v[92:95], v[160:163], v[20:23]\n"
    "v_mfma_f32_16x16x32_bf16 v[32:35], v[96:99], v[160:163], v[32:35]\n"
    "ds_read_b128 v[92:95], v0 offset:45056\n"
    "v_mfma_f32_16x16x32_bf16 v[40:43], v[96:99], v[164:167], v[40:43]\n"
    "buffer_load_dwordx4 v[208:211], v0, s[8:11], s71 offen offset:0 sc1\n"
    "v_mfma_f32_16x16x32_bf16 v[44:47], v[64:67], v[164:167], v[44:47]\n"
    "ds_read_b128 v[96:99], v0 offset:46080\n"
    "v_mfma_f32_16x16x32_bf16 v[36:39], v[64:67], v[160:163], v[36:39]\n"
    "v_cmp_u_f32 s[30:31], v16, v24\n"
    "v_mfma_f32_16x16x32_bf16 v[48:51], v[68:71], v[160:163], v[48:51]\n"
    "ds_read_b128 v[64:67], v1 offset:12288\n"
    "v_mfma_f32_16x16x32_bf16 v[56:59], v[68:71], v[164:167], v[56:59]\n"
    "s_or_b64 s[30:31], s[30:31], s[30:31]\n"
    "s_cbranch_scc1 Lbad55\n"
    "Lok55:\n"
    "s_waitcnt vmcnt(11) lgkmcnt(3)\n"
    "v_mfma_f32_16x16x32_bf16 v[24:27], v[72:75], v[172:175], v[24:27]\n"
    "ds_read_b128 v[68:71], v1 offset:13312\n"
    "buffer_load_dwordx4 v[212:215], v0, s[8:11], s71 offen offset:1024 sc1\n"
    "v_mfma_f32_16x16x32_bf16 v[16:19], v[72:75], v[168:171], v[16:19]\n"
    "v_mfma_f32_16x16x32_bf16 v[20:23], v[76:79], v[168:171], v[20:23]\n"
    "ds_read_b128 v[72:75], v0 offset:10240\n"
    "v_mfma_f32_16x16x32_bf16 v[28:31], v[76:79], v[172:175], v[28:31]\n"
    "buffer_load_dwordx4 v[216:219], v0, s[8:11], s71 offen offset:2048 sc1\n"
    "v_mfma_f32_16x16x32_bf16 v[40:43], v[80:83], v[172:175], v[40:43]\n"
    "ds_read_b128 v[76:79], v0 offset:47104\n"
    "v_mfma_f32_16x16x32_bf16 v[32:35], v[80:83], v[168:171], v[32:35]\n"
    "v_mfma_f32_16x16x32_bf16 v[36:39], v[84:87], v[168:171], v[36:39]\n"
    "ds_read_b128 v[80:83], v0 offset:48128\n"
    "v_mfma_f32_16x16x32_bf16 v[44:47], v[84:87], v[172:175], v[44:47]\n"
    "buffer_load_dwordx4 v[220:223], v0, s[8:11], s71 offen offset:3072 sc1\n"
    "v_mfma_f32_16x16x32_bf16 v[56:59], v[88:91], v[172:175], v[56:59]\n"
    "ds_read_b128 v[84:87], v1 offset:14336\n"
    "v_mfma_f32_16x16x32_bf16 v[48:51], v[88:91], v[168:171], v[48:51]\n"
    "s_add_u32 s71, s80, 16674816\n"
    "s_waitcnt vmcnt(12) lgkmcnt(3)\n"
    "v_mfma_f32_16x16x32_bf16 v[16:19], v[92:95], v[176:179], v[16:19]\n"
    "ds_read_b128 v[88:91], v1 offset:15360\n"
    "v_mfma_f32_16x16x32_bf16 v[24:27], v[92:95], v[180:183], v[24:27]\n"
    "v_mfma_f32_16x16x32_bf16 v[28:31], v[96:99], v[180:183], v[28:31]\n"
    "ds_read_b128 v[92:95], v0 offset:11264\n"
    "v_mfma_f32_16x16x32_bf16 v[20:23], v[96:99], v[176:179], v[20:23]\n"
    "buffer_load_dwordx4 v[224:227], v0, s[8:11], s71 offen offset:0 sc1\n"
    "v_mfma_f32_16x16x32_bf16 v[32:35], v[64:67], v[176:179], v[32:35]\n"
    "ds_read_b128 v[96:99], v0 offset:49152\n"
    "v_mfma_f32_16x16x32_bf16 v[40:43], v[64:67], v[180:183], v[40:43]\n"
    "v_mfma_f32_16x16x32_bf16 v[44:47], v[68:71], v[180:183], v[44:47]\n"
    "ds_read_b128 v[64:67], v0 offset:50176\n"
    "v_mfma_f32_16x16x32_bf16 v[36:39], v[68:71], v[176:179], v[36:39]\n"
    "buffer_load_dwordx4 v[228:231], v0, s[8:11], s71 offen offset:1024 sc1\n"
    "v_mfma_f32_16x16x32_bf16 v[48:51], v[72:75], v[176:179], v[48:51]\n"
    "ds_read_b128 v[68:71], v1 offset:16384\n"
    "v_mfma_f32_16x16x32_bf16 v[56:59], v[72:75], v[180:183], v[56:59]\n"
    "s_waitcnt vmcnt(12) lgkmcnt(3)\n"
    "v_mfma_f32_16x16x32_bf16 v[24:27], v[76:79], v[188:191], v[24:27]\n"
    "ds_read_b128 v[72:75], v1 offset:17408\n"
    "v_mfma_f32_16x16x32_bf16 v[16:19], v[76:79], v[184:187], v[16:19]\n"
    "buffer_load_dwordx4 v[232:235], v0, s[8:11], s71 offen offset:2048 sc1\n"
    "v_mfma_f32_16x16x32_bf16 v[20:23], v[80:83], v[184:187], v[20:23]\n"
    "ds_read_b128 v[76:79], v0 offset:16384\n"
    "v_mfma_f32_16x16x32_bf16 v[28:31], v[80:83], v[188:191], v[28:31]\n"
    "v_mfma_f32_16x16x32_bf16 v[40:43], v[84:87], v[188:191], v[40:43]\n"
    "ds_read_b128 v[80:83], v0 offset:51200\n"
    "v_mfma_f32_16x16x32_bf16 v[32:35], v[84:87], v[184:187], v[32:35]\n"
    "buffer_load_dwordx4 v[236:239], v0, s[8:11], s71 offen offset:3072 sc1\n"
    "v_mfma_f32_16x16x32_bf16 v[36:39], v[88:91], v[184:187], v[36:39]\n"
    "ds_read_b128 v[84:87], v0 offset:52224\n"
    "v_mfma_f32_16x16x32_bf16 v[44:47], v[88:91], v[188:191], v[44:47]\n"
    "s_add_u32 s71, s80, 16678912\n"
    "v_mfma_f32_16x16x32_bf16 v[56:59], v[92:95], v[188:191], v[56:59]\n"
    "ds_read_b128 v[88:91], v1 offset:18432\n"
    "v_mfma_f32_16x16x32_bf16 v[48:51], v[92:95], v[184:187], v[48:51]\n"
    "s_waitcnt vmcnt(11) lgkmcnt(3)\n"
    "v_mfma_f32_16x16x32_bf16 v[16:19], v[96:99], v[192:195], v[16:19]\n"
    "ds_read_b128 v[92:95], v1 offset:19456\n"
    "v_mfma_f32_16x16x32_bf16 v[24:27], v[96:99], v[196:199], v[24:27]\n"
    "buffer_load_dwordx4 v[240:243], v0, s[8:11], s71 offen offset:0 sc1\n"
    "v_mfma_f32_16x16x32_bf16 v[28:31], v[64:67], v[196:199], v[28:31]\n"
    "ds_read_b128 v[96:99], v0 offset:17408\n"
    "v_mfma_f32_16x16x32_bf16 v[20:23], v[64:67], v[192:195], v[20:23]\n"
    "v_mfma_f32_16x16x32_bf16 v[32:35], v[68:71], v[192:195], v[32:35]\n"
    "ds_read_b128 v[64:67], v0 offset:53248\n"
    "v_mfma_f32_16x16x32_bf16 v[40:43], v[68:71], v[196:199], v[40:43]\n"
    "buffer_load_dwordx4 v[244:247], v0, s[8:11], s71 offen offset:1024 sc1\n"
    "v_mfma_f32_16x16x32_bf16 v[44:47], v[72:75], v[196:199], v[44:47]\n"
    "ds_read_b128 v[68:71], v0 offset:54272\n"
    "v_mfma_f32_16x16x32_bf16 v[36:39], v[72:75], v[192:195], v[36:39]\n"
    "v_mfma_f32_16x16x32_bf16 v[48:51], v[76:79], v[192:195], v[48:51]\n"
    "ds_read_b128 v[72:75], v1 offset:20480\n"
    "v_mfma_f32_16x16x32_bf16 v[56:59], v[76:79], v[196:199], v[56:59]\n"
    "s_waitcnt vmcnt(10) lgkmcnt(3)\n"
    "v_mfma_f32_16x16x32_bf16 v[24:27], v[80:83], v[204:207], v[24:27]\n"
    "ds_read_b128 v[76:79], v1 offset:21504\n"
    "buffer_load_dwordx4 v[248:251], v0, s[8:11], s71 offen offset:2048 sc1\n"
    "v_max3_u32 v110, v100, v101, v102\n"
    "v_max_u32 v110, v110, v103\n"
    "v_mfma_f32_16x16x32_bf16 v[16:19], v[80:83], v[200:203], v[16:19]\n"
    "v_max3_u32 v110, v110, v104, v105\n"
    "v_max3_u32 v110, v110, v106, v107\n"
    "v_mfma_f32_16x16x32_bf16 v[20:23], v[84:87], v[200:203], v[20:23]\n"
    "ds_read_b128 v[80:83], v0 offset:18432\n"
    "v_cmp_eq_u32 s[26:27], -1, v110\n"
    "v_mfma_f32_16x16x32_bf16 v[28:31], v[84:87], v[204:207], v[28:31]\n"
    "buffer_load_dwordx4 v[252:255], v0, s[8:11], s71 offen offset:3072 sc1\n"
    "v_mfma_f32_16x16x32_bf16 v[40:43], v[88:91], v[204:207], v[40:43]\n"
    "ds_read_b128 v[84:87], v0 offset:55296\n"
    "s_add_u32 m0, s66, 98304\n"
    "v_mfma_f32_16x16x32_bf16 v[32:35], v[88:91], v[200:203], v[32:35]\n"
    "s_add_u32 s69, s77, 65536\n"
    "s_cmp_lg_u64 s[26:27], 0\n"
    "s_cbranch_scc1 Lgb58\n"
    "Lgo58:\n"
    "v_mfma_f32_16x16x32_bf16 v[36:39], v[92:95], v[200:203], v[36:39]\n"
    "ds_read_b128 v[88:91], v0 offset:56320\n"
    "v_add_f32_dpp v112, v100, v100 row_ror:8 row_mask:0xf bank_mask:3\n"
    "v_mfma_f32_16x16x32_bf16 v[44:47], v[92:95], v[204:207], v[44:47]\n"
    "buffer_load_dwordx4 v0, s[8:11], s69 offen offset:0 sc1 lds\n"
    "v_mfma_f32_16x16x32_bf16 v[56:59], v[96:99], v[204:207], v[56:59]\n"
    "ds_read_b128 v[92:95], v1 offset:22528\n"
    "v_add_f32_dpp v113, v101, v101 row_ror:8 row_mask:0xf bank_mask:3\n"
    "v_mfma_f32_16x16x32_bf16 v[48:51], v[96:99], v[200:203], v[48:51]\n"
    "v_add_f32_dpp v114, v104, v104 row_ror:8 row_mask:0xf bank_mask:3\n"
    "s_waitcnt vmcnt(11) lgkmcnt(3)\n"
    "v_mfma_f32_16x16x32_bf16 v[16:19], v[64:67], v[208:211], v[16:19]\n"
    "ds_read_b128 v[96:99], v1 offset:23552\n"
    "v_add_f32_dpp v115, v105, v105 row_ror:8 row_mask:0xf bank_mask:3\n"
    "v_add_f32_dpp v112, v102, v102 row_ror:8 row_mask:0xf bank_mask:12\n"
    "v_mfma_f32_16x16x32_bf16 v[24:27], v[64:67], v[212:215], v[24:27]\n"
    "buffer_load_dwordx4 v0, s[8:11], s69 offen offset:1024 sc1 lds\n"
    "v_mfma_f32_16x16x32_bf16 v[28:31], v[68:71], v[212:215], v[28:31]\n"
    "ds_read_b128 v[64:67], v0 offset:19456\n"
    "v_add_f32_dpp v113, v103, v103 row_ror:8 row_mask:0xf bank_mask:12\n"
    "v_mfma_f32_16x16x32_bf16 v[20:23], v[68:71], v[208:211], v[20:23]\n"
    "buffer_load_dwordx4 v0, s[8:11], s69 offen offset:2048 sc1 lds\n"
    "v_mfma_f32_16x16x32_bf16 v[32:35], v[72:75], v[208:211], v[32:35]\n"
    "ds_read_b128 v[68:71], v0 offset:57344\n"
    "v_add_f32_dpp v114, v106, v106 row_ror:8 row_mask:0xf bank_mask:12\n"
    "v_mfma_f32_16x16x32_bf16 v[40:43], v[72:75], v[212:215], v[40:43]\n"
    "buffer_load_dwordx4 v0, s[8:11], s69 offen offset:3072 sc1 lds\n"
    "v_add_f32_dpp v115, v107, v107 row_ror:8 row_mask:0xf bank_mask:12\n"
    "v_mfma_f32_16x16x32_bf16 v[44:47], v[76:79], v[212:215], v[44:47]\n"
    "ds_read_b128 v[72:75], v0 offset:58368\n"
    "s_and_b32 s74, s60, 4\n"
    "v_mfma_f32_16x16x32_bf16 v[36:39], v[76:79], v[208:211], v[36:39]\n"
    "s_lshl_b32 s74, s74, 8\n"
    "v_and_b32 v7, 0x200, v0\n"
    "v_mfma_f32_16x16x32_bf16 v[48:51], v[80:83], v[208:211], v[48:51]\n"
    "ds_read_b128 v[76:79], v1 offset:24576\n"
    "v_add_u32 v7, v0, v7\n"
    "v_permlane32_swap_b32 v112, v113\n"
    "v_mfma_f32_16x16x32_bf16 v[56:59], v[80:83], v[212:215], v[56:59]\n"
    "v_xor_b32 v7, s74, v7\n"
    "v_cndmask_b32 v7, v7, 2.0, s[54:55]\n"
    "s_add_u32 s74, s86, 28839936\n"
    "s_waitcnt vmcnt(12) lgkmcnt(3)\n"
    "v_mfma_f32_16x16x32_bf16 v[24:27], v[84:87], v[220:223], v[24:27]\n"
    "ds_read_b128 v[80:83], v1 offset:25600\n"
    "buffer_store_dwordx4 a[236:239], v7, s[8:11], s74 offen sc1\n"
    "v_mfma_f32_16x16x32_bf16 v[16:19], v[84:87], v[216:219], v[16:19]\n"
    "s_add_u32 m0, s66, 102400\n"
    "s_add_u32 s69, s77, 69632\n"
    "v_permlane32_swap_b32 v114, v115\n"
    "v_mfma_f32_16x16x32_bf16 v[20:23], v[88:91], v[216:219], v[20:23]\n"
    "ds_read_b128 v[84:87], v0 offset:24576\n"
    "v_add_f32 v112, v112, v113\n"
    "v_mfma_f32_16x16x32_bf16 v[28:31], v[88:91], v[220:223], v[28:31]\n"
    "buffer_load_dwordx4 v0, s[8:11], s69 offen offset:0 sc1 lds\n"
    "v_mfma_f32_16x16x32_bf16 v[40:43], v[92:95], v[220:223], v[40:43]\n"
    "ds_read_b128 v[88:91], v0 offset:59392\n"
    "v_add_f32 v114, v114, v115\n"
    "v_mfma_f32_16x16x32_bf16 v[32:35], v[92:95], v[216:219], v[32:35]\n"
    "buffer_load_dwordx4 v0, s[8:11], s69 offen offset:1024 sc1 lds\n"
    "v_mfma_f32_16x16x32_bf16 v[36:39], v[96:99], v[216:219], v[36:39]\n"
    "ds_read_b128 v[92:95], v0 offset:60416\n"
    "v_mfma_f32_16x16x32_bf16 v[44:47], v[96:99], v[220:223], v[44:47]\n"
    "buffer_load_dwordx4 v0, s[8:11], s69 offen offset:2048 sc1 lds\n"
    "v_permlane16_swap_b32 v112, v114\n"
    "v_mfma_f32_16x16x32_bf16 v[56:59], v[64:67], v[220:223], v[56:59]\n"
    "ds_read_b128 v[96:99], v1 offset:26624\n"
    "v_add_f32 v112, v112, v114\n"
    "v_mfma_f32_16x16x32_bf16 v[48:51], v[64:67], v[216:219], v[48:51]\n"
    "v_mul_f32 v108, 0xbb000000, v112\n"
    "s_waitcnt vmcnt(14) lgkmcnt(3)\n"
    "v_mfma_f32_16x16x32_bf16 v[16:19], v[68:71], v[224:227], v[16:19]\n"
    "ds_read_b128 v[64:67], v1 offset:27648\n"
    "v_mul_f32 v116, 0xbb000000, v112\n"
    "v_mfma_f32_16x16x32_bf16 v[24:27], v[68:71], v[228:231], v[24:27]\n"
    "buffer_load_dwordx4 v0, s[8:11], s69 offen offset:3072 sc1 lds\n"
    "v_mfma_f32_16x16x32_bf16 v[28:31], v[72:75], v[228:231], v[28:31]\n"
    "ds_read_b128 v[68:71], v0 offset:25600\n"
    "v_and_b32 v7, 0x200, v0\n"
    "v_mfma_f32_16x16x32_bf16 v[20:23], v[72:75], v[224:227], v[20:23]\n"
    "v_add_u32 v7, v0, v7\n"
    "v_xor_b32 v7, s92, v7\n"
    "s_add_u32 s74, s85, 28839936\n"
    "v_permlane32_swap_b32 v108, v116\n"
    "v_mfma_f32_16x16x32_bf16 v[32:35], v[76:79], v[224:227], v[32:35]\n"
    "ds_read_b128 v[72:75], v0 offset:61440\n"
    "buffer_store_dwordx4 a[236:239], v7, s[8:11], s74 offen sc1\n"
    "v_mfma_f32_16x16x32_bf16 v[40:43], v[76:79], v[228:231], v[40:43]\n"
    "s_add_u32 m0, s66, 131072\n"
    "s_add_u32 s69, s77, 98304\n"
    "v_mfma_f32_16x16x32_bf16 v[44:47], v[80:83], v[228:231], v[44:47]\n"
    "ds_read_b128 v[76:79], v0 offset:62464\n"
    "v_fma_f32 v109, -v108, v108, -v116\n"
    "v_mfma_f32_16x16x32_bf16 v[36:39], v[80:83], v[224:227], v[36:39]\n"
    "buffer_load_dwordx4 v0, s[8:11], s69 offen offset:0 sc1 lds\n"
    "v_mfma_f32_16x16x32_bf16 v[48:51], v[84:87], v[224:227], v[48:51]\n"
    "ds_read_b128 v[80:83], v1 offset:28672\n"
    "v_cvt_pk_bf16_f32 v124, v108, v108\n"
    "v_mfma_f32_16x16x32_bf16 v[56:59], v[84:87], v[228:231], v[56:59]\n"
    "buffer_load_dwordx4 v0, s[8:11], s69 offen offset:1024 sc1 lds\n"
    "v_max_f32 v116, 0, v109\n"
    "s_waitcnt vmcnt(16) lgkmcnt(3)\n"
    "v_mfma_f32_16x16x32_bf16 v[24:27], v[88:91], v[236:239], v[24:27]\n"
    "ds_read_b128 v[84:87], v1 offset:29696\n"
    "s_add_u32 s74, s85, 28844032\n"
    "v_mfma_f32_16x16x32_bf16 v[16:19], v[88:91], v[232:235], v[16:19]\n"
    "buffer_store_dwordx4 a[236:239], v7, s[8:11], s74 offen sc1\n"
    "v_mfma_f32_16x16x32_bf16 v[20:23], v[92:95], v[232:235], v[20:23]\n"
    "ds_read_b128 v[88:91], v0 offset:26624\n"
    "s_add_u32 m0, s66, 131072\n"
    "v_lshlrev_b32 v112, 16, v124\n"
    "v_mfma_f32_16x16x32_bf16 v[28:31], v[92:95], v[236:239], v[28:31]\n"
    "s_add_u32 s69, s77, 98304\n"
    "v_add_f32 v116, 0x3727c5ac, v116\n"
    "v_mfma_f32_16x16x32_bf16 v[40:43], v[96:99], v[236:239], v[40:43]\n"
    "ds_read_b128 v[92:95], v0 offset:63488\n"
    "v_sub_f32 v112, v108, v112\n"
    "v_mfma_f32_16x16x32_bf16 v[32:35], v[96:99], v[232:235], v[32:35]\n"
    "buffer_load_dwordx4 v0, s[8:11], s69 offen offset:2048 sc1 lds\n"
    "v_mfma_f32_16x16x32_bf16 v[36:39], v[64:67], v[232:235], v[36:39]\n"
    "ds_read_b128 v[96:99], v0 offset:64512\n"
    "v_fmac_f32 v116, 0, v109\n"
    "v_mfma_f32_16x16x32_bf16 v[44:47], v[64:67], v[236:239], v[44:47]\n"
    "buffer_load_dwordx4 v0, s[8:11], s69 offen offset:3072 sc1 lds\n"
    "v_mfma_f32_16x16x32_bf16 v[56:59], v[68:71], v[236:239], v[56:59]\n"
    "ds_read_b128 v[64:67], v1 offset:30720\n"
    "s_add_u32 s74, s85, 28848128\n"
    "v_cvt_pk_bf16_f32 v125, v108, v112\n"
    "v_mfma_f32_16x16x32_bf16 v[48:51], v[68:71], v[232:235], v[48:51]\n"
    "v_mov_b32 v126, v124\n"
    "s_waitcnt vmcnt(17) lgkmcnt(3)\n"
    "v_mfma_f32_16x16x32_bf16 v[16:19], v[72:75], v[240:243], v[16:19]\n"
    "ds_read_b128 v[68:71], v1 offset:31744\n"
    "buffer_store_dwordx4 a[236:239], v7, s[8:11], s74 offen sc1\n"
    "v_mfma_f32_16x16x32_bf16 v[24:27], v[72:75], v[244:247], v[24:27]\n"
    "s_add_u32 m0, s66, 135168\n"
    "s_add_u32 s69, s77, 102400\n"
    "v_mfma_f32_16x16x32_bf16 v[28:31], v[76:79], v[244:247], v[28:31]\n"
    "ds_read_b128 v[72:75], v0 offset:27648\n"
    "v_rsq_f32 v118, v116\n"
    "v_mfma_f32_16x16x32_bf16 v[20:23], v[76:79], v[240:243], v[20:23]\n"
    "buffer_load_dwordx4 v0, s[8:11], s69 offen offset:0 sc1 lds\n"
    "v_mfma_f32_16x16x32_bf16 v[32:35], v[80:83], v[240:243], v[32:35]\n"
    "buffer_load_dwordx4 v0, s[8:11], s69 offen offset:1024 sc1 lds\n"
    "v_mfma_f32_16x16x32_bf16 v[40:43], v[80:83], v[244:247], v[40:43]\n"
    "v_mov_b32 v127, v125\n"
    "v_mov_b32 v117, v116\n"
    "v_mfma_f32_16x16x32_bf16 v[44:47], v[84:87], v[244:247], v[44:47]\n"
    "v_permlane16_swap_b32 v124, v126\n"
    "v_mfma_f32_16x16x32_bf16 v[36:39], v[84:87], v[240:243], v[36:39]\n"
    "buffer_load_dwordx4 v0, s[8:11], s69 offen offset:2048 sc1 lds\n"
    "v_mfma_f32_16x16x32_bf16 v[48:51], v[88:91], v[240:243], v[48:51]\n"
    "v_permlane16_swap_b32 v125, v127\n"
    "v_mul_f32 v120, 0xc0135761, v118\n"
    "v_mfma_f32_16x16x32_bf16 v[56:59], v[88:91], v[244:247], v[56:59]\n"
    "buffer_load_dwordx4 v0, s[8:11], s69 offen offset:3072 sc1 lds\n"
    "v_mfma_f32_16x16x16_bf16 v[16:19], a[188:189], v[124:125], v[16:19]\n"
    "v_mfma_f32_16x16x16_bf16 v[24:27], a[188:189], v[126:127], v[24:27]\n"
    "v_mfma_f32_16x16x16_bf16 v[20:23], a[190:191], v[124:125], v[20:23]\n"
    "v_mfma_f32_16x16x16_bf16 v[28:31], a[190:191], v[126:127], v[28:31]\n"
    "v_mfma_f32_16x16x16_bf16 v[32:35], a[192:193], v[124:125], v[32:35]\n"
    "v_mfma_f32_16x16x16_bf16 v[40:43], a[192:193], v[126:127], v[40:43]\n"
    "v_mfma_f32_16x16x16_bf16 v[36:39], a[194:195], v[124:125], v[36:39]\n"
    "v_mfma_f32_16x16x16_bf16 v[44:47], a[194:195], v[126:127], v[44:47]\n"
    "v_mfma_f32_16x16x16_bf16 v[48:51], a[208:209], v[124:125], v[48:51]\n"
    "v_mfma_f32_16x16x16_bf16 v[56:59], a[208:209], v[126:127], v[56:59]\n"
    "s_waitcnt vmcnt(20) lgkmcnt(0)\n"
    "v_mfma_f32_16x16x32_bf16 v[24:27], v[92:95], v[252:255], v[24:27]\n"
    "s_add_u32 s74, s85, 28852224\n"
    "v_mul_f32 v109, v118, v118\n"
    "v_mfma_f32_16x16x32_bf16 v[16:19], v[92:95], v[248:251], v[16:19]\n"
    "v_permlane16_swap_b32 v116, v117\n"
    "v_mfma_f32_16x16x32_bf16 v[20:23], v[96:99], v[248:251], v[20:23]\n"
    "buffer_store_dwordx4 a[236:239], v7, s[8:11], s74 offen sc1\n"
    "v_mfma_f32_16x16x32_bf16 v[28:31], v[96:99], v[252:255], v[28:31]\n"
    "s_add_u32 s74, s85, 28856320\n"
    "v_mul_f32 v109, v109, v120\n"
    "v_mov_b32 v121, v120\n"
    "v_mfma_f32_16x16x32_bf16 v[40:43], v[64:67], v[252:255], v[40:43]\n"
    "v_mul_f32 v122, 0x3d372713, v109\n"
    "v_mul_f32 v123, 0x3d372713, v109\n"
    "v_mfma_f32_16x16x32_bf16 v[32:35], v[64:67], v[248:251], v[32:35]\n"
    "buffer_store_dwordx4 a[236:239], v7, s[8:11], s74 offen sc1\n"
    "v_mfma_f32_16x16x32_bf16 v[36:39], v[68:71], v[248:251], v[36:39]\n"
    "v_permlane16_swap_b32 v120, v121\n"
    "v_mfma_f32_16x16x32_bf16 v[44:47], v[68:71], v[252:255], v[44:47]\n"
    "v_permlane16_swap_b32 v122, v123\n"
    "v_mfma_f32_16x16x32_bf16 v[56:59], v[72:75], v[252:255], v[56:59]\n"
    "v_mfma_f32_16x16x32_bf16 v[48:51], v[72:75], v[248:251], v[48:51]\n"
    "v_cmp_u_f32 s[30:31], v16, v24\n"
    "v_cmp_u_f32 s[24:25], v116, v117\n"
    "v_mfma_f32_16x16x16_bf16 v[64:67], a[196:197], v[124:125], 0\n"
    "v_mfma_f32_16x16x16_bf16 v[72:75], a[196:197], v[126:127], 0\n"
    "v_mfma_f32_16x16x16_bf16 v[68:71], a[198:199], v[124:125], 0\n"
    "v_mfma_f32_16x16x16_bf16 v[76:79], a[198:199], v[126:127], 0\n"
    "s_or_b64 s[30:31], s[30:31], s[24:25]\n"
    "s_cbranch_scc1 Lbad59\n"
    "Lok59:\n"
    "s_waitcnt vmcnt(13)\n"
    "s_barrier\n"
    "ds_read_b128 v[80:83], v1 offset:32768\n"
    "ds_read_b128 v[84:87], v1 offset:34816\n"
    "ds_read_b128 v[88:91], v1 offset:36864\n"
    "ds_read_b128 v[92:95], v1 offset:38912\n"
    "ds_read_b128 v[96:99], v1 offset:40960\n"
    "ds_read_b128 v[112:115], v1 offset:43008\n"
    "v_permlane32_swap_b32 v48, v56\n"
    "v_permlane32_swap_b32 v49, v57\n"
    "v_permlane32_swap_b32 v50, v58\n"
    "v_permlane32_swap_b32 v51, v59\n"
    "s_mov_b32 s30, 0\n"
    "s_mov_b32 s31, -1\n"
    "v_cndmask_b32 v52, v120, v121, s[30:31]\n"
    "v_cndmask_b32 v53, v122, v123, s[30:31]\n"
    "v_cndmask_b32 v54, v116, v117, s[30:31]\n"
    "v_mul_f32 v100, v48, v48\n"
    "v_mul_f32 v101, v49, v49\n"
    "v_mul_f32 v102, v50, v50\n"
    "v_mul_f32 v103, v51, v51\n"
    "v_fma_f32 v100, v100, v53, v52\n"
    "v_fma_f32 v101, v101, v53, v52\n"
    "v_fma_f32 v102, v102, v53, v52\n"
    "s_waitcnt lgkmcnt(3)\n"
    "v_mfma_f32_16x16x32_bf16 v[64:67], v[80:83], v[128:131], v[64:67]\n"
    "ds_read_b128 v[10:13], v1 offset:45056\n"
    "s_add_u32 m0, s66, 32768\n"
    "v_mfma_f32_16x16x32_bf16 v[72:75], v[80:83], v[132:135], v[72:75]\n"
    "s_add_u32 s69, s77, 131072\n"
    "v_fma_f32 v103, v103, v53, v52\n"
    "v_mul_f32 v56, v48, v56\n"
    "v_mfma_f32_16x16x32_bf16 v[72:75], v[84:87], v[140:143], v[72:75]\n"
    "ds_read_b128 v[80:83], v1 offset:47104\n"
    "v_mul_f32 v48, v100, v48\n"
    "v_mul_f32 v57, v49, v57\n"
    "v_mfma_f32_16x16x32_bf16 v[64:67], v[84:87], v[136:139], v[64:67]\n"
    "buffer_load_dwordx4 v0, s[8:11], s69 offen offset:0 sc1 lds\n"
    "v_mul_f32 v49, v101, v49\n"
    "v_mfma_f32_16x16x32_bf16 v[64:67], v[88:91], v[144:147], v[64:67]\n"
    "ds_read_b128 v[84:87], v1 offset:49152\n"
    "v_mul_f32 v58, v50, v58\n"
    "v_mul_f32 v50, v102, v50\n"
    "v_mfma_f32_16x16x32_bf16 v[72:75], v[88:91], v[148:151], v[72:75]\n"
    "v_mul_f32 v59, v51, v59\n"
    "v_mul_f32 v51, v103, v51\n"
    "s_waitcnt lgkmcnt(3)\n"
    "v_mfma_f32_16x16x32_bf16 v[72:75], v[92:95], v[156:159], v[72:75]\n"
    "ds_read_b128 v[88:91], v1 offset:51200\n"
    "v_exp_f32 v48, v48\n"
    "v_mfma_f32_16x16x32_bf16 v[64:67], v[92:95], v[152:155], v[64:67]\n"
    "v_exp_f32 v49, v49\n"
    "v_mfma_f32_16x16x32_bf16 v[64:67], v[96:99], v[160:163], v[64:67]\n"
    "ds_read_b128 v[92:95], v1 offset:53248\n"
    "v_exp_f32 v50, v50\n"
    "v_mfma_f32_16x16x32_bf16 v[72:75], v[96:99], v[164:167], v[72:75]\n"
    "v_exp_f32 v51, v51\n"
    "v_fma_f32 v48, v48, v54, v54\n"
    "v_fma_f32 v49, v49, v54, v54\n"
    "v_mfma_f32_16x16x32_bf16 v[72:75], v[112:115], v[172:175], v[72:75]\n"
    "ds_read_b128 v[96:99], v1 offset:55296\n"
    "v_fma_f32 v50, v50, v54, v54\n"
    "v_fma_f32 v51, v51, v54, v54\n"
    "v_mfma_f32_16x16x32_bf16 v[64:67], v[112:115], v[168:171], v[64:67]\n"
    "v_rcp_f32 v48, v48\n"
    "s_waitcnt lgkmcnt(3)\n"
    "v_mfma_f32_16x16x32_bf16 v[64:67], v[10:13], v[176:179], v[64:67]\n"
    "ds_read_b128 v[112:115], v1 offset:57344\n"
    "buffer_load_dwordx4 v0, s[8:11], s69 offen offset:1024 sc1 lds\n"
    "v_mfma_f32_16x16x32_bf16 v[72:75], v[10:13], v[180:183], v[72:75]\n"
    "v_rcp_f32 v49, v49\n"
    "v_rcp_f32 v50, v50\n"
    "v_mfma_f32_16x16x32_bf16 v[72:75], v[80:83], v[188:191], v[72:75]\n"
    "ds_read_b128 v[10:13], v1 offset:59392\n"
    "v_rcp_f32 v51, v51\n"
    "v_mfma_f32_16x16x32_bf16 v[64:67], v[80:83], v[184:187], v[64:67]\n"
    "v_mul_f32 v56, v56, v48\n"
    "v_mul_f32 v57, v57, v49\n"
    "v_mul_f32 v58, v58, v50\n"
    "v_mfma_f32_16x16x32_bf16 v[64:67], v[84:87], v[192:195], v[64:67]\n"
    "ds_read_b128 v[80:83], v1 offset:61440\n"
    "v_mul_f32 v59, v59, v51\n"
    "v_mfma_f32_16x16x32_bf16 v[72:75], v[84:87], v[196:199], v[72:75]\n"
    "v_cvt_pk_bf16_f32 v108, v56, v57\n"
    "v_cvt_pk_bf16_f32 v109, v58, v59\n"
    "s_waitcnt lgkmcnt(3)\n"
    "v_mfma_f32_16x16x32_bf16 v[72:75], v[88:91], v[204:207], v[72:75]\n"
    "ds_read_b128 v[84:87], v1 offset:63488\n"
    "v_cvt_pk_bf16_f32 v110, v56, v57\n"
    "v_cvt_pk_bf16_f32 v111, v58, v59\n"
    "v_mfma_f32_16x16x32_bf16 v[64:67], v[88:91], v[200:203], v[64:67]\n"
    "buffer_load_dwordx4 v0, s[8:11], s69 offen offset:2048 sc1 lds\n"
    "s_add_u32 s73, s86, 28839936\n"
    "v_mfma_f32_16x16x32_bf16 v[64:67], v[92:95], v[208:211], v[64:67]\n"
    "ds_read_b128 v[88:91], v1 offset:33792\n"
    "v_permlane16_swap_b32 v108, v110\n"
    "v_mfma_f32_16x16x32_bf16 v[72:75], v[92:95], v[212:215], v[72:75]\n"
    "v_permlane16_swap_b32 v109, v111\n"
    "v_mfma_f32_16x16x32_bf16 v[72:75], v[96:99], v[220:223], v[72:75]\n"
    "ds_read_b128 v[92:95], v1 offset:35840\n"
    "buffer_store_dwordx4 v[108:111], v14, s[8:11], s73 offen sc1\n"
    "v_mfma_f32_16x16x32_bf16 v[64:67], v[96:99], v[216:219], v[64:67]\n"
    "v_mul_f32 v100, v16, v16\n"
    "v_mul_f32 v101, v17, v17\n"
    "s_waitcnt lgkmcnt(3)\n"
    "v_mfma_f32_16x16x32_bf16 v[64:67], v[112:115], v[224:227], v[64:67]\n"
    "ds_read_b128 v[96:99], v1 offset:37888\n"
    "v_mul_f32 v102, v18, v18\n"
    "v_mul_f32 v103, v19, v19\n"
    "v_mfma_f32_16x16x32_bf16 v[72:75], v[112:115], v[228:231], v[72:75]\n"
    "v_fma_f32 v100, v100, v122, v120\n"
    "v_fma_f32 v101, v101, v122, v120\n"
    "v_fma_f32 v102, v102, v122, v120\n"
    "v_mfma_f32_16x16x32_bf16 v[72:75], v[10:13], v[236:239], v[72:75]\n"
    "ds_read_b128 v[112:115], v1 offset:39936\n"
    "v_fma_f32 v103, v103, v122, v120\n"
    "v_mul_f32 v20, v16, v20\n"
    "v_mfma_f32_16x16x32_bf16 v[64:67], v[10:13], v[232:235], v[64:67]\n"
    "buffer_load_dwordx4 v0, s[8:11], s69 offen offset:3072 sc1 lds\n"
    "v_mul_f32 v16, v100, v16\n"
    "v_mfma_f32_16x16x32_bf16 v[64:67], v[80:83], v[240:243], v[64:67]\n"
    "ds_read_b128 v[10:13], v1 offset:41984\n"
    "v_mul_f32 v21, v17, v21\n"
    "v_mul_f32 v17, v101, v17\n"
    "v_mfma_f32_16x16x32_bf16 v[72:75], v[80:83], v[244:247], v[72:75]\n"
    "v_mul_f32 v22, v18, v22\n"
    "v_mul_f32 v18, v102, v18\n"
    "s_waitcnt lgkmcnt(3)\n"
    "v_mfma_f32_16x16x32_bf16 v[72:75], v[84:87], v[252:255], v[72:75]\n"
    "ds_read_b128 v[80:83], v1 offset:44032\n"
    "v_mul_f32 v23, v19, v23\n"
    "v_mul_f32 v19, v103, v19\n"
    "v_mfma_f32_16x16x32_bf16 v[64:67], v[84:87], v[248:251], v[64:67]\n"
    "s_add_u32 m0, s66, 36864\n"
    "s_add_u32 s69, s77, 135168\n"
    "v_mfma_f32_16x16x32_bf16 v[68:71], v[88:91], v[128:131], v[68:71]\n"
    "ds_read_b128 v[84:87], v1 offset:46080\n"
    "v_exp_f32 v16, v16\n"
    "v_mfma_f32_16x16x32_bf16 v[76:79], v[88:91], v[132:135], v[76:79]\n"
    "v_exp_f32 v17, v17\n"
    "v_mfma_f32_16x16x32_bf16 v[76:79], v[92:95], v[140:143], v[76:79]\n"
    "ds_read_b128 v[88:91], v1 offset:48128\n"
    "v_exp_f32 v18, v18\n"
    "v_mfma_f32_16x16x32_bf16 v[68:71], v[92:95], v[136:139], v[68:71]\n"
    "v_exp_f32 v19, v19\n"
    "v_fma_f32 v16, v16, v116, v116\n"
    "s_waitcnt lgkmcnt(3)\n"
    "v_mfma_f32_16x16x32_bf16 v[68:71], v[96:99], v[144:147], v[68:71]\n"
    "ds_read_b128 v[92:95], v1 offset:50176\n"
    "buffer_load_dwordx4 v0, s[8:11], s69 offen offset:0 sc1 lds\n"
    "v_fma_f32 v17, v17, v116, v116\n"
    "v_mfma_f32_16x16x32_bf16 v[76:79], v[96:99], v[148:151], v[76:79]\n"
    "v_fma_f32 v18, v18, v116, v116\n"
    "v_fma_f32 v19, v19, v116, v116\n"
    "v_mfma_f32_16x16x32_bf16 v[76:79], v[112:115], v[156:159], v[76:79]\n"
    "ds_read_b128 v[96:99], v1 offset:52224\n"
    "Lpad10:\n"
    "s_cbranch_execz Lwarm0\n"
    "v_rcp_f32 v16, v16\n"
    "v_mfma_f32_16x16x32_bf16 v[68:71], v[112:115], v[152:155], v[68:71]\n"
    "v_rcp_f32 v17, v17\n"
    "v_rcp_f32 v18, v18\n"
    "v_mfma_f32_16x16x32_bf16 v[68:71], v[10:13], v[160:163], v[68:71]\n"
    "ds_read_b128 v[112:115], v1 offset:54272\n"
    "v_rcp_f32 v19, v19\n"
    "v_mfma_f32_16x16x32_bf16 v[76:79], v[10:13], v[164:167], v[76:79]\n"
    "v_mul_f32 v20, v20, v16\n"
    "v_mul_f32 v21, v21, v17\n"
    "s_waitcnt lgkmcnt(3)\n"
    "v_mfma_f32_16x16x32_bf16 v[76:79], v[80:83], v[172:175], v[76:79]\n"
    "ds_read_b128 v[10:13], v1 offset:56320\n"
    "v_mul_f32 v22, v22, v18\n"
    "v_mul_f32 v23, v23, v19\n"
    "v_mfma_f32_16x16x32_bf16 v[68:71], v[80:83], v[168:171], v[68:71]\n"
    "buffer_load_dwordx4 v0, s[8:11], s69 offen offset:1024 sc1 lds\n"
    "v_cvt_pk_bf16_f32 v104, v20, v21\n"
    "v_mfma_f32_16x16x32_bf16 v[68:71], v[84:87], v[176:179], v[68:71]\n"
    "ds_read_b128 v[80:83], v1 offset:58368\n"
    "v_cvt_pk_bf16_f32 v105, v22, v23\n"
    "v_mul_f32 v100, v24, v24\n"
    "v_mfma_f32_16x16x32_bf16 v[76:79], v[84:87], v[180:183], v[76:79]\n"
    "v_mul_f32 v101, v25, v25\n"
    "v_mul_f32 v102, v26, v26\n"
    "v_mul_f32 v103, v27, v27\n"
    "v_mfma_f32_16x16x32_bf16 v[76:79], v[88:91], v[188:191], v[76:79]\n"
    "ds_read_b128 v[84:87], v1 offset:60416\n"
    "v_fma_f32 v100, v100, v123, v121\n"
    "v_fma_f32 v101, v101, v123, v121\n"
    "v_mfma_f32_16x16x32_bf16 v[68:71], v[88:91], v[184:187], v[68:71]\n"
    "v_fma_f32 v102, v102, v123, v121\n"
    "s_waitcnt vmcnt(7)\n"
    "s_barrier\n"
    "s_waitcnt lgkmcnt(3)\n"
    "v_mfma_f32_16x16x32_bf16 v[68:71], v[92:95], v[192:195], v[68:71]\n"
    "ds_read_b128 v[88:91], v1 offset:62464\n"
    "v_fma_f32 v103, v103, v123, v121\n"
    "v_mfma_f32_16x16x32_bf16 v[76:79], v[92:95], v[196:199], v[76:79]\n"
    "v_mul_f32 v28, v24, v28\n"
    "v_mul_f32 v24, v100, v24\n"
    "v_mul_f32 v29, v25, v29\n"
    "v_mfma_f32_16x16x32_bf16 v[76:79], v[96:99], v[204:207], v[76:79]\n"
    "ds_read_b128 v[92:95], v1 offset:64512\n"
    "v_mul_f32 v25, v101, v25\n"
    "v_mul_f32 v30, v26, v30\n"
    "v_mfma_f32_16x16x32_bf16 v[68:71], v[96:99], v[200:203], v[68:71]\n"
    "buffer_load_dwordx4 v0, s[8:11], s69 offen offset:2048 sc1 lds\n"
    "v_mul_f32 v26, v102, v26\n"
    "v_mfma_f32_16x16x32_bf16 v[68:71], v[112:115], v[208:211], v[68:71]\n"
    "ds_read_b128 v[96:99], v2 offset:0\n"
    "v_mul_f32 v31, v27, v31\n"
    "v_mul_f32 v27, v103, v27\n"
    "v_mfma_f32_16x16x32_bf16 v[76:79], v[112:115], v[212:215], v[76:79]\n"
    "v_exp_f32 v24, v24\n"
    "s_waitcnt lgkmcnt(3)\n"
    "v_mfma_f32_16x16x32_bf16 v[76:79], v[10:13], v[220:223], v[76:79]\n"
    "ds_read_b128 v[112:115], v2 offset:2048\n"
    "v_exp_f32 v25, v25\n"
    "v_mfma_f32_16x16x32_bf16 v[68:71], v[10:13], v[216:219], v[68:71]\n"
    "v_exp_f32 v26, v26\n"
    "v_mfma_f32_16x16x32_bf16 v[68:71], v[80:83], v[224:227], v[68:71]\n"
    "ds_read_b128 v[10:13], v2 offset:4096\n"
    "v_exp_f32 v27, v27\n"
    "v_fma_f32 v24, v24, v117, v117\n"
    "v_mfma_f32_16x16x32_bf16 v[76:79], v[80:83], v[228:231], v[76:79]\n"
    "v_fma_f32 v25, v25, v117, v117\n"
    "v_fma_f32 v26, v26, v117, v117\n"
    "v_fma_f32 v27, v27, v117, v117\n"
    "v_mfma_f32_16x16x32_bf16 v[76:79], v[84:87], v[236:239], v[76:79]\n"
    "ds_read_b128 v[80:83], v2 offset:6144\n"
    "v_rcp_f32 v24, v24\n"
    "v_mfma_f32_16x16x32_bf16 v[68:71], v[84:87], v[232:235], v[68:71]\n"
    "v_rcp_f32 v25, v25\n"
    "s_waitcnt lgkmcnt(3)\n"
    "v_mfma_f32_16x16x32_bf16 v[68:71], v[88:91], v[240:243], v[68:71]\n"
    "ds_read_b128 v[84:87], v2 offset:8192\n"
    "buffer_load_dwordx4 v0, s[8:11], s69 offen offset:3072 sc1 lds\n"
    "v_mfma_f32_16x16x32_bf16 v[76:79], v[88:91], v[244:247], v[76:79]\n"
    "v_rcp_f32 v26, v26\n"
    "v_rcp_f32 v27, v27\n"
    "v_mfma_f32_16x16x32_bf16 v[76:79], v[92:95], v[252:255], v[76:79]\n"
    "ds_read_b128 v[88:91], v2 offset:10240\n"
    "v_mul_f32 v28, v28, v24\n"
    "v_mul_f32 v29, v29, v25\n"
    "v_mfma_f32_16x16x32_bf16 v[68:71], v[92:95], v[248:251], v[68:71]\n"
    "v_mul_f32 v30, v30, v26\n"
    "v_mul_f32 v31, v31, v27\n"
    "v_cvt_pk_bf16_f32 v106, v28, v29\n"
    "v_mfma_f32_16x16x16_bf16 v[48:51], a[200:201], v[124:125], 0\n"
    "v_cvt_pk_bf16_f32 v107, v30, v31\n"
    "s_add_u32 s73, s85, 28839936\n"
    "v_mfma_f32_16x16x16_bf16 v[56:59], a[200:201], v[126:127], 0\n"
    "v_permlane32_swap_b32 v104, v106\n"
    "v_mfma_f32_16x16x16_bf16 v[52:55], a[202:203], v[124:125], 0\n"
    "v_permlane32_swap_b32 v105, v107\n"
    "s_mov_b32 s65, 397312\n"
    "v_mfma_f32_16x16x16_bf16 v[60:63], a[202:203], v[126:127], 0\n"
    "buffer_atomic_add a255, v3, s[8:11], s65 offen\n"
    "v_mfma_f32_16x16x32_bf16 v[48:51], v[96:99], v[128:131], v[48:51]\n"
    "ds_read_b128 v[92:95], v2 offset:12288\n"
    "buffer_store_dwordx4 v[104:107], v15, s[8:11], s73 offen sc1\n"
    "v_mfma_f32_16x16x32_bf16 v[56:59], v[96:99], v[132:135], v[56:59]\n"
    "v_mul_f32 v100, v32, v32\n"
    "v_mul_f32 v101, v33, v33\n"
    "s_waitcnt lgkmcnt(3)\n"
    "v_mfma_f32_16x16x32_bf16 v[56:59], v[112:115], v[140:143], v[56:59]\n"
    "ds_read_b128 v[96:99], v2 offset:14336\n"
    "v_mul_f32 v102, v34, v34\n"
    "v_mul_f32 v103, v35, v35\n"
    "v_mfma_f32_16x16x32_bf16 v[48:51], v[112:115], v[136:139], v[48:51]\n"
    "v_fma_f32 v100, v100, v122, v120\n"
    "v_fma_f32 v101, v101, v122, v120\n"
    "v_fma_f32 v102, v102, v122, v120\n"
    "v_mfma_f32_16x16x32_bf16 v[48:51], v[10:13], v[144:147], v[48:51]\n"
    "ds_read_b128 v[112:115], v2 offset:16384\n"
    "v_fma_f32 v103, v103, v122, v120\n"
    "v_mul_f32 v36, v32, v36\n"
    "v_mfma_f32_16x16x32_bf16 v[56:59], v[10:13], v[148:151], v[56:59]\n"
    "v_mul_f32 v32, v100, v32\n"
    "v_mul_f32 v37, v33, v37\n"
    "v_mul_f32 v33, v101, v33\n"
    "v_mfma_f32_16x16x32_bf16 v[56:59], v[80:83], v[156:159], v[56:59]\n"
    "ds_read_b128 v[10:13], v2 offset:18432\n"
    "v_mul_f32 v38, v34, v38\n"
    "v_mul_f32 v34, v102, v34\n"
    "v_mfma_f32_16x16x32_bf16 v[48:51], v[80:83], v[152:155], v[48:51]\n"
    "v_mul_f32 v39, v35, v39\n"
    "v_mul_f32 v35, v103, v35\n"
    "s_waitcnt lgkmcnt(3)\n"
    "v_mfma_f32_16x16x32_bf16 v[48:51], v[84:87], v[160:163], v[48:51]\n"
    "ds_read_b128 v[80:83], v2 offset:20480\n"
    "v_exp_f32 v32, v32\n"
    "v_mfma_f32_16x16x32_bf16 v[56:59], v[84:87], v[164:167], v[56:59]\n"
    "v_exp_f32 v33, v33\n"
    "v_mfma_f32_16x16x32_bf16 v[56:59], v[88:91], v[172:175], v[56:59]\n"
    "ds_read_b128 v[84:87], v2 offset:22528\n"
    "v_exp_f32 v34, v34\n"
    "v_mfma_f32_16x16x32_bf16 v[48:51], v[88:91], v[168:171], v[48:51]\n"
    "v_exp_f32 v35, v35\n"
    "v_fma_f32 v32, v32, v116, v116\n"
    "v_fma_f32 v33, v33, v116, v116\n"
    "v_mfma_f32_16x16x32_bf16 v[48:51], v[92:95], v[176:179], v[48:51]\n"
    "ds_read_b128 v[88:91], v2 offset:24576\n"
    "v_fma_f32 v34, v34, v116, v116\n"
    "v_mfma_f32_16x16x32_bf16 v[56:59], v[92:95], v[180:183], v[56:59]\n"
    "v_fma_f32 v35, v35, v116, v116\n"
    "s_waitcnt lgkmcnt(3)\n"
    "v_mfma_f32_16x16x32_bf16 v[56:59], v[96:99], v[188:191], v[56:59]\n"
    "ds_read_b128 v[92:95], v2 offset:26624\n"
    "v_rcp_f32 v32, v32\n"
    "v_mfma_f32_16x16x32_bf16 v[48:51], v[96:99], v[184:187], v[48:51]\n"
    "v_rcp_f32 v33, v33\n"
    "v_rcp_f32 v34, v34\n"
    "v_mfma_f32_16x16x32_bf16 v[48:51], v[112:115], v[192:195], v[48:51]\n"
    "ds_read_b128 v[96:99], v2 offset:28672\n"
    "v_rcp_f32 v35, v35\n"
    "v_mfma_f32_16x16x32_bf16 v[56:59], v[112:115], v[196:199], v[56:59]\n"
    "v_mul_f32 v36, v36, v32\n"
    "v_mul_f32 v37, v37, v33\n"
    "v_mul_f32 v38, v38, v34\n"
    "v_mfma_f32_16x16x32_bf16 v[56:59], v[10:13], v[204:207], v[56:59]\n"
    "ds_read_b128 v[112:115], v2 offset:30720\n"
    "v_mul_f32 v39, v39, v35\n"
    "v_cvt_pk_bf16_f32 v108, v36, v37\n"
    "v_mfma_f32_16x16x32_bf16 v[48:51], v[10:13], v[200:203], v[48:51]\n"
    "v_cvt_pk_bf16_f32 v109, v38, v39\n"
    "v_mul_f32 v100, v40, v40\n"
    "s_waitcnt lgkmcnt(3)\n"
    "v_mfma_f32_16x16x32_bf16 v[48:51], v[80:83], v[208:211], v[48:51]\n"
    "ds_read_b128 v[10:13], v2 offset:1024\n"
    "v_mul_f32 v101, v41, v41\n"
    "v_mul_f32 v102, v42, v42\n"
    "v_mfma_f32_16x16x32_bf16 v[56:59], v[80:83], v[212:215], v[56:59]\n"
    "v_mul_f32 v103, v43, v43\n"
    "v_fma_f32 v100, v100, v123, v121\n"
    "v_fma_f32 v101, v101, v123, v121\n"
    "v_mfma_f32_16x16x32_bf16 v[56:59], v[84:87], v[220:223], v[56:59]\n"
    "ds_read_b128 v[80:83], v2 offset:3072\n"
    "v_fma_f32 v102, v102, v123, v121\n"
    "v_fma_f32 v103, v103, v123, v121\n"
    "v_mfma_f32_16x16x32_bf16 v[48:51], v[84:87], v[216:219], v[48:51]\n"
    "v_mul_f32 v44, v40, v44\n"
    "v_mul_f32 v40, v100, v40\n"
    "v_mul_f32 v45, v41, v45\n"
    "v_mfma_f32_16x16x32_bf16 v[48:51], v[88:91], v[224:227], v[48:51]\n"
    "ds_read_b128 v[84:87], v2 offset:5120\n"
    "v_mul_f32 v41, v101, v41\n"
    "v_mul_f32 v46, v42, v46\n"
    "v_mfma_f32_16x16x32_bf16 v[56:59], v[88:91], v[228:231], v[56:59]\n"
    "v_mul_f32 v42, v102, v42\n"
    "v_mul_f32 v47, v43, v47\n"
    "s_waitcnt lgkmcnt(3)\n"
    "v_mfma_f32_16x16x32_bf16 v[56:59], v[92:95], v[236:239], v[56:59]\n"
    "ds_read_b128 v[88:91], v2 offset:7168\n"
    "v_mul_f32 v43, v103, v43\n"
    "v_mfma_f32_16x16x32_bf16 v[48:51], v[92:95], v[232:235], v[48:51]\n"
    "v_exp_f32 v40, v40\n"
    "v_exp_f32 v41, v41\n"
    "v_mfma_f32_16x16x32_bf16 v[48:51], v[96:99], v[240:243], v[48:51]\n"
    "ds_read_b128 v[92:95], v2 offset:9216\n"
    "v_mfma_f32_16x16x32_bf16 v[56:59], v[96:99], v[244:247], v[56:59]\n"
    "v_exp_f32 v42, v42\n"
    "v_mfma_f32_16x16x32_bf16 v[56:59], v[112:115], v[252:255], v[56:59]\n"
    "ds_read_b128 v[96:99], v2 offset:11264\n"
    "v_exp_f32 v43, v43\n"
    "v_fma_f32 v40, v40, v117, v117\n"
    "v_mfma_f32_16x16x32_bf16 v[48:51], v[112:115], v[248:251], v[48:51]\n"
    "v_fma_f32 v41, v41, v117, v117\n"
    "v_fma_f32 v42, v42, v117, v117\n"
    "s_waitcnt lgkmcnt(3)\n"
    "v_mfma_f32_16x16x32_bf16 v[52:55], v[10:13], v[128:131], v[52:55]\n"
    "ds_read_b128 v[112:115], v2 offset:13312\n"
    "v_fma_f32 v43, v43, v117, v117\n"
    "v_mfma_f32_16x16x32_bf16 v[60:63], v[10:13], v[132:135], v[60:63]\n"
    "s_add_u32 s68, s67, 393216\n"
    "v_mov_b32 v8, 0\n"
    "v_rcp_f32 v40, v40\n"
    "v_mfma_f32_16x16x32_bf16 v[60:63], v[80:83], v[140:143], v[60:63]\n"
    "ds_read_b128 v[10:13], v2 offset:15360\n"
    "v_rcp_f32 v41, v41\n"
    "v_mfma_f32_16x16x32_bf16 v[52:55], v[80:83], v[136:139], v[52:55]\n"
    "v_rcp_f32 v42, v42\n"
    "v_mfma_f32_16x16x32_bf16 v[52:55], v[84:87], v[144:147], v[52:55]\n"
    "ds_read_b128 v[80:83], v2 offset:17408\n"
    "v_rcp_f32 v43, v43\n"
    "v_mul_f32 v44, v44, v40\n"
    "v_mfma_f32_16x16x32_bf16 v[60:63], v[84:87], v[148:151], v[60:63]\n"
    "v_mul_f32 v45, v45, v41\n"
    "v_mul_f32 v46, v46, v42\n"
    "s_waitcnt lgkmcnt(3)\n"
    "v_mfma_f32_16x16x32_bf16 v[60:63], v[88:91], v[156:159], v[60:63]\n"
    "ds_read_b128 v[84:87], v2 offset:19456\n"
    "buffer_atomic_add v8, v6, s[8:11], s68 offen sc0\n"
    "v_mul_f32 v47, v47, v43\n"
    "v_mfma_f32_16x16x32_bf16 v[52:55], v[88:91], v[152:155], v[52:55]\n"
    "v_cvt_pk_bf16_f32 v110, v44, v45\n"
    "v_cvt_pk_bf16_f32 v111, v46, v47\n"
    "s_add_u32 s73, s85, 28844032\n"
    "v_mfma_f32_16x16x32_bf16 v[52:55], v[92:95], v[160:163], v[52:55]\n"
    "ds_read_b128 v[88:91], v2 offset:21504\n"
    "v_permlane32_swap_b32 v108, v110\n"
    "v_mfma_f32_16x16x32_bf16 v[60:63], v[92:95], v[164:167], v[60:63]\n"
    "v_permlane32_swap_b32 v109, v111\n"
    "v_mfma_f32_16x16x32_bf16 v[60:63], v[96:99], v[172:175], v[60:63]\n"
    "ds_read_b128 v[92:95], v2 offset:23552\n"
    "buffer_store_dwordx4 v[108:111], v15, s[8:11], s73 offen sc1\n"
    "v_mfma_f32_16x16x32_bf16 v[52:55], v[96:99], v[168:171], v[52:55]\n"
    "v_mul_f32 v100, v64, v64\n"
    "v_mul_f32 v101, v65, v65\n"
    "s_waitcnt lgkmcnt(3)\n"
    "v_mfma_f32_16x16x32_bf16 v[52:55], v[112:115], v[176:179], v[52:55]\n"
    "ds_read_b128 v[96:99], v2 offset:25600\n"
    "v_mul_f32 v102, v66, v66\n"
    "v_mfma_f32_16x16x32_bf16 v[60:63], v[112:115], v[180:183], v[60:63]\n"
    "v_mul_f32 v103, v67, v67\n"
    "v_fma_f32 v100, v100, v122, v120\n"
    "v_fma_f32 v101, v101, v122, v120\n"
    "v_mfma_f32_16x16x32_bf16 v[60:63], v[10:13], v[188:191], v[60:63]\n"
    "ds_read_b128 v[112:115], v2 offset:27648\n"
    "v_fma_f32 v102, v102, v122, v120\n"
    "v_fma_f32 v103, v103, v122, v120\n"
    "v_mfma_f32_16x16x32_bf16 v[52:55], v[10:13], v[184:187], v[52:55]\n"
    "v_mul_f32 v68, v64, v68\n"
    "s_waitcnt vmcnt(4)\n"
    "s_barrier\n"
    "v_mfma_f32_16x16x32_bf16 v[52:55], v[80:83], v[192:195], v[52:55]\n"
    "ds_read_b128 v[10:13], v2 offset:29696\n"
    "v_mul_f32 v64, v100, v64\n"
    "v_mul_f32 v69, v65, v69\n"
    "v_mfma_f32_16x16x32_bf16 v[60:63], v[80:83], v[196:199], v[60:63]\n"
    "v_mul_f32 v65, v101, v65\n"
    "v_mul_f32 v70, v66, v70\n"
    "s_waitcnt lgkmcnt(3)\n"
    "v_mfma_f32_16x16x32_bf16 v[60:63], v[84:87], v[204:207], v[60:63]\n"
    "ds_read_b128 v[80:83], v2 offset:31744\n"
    "v_mul_f32 v66, v102, v66\n"
    "v_mul_f32 v71, v67, v71\n"
    "v_mfma_f32_16x16x32_bf16 v[52:55], v[84:87], v[200:203], v[52:55]\n"
    "v_mul_f32 v67, v103, v67\n"
    "v_exp_f32 v64, v64\n"
    "v_mfma_f32_16x16x32_bf16 v[52:55], v[88:91], v[208:211], v[52:55]\n"
    "ds_read_b128 v[84:87], v0 offset:32768\n"
    "v_exp_f32 v65, v65\n"
    "v_mfma_f32_16x16x32_bf16 v[60:63], v[88:91], v[212:215], v[60:63]\n"
    "v_exp_f32 v66, v66\n"
    "v_mfma_f32_16x16x32_bf16 v[60:63], v[92:95], v[220:223], v[60:63]\n"
    "ds_read_b128 v[88:91], v0 offset:33792\n"
    "v_exp_f32 v67, v67\n"
    "v_fma_f32 v64, v64, v116, v116\n"
    "v_mfma_f32_16x16x32_bf16 v[52:55], v[92:95], v[216:219], v[52:55]\n"
    "v_fma_f32 v65, v65, v116, v116\n"
    "v_fma_f32 v66, v66, v116, v116\n"
    "s_waitcnt lgkmcnt(3)\n"
    "v_mfma_f32_16x16x32_bf16 v[52:55], v[96:99], v[224:227], v[52:55]\n"
    "ds_read_b128 v[92:95], v0 offset:34816\n"
    "v_fma_f32 v67, v67, v116, v116\n"
    "v_mfma_f32_16x16x32_bf16 v[60:63], v[96:99], v[228:231], v[60:63]\n"
    "v_rcp_f32 v64, v64\n"
    "v_rcp_f32 v65, v65\n"
    "v_mfma_f32_16x16x32_bf16 v[60:63], v[112:115], v[236:239], v[60:63]\n"
    "ds_read_b128 v[96:99], v0 offset:35840\n"
    "v_rcp_f32 v66, v66\n"
    "v_mfma_f32_16x16x32_bf16 v[52:55], v[112:115], v[232:235], v[52:55]\n"
    "v_rcp_f32 v67, v67\n"
    "v_mul_f32 v68, v68, v64\n"
    "v_mfma_f32_16x16x32_bf16 v[52:55], v[10:13], v[240:243], v[52:55]\n"
    "ds_read_b128 v[112:115], v0 offset:36864\n"
    "v_mul_f32 v69, v69, v65\n"
    "v_mfma_f32_16x16x32_bf16 v[60:63], v[10:13], v[244:247], v[60:63]\n"
    "v_mul_f32 v70, v70, v66\n"
    "v_mul_f32 v71, v71, v67\n"
    "s_waitcnt lgkmcnt(3)\n"
    "v_mfma_f32_16x16x32_bf16 v[60:63], v[80:83], v[252:255], v[60:63]\n"
    "ds_read_b128 v[10:13], v0 offset:37888\n"
    "v_cvt_pk_bf16_f32 v104, v68, v69\n"
    "v_cvt_pk_bf16_f32 v105, v70, v71\n"
    "v_mfma_f32_16x16x32_bf16 v[52:55], v[80:83], v[248:251], v[52:55]\n"
    "v_mul_f32 v100, v72, v72\n"
    "v_mul_f32 v101, v73, v73\n"
    "v_mul_f32 v102, v74, v74\n"
    "v_mfma_f32_16x16x16_bf16 v[32:35], a[204:205], v[124:125], 0\n"
    "v_mul_f32 v103, v75, v75\n"
    "v_fma_f32 v100, v100, v123, v121\n"
    "v_fma_f32 v101, v101, v123, v121\n"
    "v_mfma_f32_16x16x16_bf16 v[40:43], a[204:205], v[126:127], 0\n"
    "v_fma_f32 v102, v102, v123, v121\n"
    "v_fma_f32 v103, v103, v123, v121\n"
    "v_mul_f32 v76, v72, v76\n"
    "v_mfma_f32_16x16x16_bf16 v[36:39], a[206:207], v[124:125], 0\n"
    "v_mul_f32 v72, v100, v72\n"
    "v_mul_f32 v77, v73, v77\n"
    "v_mul_f32 v73, v101, v73\n"
    "v_mfma_f32_16x16x16_bf16 v[44:47], a[206:207], v[126:127], 0\n"
    "v_mul_f32 v78, v74, v78\n"
    "v_mul_f32 v74, v102, v74\n"
    "v_mul_f32 v79, v75, v79\n"
    "v_mfma_f32_16x16x32_bf16 v[32:35], v[84:87], v[128:131], v[32:35]\n"
    "ds_read_b128 v[80:83], v0 offset:38912\n"
    "v_mul_f32 v75, v103, v75\n"
    "v_mfma_f32_16x16x32_bf16 v[40:43], v[84:87], v[132:135], v[40:43]\n"
    "v_exp_f32 v72, v72\n"
    "v_exp_f32 v73, v73\n"
    "v_mfma_f32_16x16x32_bf16 v[44:47], v[88:91], v[132:135], v[44:47]\n"
    "ds_read_b128 v[84:87], v0 offset:39936\n"
    "v_exp_f32 v74, v74\n"
    "v_mfma_f32_16x16x32_bf16 v[36:39], v[88:91], v[128:131], v[36:39]\n"
    "v_exp_f32 v75, v75\n"
    "s_waitcnt lgkmcnt(3)\n"
    "v_mfma_f32_16x16x32_bf16 v[32:35], v[92:95], v[136:139], v[32:35]\n"
    "ds_read_b128 v[88:91], v0 offset:40960\n"
    "v_fma_f32 v72, v72, v117, v117\n"
    "v_fma_f32 v73, v73, v117, v117\n"
    "v_mfma_f32_16x16x32_bf16 v[40:43], v[92:95], v[140:143], v[40:43]\n"
    "v_fma_f32 v74, v74, v117, v117\n"
    "v_fma_f32 v75, v75, v117, v117\n"
    "v_mfma_f32_16x16x32_bf16 v[44:47], v[96:99], v[140:143], v[44:47]\n"
    "ds_read_b128 v[92:95], v0 offset:41984\n"
    "v_rcp_f32 v72, v72\n"
    "v_mfma_f32_16x16x32_bf16 v[36:39], v[96:99], v[136:139], v[36:39]\n"
    "v_rcp_f32 v73, v73\n"
    "v_rcp_f32 v74, v74\n"
    "v_mfma_f32_16x16x32_bf16 v[32:35], v[112:115], v[144:147], v[32:35]\n"
    "ds_read_b128 v[96:99], v0 offset:43008\n"
    "v_mfma_f32_16x16x32_bf16 v[40:43], v[112:115], v[148:151], v[40:43]\n"
    "v_rcp_f32 v75, v75\n"
    "s_waitcnt lgkmcnt(3)\n"
    "v_mfma_f32_16x16x32_bf16 v[44:47], v[10:13], v[148:151], v[44:47]\n"
    "ds_read_b128 v[112:115], v0 offset:44032\n"
    "v_mul_f32 v76, v76, v72\n"
    "v_mul_f32 v77, v77, v73\n"
    "v_mfma_f32_16x16x32_bf16 v[36:39], v[10:13], v[144:147], v[36:39]\n"
    "s_waitcnt vmcnt(1)\n"
    "v_readfirstlane_b32 s29, v8\n"
    "v_mul_f32 v78, v78, v74\n"
    "v_mul_f32 v79, v79, v75\n"
    "v_mfma_f32_16x16x32_bf16 v[32:35], v[80:83], v[152:155], v[32:35]\n"
    "ds_read_b128 v[10:13], v0 offset:45056\n"
    "s_cmp_lt_u32 s29, 16\n"
    "s_cbranch_scc1 Lslow61\n"
    "Lback61:\n"
    "v_cvt_pk_bf16_f32 v106, v76, v77\n"
    "v_mfma_f32_16x16x32_bf16 v[40:43], v[80:83], v[156:159], v[40:43]\n"
    "s_add_u32 s71, s87, 28860416\n"
    "s_add_u32 s71, s71, s89\n"
    "v_cvt_pk_bf16_f32 v107, v78, v79\n"
    "v_mfma_f32_16x16x32_bf16 v[44:47], v[84:87], v[156:159], v[44:47]\n"
    "ds_read_b128 v[80:83], v0 offset:46080\n"
    "s_add_u32 s73, s85, 28848128\n"
    "v_mfma_f32_16x16x32_bf16 v[36:39], v[84:87], v[152:155], v[36:39]\n"
    "v_permlane32_swap_b32 v104, v106\n"
    "s_waitcnt lgkmcnt(3)\n"
    "v_mfma_f32_16x16x32_bf16 v[32:35], v[88:91], v[160:163], v[32:35]\n"
    "ds_read_b128 v[84:87], v0 offset:47104\n"
    "buffer_load_dwordx4 v[16:19], v0, s[8:11], s71 offen offset:0 sc1\n"
    "v_permlane32_swap_b32 v105, v107\n"
    "v_mfma_f32_16x16x32_bf16 v[40:43], v[88:91], v[164:167], v[40:43]\n"
    "buffer_load_dwordx4 v[20:23], v0, s[8:11], s71 offen offset:1024 sc1\n"
    "v_mfma_f32_16x16x32_bf16 v[44:47], v[92:95], v[164:167], v[44:47]\n"
    "ds_read_b128 v[88:91], v0 offset:48128\n"
    "s_add_u32 s72, s88, 28860416\n"
    "buffer_store_dwordx4 v[104:107], v15, s[8:11], s73 offen sc1\n"
    "v_mfma_f32_16x16x32_bf16 v[36:39], v[92:95], v[160:163], v[36:39]\n"
    "s_add_u32 s72, s72, s89\n"
    "v_mul_f32 v100, v48, v48\n"
    "v_mfma_f32_16x16x32_bf16 v[32:35], v[96:99], v[168:171], v[32:35]\n"
    "ds_read_b128 v[92:95], v0 offset:49152\n"
    "v_mul_f32 v101, v49, v49\n"
    "v_mul_f32 v102, v50, v50\n"
    "v_mfma_f32_16x16x32_bf16 v[40:43], v[96:99], v[172:175], v[40:43]\n"
    "v_mul_f32 v103, v51, v51\n"
    "v_fma_f32 v100, v100, v122, v120\n"
    "s_waitcnt lgkmcnt(3)\n"
    "v_mfma_f32_16x16x32_bf16 v[44:47], v[112:115], v[172:175], v[44:47]\n"
    "ds_read_b128 v[96:99], v0 offset:50176\n"
    "v_fma_f32 v101, v101, v122, v120\n"
    "v_fma_f32 v102, v102, v122, v120\n"
    "v_mfma_f32_16x16x32_bf16 v[36:39], v[112:115], v[168:171], v[36:39]\n"
    "buffer_load_dwordx4 v[24:27], v0, s[8:11], s72 offen offset:0 sc1\n"
    "v_fma_f32 v103, v103, v122, v120\n"
    "v_mfma_f32_16x16x32_bf16 v[32:35], v[10:13], v[176:179], v[32:35]\n"
    "ds_read_b128 v[112:115], v0 offset:51200\n"
    "v_mul_f32 v52, v48, v52\n"
    "v_mul_f32 v48, v100, v48\n"
    "v_mfma_f32_16x16x32_bf16 v[40:43], v[10:13], v[180:183], v[40:43]\n"
    "buffer_load_dwordx4 v[28:31], v0, s[8:11], s72 offen offset:1024 sc1\n"
    "v_mul_f32 v53, v49, v53\n"
    "v_mfma_f32_16x16x32_bf16 v[44:47], v[80:83], v[180:183], v[44:47]\n"
    "ds_read_b128 v[10:13], v0 offset:52224\n"
    "s_add_u32 s71, s87, 28839936\n"
    "v_mul_f32 v49, v101, v49\n"
    "v_mfma_f32_16x16x32_bf16 v[36:39], v[80:83], v[176:179], v[36:39]\n"
    "v_mul_f32 v54, v50, v54\n"
    "v_mul_f32 v50, v102, v50\n"
    "s_waitcnt lgkmcnt(3)\n"
    "v_mfma_f32_16x16x32_bf16 v[32:35], v[84:87], v[184:187], v[32:35]\n"
    "ds_read_b128 v[80:83], v0 offset:53248\n"
    "v_mul_f32 v55, v51, v55\n"
    "v_mul_f32 v51, v103, v51\n"
    "v_mfma_f32_16x16x32_bf16 v[40:43], v[84:87], v[188:191], v[40:43]\n"
    "buffer_load_dwordx4 v[128:131], v0, s[8:11], s71 offen offset:0 sc1\n"
    "v_mfma_f32_16x16x32_bf16 v[44:47], v[88:91], v[188:191], v[44:47]\n"
    "ds_read_b128 v[84:87], v0 offset:54272\n"
    "v_exp_f32 v48, v48\n"
    "v_mfma_f32_16x16x32_bf16 v[36:39], v[88:91], v[184:187], v[36:39]\n"
    "v_exp_f32 v49, v49\n"
    "v_exp_f32 v50, v50\n"
    "v_mfma_f32_16x16x32_bf16 v[32:35], v[92:95], v[192:195], v[32:35]\n"
    "ds_read_b128 v[88:91], v0 offset:55296\n"
    "v_exp_f32 v51, v51\n"
    "v_mfma_f32_16x16x32_bf16 v[40:43], v[92:95], v[196:199], v[40:43]\n"
    "v_fma_f32 v48, v48, v116, v116\n"
    "v_fma_f32 v49, v49, v116, v116\n"
    "s_waitcnt lgkmcnt(3)\n"
    "v_mfma_f32_16x16x32_bf16 v[44:47], v[96:99], v[196:199], v[44:47]\n"
    "ds_read_b128 v[92:95], v0 offset:56320\n"
    "v_fma_f32 v50, v50, v116, v116\n"
    "v_fma_f32 v51, v51, v116, v116\n"
    "v_mfma_f32_16x16x32_bf16 v[36:39], v[96:99], v[192:195], v[36:39]\n"
    "buffer_load_dwordx4 v[132:135], v0, s[8:11], s71 offen offset:1024 sc1\n"
    "v_mfma_f32_16x16x32_bf16 v[32:35], v[112:115], v[200:203], v[32:35]\n"
    "ds_read_b128 v[96:99], v0 offset:57344\n"
    "s_add_u32 s72, s88, 28839936\n"
    "v_mfma_f32_16x16x32_bf16 v[40:43], v[112:115], v[204:207], v[40:43]\n"
    "buffer_load_dwordx4 v[136:139], v0, s[8:11], s72 offen offset:0 sc1\n"
    "v_rcp_f32 v48, v48\n"
    "v_mfma_f32_16x16x32_bf16 v[44:47], v[10:13], v[204:207], v[44:47]\n"
    "ds_read_b128 v[112:115], v0 offset:58368\n"
    "v_rcp_f32 v49, v49\n"
    "v_mfma_f32_16x16x32_bf16 v[36:39], v[10:13], v[200:203], v[36:39]\n"
    "v_rcp_f32 v50, v50\n"
    "s_waitcnt lgkmcnt(3)\n"
    "v_mfma_f32_16x16x32_bf16 v[32:35], v[80:83], v[208:211], v[32:35]\n"
    "ds_read_b128 v[10:13], v0 offset:59392\n"
    "v_rcp_f32 v51, v51\n"
    "v_mfma_f32_16x16x32_bf16 v[40:43], v[80:83], v[212:215], v[40:43]\n"
    "buffer_load_dwordx4 v[140:143], v0, s[8:11], s72 offen offset:1024 sc1\n"
    "v_mul_f32 v52, v52, v48\n"
    "v_mfma_f32_16x16x32_bf16 v[44:47], v[84:87], v[212:215], v[44:47]\n"
    "ds_read_b128 v[80:83], v0 offset:60416\n"
    "v_mul_f32 v53, v53, v49\n"
    "v_mul_f32 v54, v54, v50\n"
    "v_mfma_f32_16x16x32_bf16 v[36:39], v[84:87], v[208:211], v[36:39]\n"
    "v_mul_f32 v55, v55, v51\n"
    "v_cvt_pk_bf16_f32 v108, v52, v53\n"
    "v_cvt_pk_bf16_f32 v109, v54, v55\n"
    "v_mfma_f32_16x16x32_bf16 v[32:35], v[88:91], v[216:219], v[32:35]\n"
    "ds_read_b128 v[84:87], v0 offset:61440\n"
    "v_mul_f32 v100, v56, v56\n"
    "v_mul_f32 v101, v57, v57\n"
    "v_mfma_f32_16x16x32_bf16 v[40:43], v[88:91], v[220:223], v[40:43]\n"
    "v_mul_f32 v102, v58, v58\n"
    "v_mul_f32 v103, v59, v59\n"
    "s_waitcnt lgkmcnt(3)\n"
    "v_mfma_f32_16x16x32_bf16 v[44:47], v[92:95], v[220:223], v[44:47]\n"
    "ds_read_b128 v[88:91], v0 offset:62464\n"
    "buffer_load_dwordx4 v[64:67], v0, s[8:11], s71 offen offset:2048 sc1\n"
    "v_fma_f32 v100, v100, v123, v121\n"
    "v_mfma_f32_16x16x32_bf16 v[36:39], v[92:95], v[216:219], v[36:39]\n"
    "v_fma_f32 v101, v101, v123, v121\n"
    "v_fma_f32 v102, v102, v123, v121\n"
    "v_fma_f32 v103, v103, v123, v121\n"
    "v_mfma_f32_16x16x32_bf16 v[32:35], v[96:99], v[224:227], v[32:35]\n"
    "ds_read_b128 v[92:95], v0 offset:63488\n"
    "v_mul_f32 v60, v56, v60\n"
    "v_mul_f32 v56, v100, v56\n"
    "v_mfma_f32_16x16x32_bf16 v[40:43], v[96:99], v[228:231], v[40:43]\n"
    "buffer_load_dwordx4 v[68:71], v0, s[8:11], s71 offen offset:3072 sc1\n"
    "v_mul_f32 v61, v57, v61\n"
    "v_mfma_f32_16x16x32_bf16 v[44:47], v[112:115], v[228:231], v[44:47]\n"
    "ds_read_b128 v[96:99], v0 offset:64512\n"
    "v_mul_f32 v57, v101, v57\n"
    "v_mul_f32 v62, v58, v62\n"
    "v_mfma_f32_16x16x32_bf16 v[36:39], v[112:115], v[224:227], v[36:39]\n"
    "v_mul_f32 v58, v102, v58\n"
    "s_waitcnt lgkmcnt(3)\n"
    "v_mfma_f32_16x16x32_bf16 v[32:35], v[10:13], v[232:235], v[32:35]\n"
    "buffer_load_dwordx4 v[72:75], v0, s[8:11], s72 offen offset:2048 sc1\n"
    "v_mul_f32 v63, v59, v63\n"
    "v_mfma_f32_16x16x32_bf16 v[40:43], v[10:13], v[236:239], v[40:43]\n"
    "v_mul_f32 v59, v103, v59\n"
    "v_exp_f32 v56, v56\n"
    "v_mfma_f32_16x16x32_bf16 v[44:47], v[80:83], v[236:239], v[44:47]\n"
    "v_exp_f32 v57, v57\n"
    "v_mfma_f32_16x16x32_bf16 v[36:39], v[80:83], v[232:235], v[36:39]\n"
    "buffer_load_dwordx4 v[76:79], v0, s[8:11], s72 offen offset:3072 sc1\n"
    "v_exp_f32 v58, v58\n"
    "v_mfma_f32_16x16x32_bf16 v[32:35], v[84:87], v[240:243], v[32:35]\n"
    "s_add_u32 s71, s87, 28844032\n"
    "v_exp_f32 v59, v59\n"
    "v_mfma_f32_16x16x32_bf16 v[40:43], v[84:87], v[244:247], v[40:43]\n"
    "v_fma_f32 v56, v56, v117, v117\n"
    "v_fma_f32 v57, v57, v117, v117\n"
    "s_waitcnt lgkmcnt(0)\n"
    "v_mfma_f32_16x16x32_bf16 v[44:47], v[88:91], v[244:247], v[44:47]\n"
    "v_fma_f32 v58, v58, v117, v117\n"
    "v_fma_f32 v59, v59, v117, v117\n"
    "v_mfma_f32_16x16x32_bf16 v[36:39], v[88:91], v[240:243], v[36:39]\n"
    "buffer_load_dwordx4 v[144:147], v0, s[8:11], s71 offen offset:0 sc1\n"
    "v_rcp_f32 v56, v56\n"
    "v_mfma_f32_16x16x32_bf16 v[32:35], v[92:95], v[248:251], v[32:35]\n"
    "v_rcp_f32 v57, v57\n"
    "v_mfma_f32_16x16x32_bf16 v[40:43], v[92:95], v[252:255], v[40:43]\n"
    "v_rcp_f32 v58, v58\n"
    "v_rcp_f32 v59, v59\n"
    "v_mfma_f32_16x16x32_bf16 v[44:47], v[96:99], v[252:255], v[44:47]\n"
    "v_mul_f32 v60, v60, v56\n"
    "v_mul_f32 v61, v61, v57\n"
    "v_mul_f32 v62, v62, v58\n"
    "v_mfma_f32_16x16x32_bf16 v[36:39], v[96:99], v[248:251], v[36:39]\n"
    "buffer_load_dwordx4 v[148:151], v0, s[8:11], s71 offen offset:1024 sc1\n"
    "v_mul_f32 v63, v63, v59\n"
    "v_cvt_pk_bf16_f32 v110, v60, v61\n"
    "v_cvt_pk_bf16_f32 v111, v62, v63\n"
    "s_add_u32 s73, s85, 28852224\n"
    "v_permlane32_swap_b32 v108, v110\n"
    "v_permlane32_swap_b32 v109, v111\n"
    "buffer_store_dwordx4 v[108:111], v15, s[8:11], s73 offen sc1\n"
    "v_pk_mul_f32 v[80:81], v[32:33], v[32:33] op_sel:[0,0] op_sel_hi:[1,1]\n"
    "v_pk_mul_f32 v[82:83], v[34:35], v[34:35] op_sel:[0,0] op_sel_hi:[1,1]\n"
    "v_pk_mul_f32 v[84:85], v[40:41], v[40:41] op_sel:[0,0] op_sel_hi:[1,1]\n"
    "v_pk_mul_f32 v[86:87], v[42:43], v[42:43] op_sel:[0,0] op_sel_hi:[1,1]\n"
    "v_pk_fma_f32 v[80:81], v[80:81], v[122:123], v[120:121] op_sel:[0,0,0] op_sel_hi:[1,0,0]\n"
    "v_pk_fma_f32 v[82:83], v[82:83], v[122:123], v[120:121] op_sel:[0,0,0] op_sel_hi:[1,0,0]\n"
    "s_add_u32 s72, s88, 28844032\n"
    "buffer_load_dwordx4 v[152:155], v0, s[8:11], s72 offen offset:0 sc1\n"
    "v_pk_fma_f32 v[84:85], v[84:85], v[122:123], v[120:121] op_sel:[0,1,1] op_sel_hi:[1,1,1]\n"
    "v_pk_fma_f32 v[86:87], v[86:87], v[122:123], v[120:121] op_sel:[0,1,1] op_sel_hi:[1,1,1]\n"
    "v_pk_mul_f32 v[80:81], v[80:81], v[32:33] op_sel:[0,0] op_sel_hi:[1,1]\n"
    "v_pk_mul_f32 v[82:83], v[82:83], v[34:35] op_sel:[0,0] op_sel_hi:[1,1]\n"
    "v_pk_mul_f32 v[84:85], v[84:85], v[40:41] op_sel:[0,0] op_sel_hi:[1,1]\n"
    "v_pk_mul_f32 v[86:87], v[86:87], v[42:43] op_sel:[0,0] op_sel_hi:[1,1]\n"
    "v_exp_f32 v80, v80\n"
    "v_exp_f32 v81, v81\n"
    "buffer_load_dwordx4 v[156:159], v0, s[8:11], s72 offen offset:1024 sc1\n"
    "v_exp_f32 v82, v82\n"
    "v_exp_f32 v83, v83\n"
    "v_exp_f32 v84, v84\n"
    "v_exp_f32 v85, v85\n"
    "v_exp_f32 v86, v86\n"
    "v_exp_f32 v87, v87\n"
    "v_pk_mul_f32 v[36:37], v[32:33], v[36:37] op_sel:[0,0] op_sel_hi:[1,1]\n"
    "v_pk_mul_f32 v[38:39], v[34:35], v[38:39] op_sel:[0,0] op_sel_hi:[1,1]\n"
    "v_pk_mul_f32 v[44:45], v[40:41], v[44:45] op_sel:[0,0] op_sel_hi:[1,1]\n"
    "v_pk_mul_f32 v[46:47], v[42:43], v[46:47] op_sel:[0,0] op_sel_hi:[1,1]\n"
    "buffer_load_dwordx4 v[160:163], v0, s[8:11], s71 offen offset:2048 sc1\n"
    "v_pk_fma_f32 v[80:81], v[80:81], v[116:117], v[116:117] op_sel:[0,0,0] op_sel_hi:[1,0,0]\n"
    "v_pk_fma_f32 v[82:83], v[82:83], v[116:117], v[116:117] op_sel:[0,0,0] op_sel_hi:[1,0,0]\n"
    "v_pk_fma_f32 v[84:85], v[84:85], v[116:117], v[116:117] op_sel:[0,1,1] op_sel_hi:[1,1,1]\n"
    "v_pk_fma_f32 v[86:87], v[86:87], v[116:117], v[116:117] op_sel:[0,1,1] op_sel_hi:[1,1,1]\n"
    "v_rcp_f32 v80, v80\n"
    "v_rcp_f32 v81, v81\n"
    "v_rcp_f32 v82, v82\n"
    "v_rcp_f32 v83, v83\n"
    "buffer_load_dwordx4 v[164:167], v0, s[8:11], s71 offen offset:3072 sc1\n"
    "v_rcp_f32 v84, v84\n"
    "v_rcp_f32 v85, v85\n"
    "v_rcp_f32 v86, v86\n"
    "v_rcp_f32 v87, v87\n"
    "v_pk_mul_f32 v[36:37], v[36:37], v[80:81] op_sel:[0,0] op_sel_hi:[1,1]\n"
    "v_pk_mul_f32 v[38:39], v[38:39], v[82:83] op_sel:[0,0] op_sel_hi:[1,1]\n"
    "v_pk_mul_f32 v[44:45], v[44:45], v[84:85] op_sel:[0,0] op_sel_hi:[1,1]\n"
    "v_pk_mul_f32 v[46:47], v[46:47], v[86:87] op_sel:[0,0] op_sel_hi:[1,1]\n"
    "v_cvt_pk_bf16_f32 v104, v36, v37\n"
    "v_cvt_pk_bf16_f32 v105, v38, v39\n"
    "v_cvt_pk_bf16_f32 v106, v44, v45\n"
    "v_cvt_pk_bf16_f32 v107, v46, v47\n"
    "s_add_u32 s73, s85, 28856320\n"
    "v_permlane32_swap_b32 v104, v106\n"
    "v_permlane32_swap_b32 v105, v107\n"
    "buffer_store_dwordx4 v[104:107], v15, s[8:11], s73 offen sc1\n"
    "s_barrier\n"
    "Lredo62:\n"
    "s_waitcnt vmcnt(19)\n"
    "v_mfma_f32_16x16x32_bf16 v[32:35], a[160:163], v[20:23], 0\n"
    "buffer_load_dwordx4 v[168:171], v0, s[8:11], s72 offen offset:2048 sc1\n"
    "v_mfma_f32_16x16x32_bf16 v[36:39], a[164:167], v[20:23], 0\n"
    "v_accvgpr_read_b32 v208, a220\n"
    "v_accvgpr_read_b32 v209, a221\n"
    "v_mfma_f32_16x16x32_bf16 v[40:43], a[168:171], v[20:23], 0\n"
    "v_accvgpr_read_b32 v210, a222\n"
    "v_accvgpr_read_b32 v211, a223\n"
    "v_mfma_f32_16x16x32_bf16 v[44:47], a[172:175], v[20:23], 0\n"
    "v_accvgpr_read_b32 v212, a224\n"
    "v_accvgpr_read_b32 v213, a225\n"
    "s_waitcnt vmcnt(18)\n"
    "v_mfma_f32_16x16x32_bf16 v[236:239], a[172:175], v[24:27], 0\n"
    "v_accvgpr_read_b32 v214, a226\n"
    "v_accvgpr_read_b32 v215, a227\n"
    "v_mfma_f32_16x16x32_bf16 v[232:235], a[168:171], v[24:27], 0\n"
    "buffer_load_dwordx4 v[172:175], v0, s[8:11], s72 offen offset:3072 sc1\n"
    "v_mfma_f32_16x16x32_bf16 v[228:231], a[164:167], v[24:27], 0\n"
    "s_add_u32 s71, s87, 28848128\n"
    "v_accvgpr_read_b32 v216, a228\n"
    "Lpad11:\n"
    "s_cbranch_execz Lwarm0\n"
    "v_mfma_f32_16x16x32_bf16 v[224:227], a[160:163], v[24:27], 0\n"
    "v_accvgpr_read_b32 v217, a229\n"
    "v_accvgpr_read_b32 v218, a230\n"
    "s_waitcnt vmcnt(18)\n"
    "v_mfma_f32_16x16x32_bf16 v[240:243], a[160:163], v[28:31], 0\n"
    "v_accvgpr_read_b32 v219, a231\n"
    "v_accvgpr_read_b32 v220, a232\n"
    "v_mfma_f32_16x16x32_bf16 v[244:247], a[164:167], v[28:31], 0\n"
    "buffer_load_dwordx4 v[176:179], v0, s[8:11], s71 offen offset:0 sc1\n"
    "v_mfma_f32_16x16x32_bf16 v[248:251], a[168:171], v[28:31], 0\n"
    "v_accvgpr_read_b32 v221, a233\n"
    "v_accvgpr_read_b32 v222, a234\n"
    "v_mfma_f32_16x16x32_bf16 v[252:255], a[172:175], v[28:31], 0\n"
    "v_accvgpr_read_b32 v223, a235\n"
    "s_xor_b32 s62, s58, 0\n"
    "s_nop 0\n"
    "v_mfma_f32_16x16x32_bf16 v[220:223], a[172:175], v[16:19], v[220:223]\n"
    "s_mul_i32 s62, s62, 0x3000\n"
    "s_add_u32 s62, s62, 0x18000\n"
    "v_mfma_f32_16x16x32_bf16 v[216:219], a[168:171], v[16:19], v[216:219]\n"
    "v_add_u32 v10, s62, v0\n"
    "s_xor_b32 s62, s58, 1\n"
    "v_mfma_f32_16x16x32_bf16 v[212:215], a[164:167], v[16:19], v[212:215]\n"
    "buffer_load_dwordx4 v[180:183], v0, s[8:11], s71 offen offset:1024 sc1\n"
    "v_mfma_f32_16x16x32_bf16 v[208:211], a[160:163], v[16:19], v[208:211]\n"
    "s_add_u32 s72, s88, 28848128\n"
    "s_waitcnt vmcnt(18)\n"
    "v_mfma_f32_16x16x32_bf16 v[208:211], a[0:3], v[128:131], v[208:211]\n"
    "s_mul_i32 s62, s62, 0x3000\n"
    "s_add_u32 s62, s62, 0x18000\n"
    "v_mfma_f32_16x16x32_bf16 v[212:215], a[4:7], v[128:131], v[212:215]\n"
    "v_add_u32 v11, s62, v0\n"
    "s_xor_b32 s62, s58, 2\n"
    "v_mfma_f32_16x16x32_bf16 v[216:219], a[8:11], v[128:131], v[216:219]\n"
    "buffer_load_dwordx4 v[184:187], v0, s[8:11], s72 offen offset:0 sc1\n"
    "v_mfma_f32_16x16x32_bf16 v[220:223], a[12:15], v[128:131], v[220:223]\n"
    "s_mul_i32 s62, s62, 0x3000\n"
    "s_add_u32 s62, s62, 0x19000\n"
    "v_mfma_f32_16x16x32_bf16 v[44:47], a[12:15], v[132:135], v[44:47]\n"
    "v_add_u32 v12, s62, v0\n"
    "s_xor_b32 s62, s58, 3\n"
    "v_mfma_f32_16x16x32_bf16 v[40:43], a[8:11], v[132:135], v[40:43]\n"
    "s_mul_i32 s62, s62, 0x3000\n"
    "s_add_u32 s62, s62, 0x1a000\n"
    "v_mfma_f32_16x16x32_bf16 v[36:39], a[4:7], v[132:135], v[36:39]\n"
    "buffer_load_dwordx4 v[188:191], v0, s[8:11], s72 offen offset:1024 sc1\n"
    "v_mfma_f32_16x16x32_bf16 v[32:35], a[0:3], v[132:135], v[32:35]\n"
    "v_add_u32 v13, s62, v0\n"
    "s_waitcnt vmcnt(18)\n"
    "v_mfma_f32_16x16x32_bf16 v[224:227], a[0:3], v[136:139], v[224:227]\n"
    "v_bfe_u32 v8, v0, 4, 4\n"
    "v_mul_u32_u24 v8, 0x90, v8\n"
    "v_mfma_f32_16x16x32_bf16 v[228:231], a[4:7], v[136:139], v[228:231]\n"
    "v_lshrrev_b32 v9, 8, v0\n"
    "v_lshl_add_u32 v8, v9, 3, v8\n"
    "v_mfma_f32_16x16x32_bf16 v[232:235], a[8:11], v[136:139], v[232:235]\n"
    "s_lshl_b32 s62, s58, 12\n"
    "v_add_u32 v14, s62, v8\n"
    "v_mfma_f32_16x16x32_bf16 v[236:239], a[12:15], v[136:139], v[236:239]\n"
    "buffer_load_dwordx4 v[192:195], v0, s[8:11], s71 offen offset:2048 sc1\n"
    "v_mfma_f32_16x16x32_bf16 v[252:255], a[12:15], v[140:143], v[252:255]\n"
    "v_lshrrev_b32 v8, 7, v0\n"
    "v_mul_u32_u24 v8, 0x90, v8\n"
    "v_mfma_f32_16x16x32_bf16 v[248:251], a[8:11], v[140:143], v[248:251]\n"
    "v_and_b32 v9, 0x70, v0\n"
    "v_add_u32 v9, v8, v9\n"
    "v_mfma_f32_16x16x32_bf16 v[244:247], a[4:7], v[140:143], v[244:247]\n"
    "s_lshl_b32 s62, s58, 12\n"
    "v_add_u32 v15, s62, v9\n"
    "v_mfma_f32_16x16x32_bf16 v[240:243], a[0:3], v[140:143], v[240:243]\n"
    "v_and_b32 v8, 0x70, v0\n"
    "s_waitcnt vmcnt(17)\n"
    "v_mfma_f32_16x16x32_bf16 v[208:211], a[16:19], v[64:67], v[208:211]\n"
    "buffer_load_dwordx4 v[196:199], v0, s[8:11], s71 offen offset:3072 sc1\n"
    "v_mfma_f32_16x16x32_bf16 v[212:215], a[20:23], v[64:67], v[212:215]\n"
    "v_lshrrev_b32 v9, 7, v0\n"
    "v_lshl_add_u32 v8, v9, 10, v8\n"
    "v_mfma_f32_16x16x32_bf16 v[216:219], a[24:27], v[64:67], v[216:219]\n"
    "v_mfma_f32_16x16x32_bf16 v[220:223], a[28:31], v[64:67], v[220:223]\n"
    "v_mfma_f32_16x16x32_bf16 v[44:47], a[28:31], v[68:71], v[44:47]\n"
    "buffer_load_dwordx4 v[200:203], v0, s[8:11], s72 offen offset:2048 sc1\n"
    "v_mfma_f32_16x16x32_bf16 v[40:43], a[24:27], v[68:71], v[40:43]\n"
    "v_mfma_f32_16x16x32_bf16 v[36:39], a[20:23], v[68:71], v[36:39]\n"
    "v_mfma_f32_16x16x32_bf16 v[32:35], a[16:19], v[68:71], v[32:35]\n"
    "s_waitcnt vmcnt(17)\n"
    "v_mfma_f32_16x16x32_bf16 v[224:227], a[16:19], v[72:75], v[224:227]\n"
    "buffer_load_dwordx4 v[204:207], v0, s[8:11], s72 offen offset:3072 sc1\n"
    "v_mfma_f32_16x16x32_bf16 v[228:231], a[20:23], v[72:75], v[228:231]\n"
    "s_add_u32 s71, s87, 28852224\n"
    "v_mfma_f32_16x16x32_bf16 v[232:235], a[24:27], v[72:75], v[232:235]\n"
    "v_mfma_f32_16x16x32_bf16 v[236:239], a[28:31], v[72:75], v[236:239]\n"
    "v_mfma_f32_16x16x32_bf16 v[252:255], a[28:31], v[76:79], v[252:255]\n"
    "buffer_load_dwordx4 v[48:51], v0, s[8:11], s71 offen offset:0 sc1\n"
    "v_mfma_f32_16x16x32_bf16 v[248:251], a[24:27], v[76:79], v[248:251]\n"
    "v_mfma_f32_16x16x32_bf16 v[244:247], a[20:23], v[76:79], v[244:247]\n"
    "v_mfma_f32_16x16x32_bf16 v[240:243], a[16:19], v[76:79], v[240:243]\n"
    "s_waitcnt vmcnt(17)\n"
    "v_mfma_f32_16x16x32_bf16 v[208:211], a[32:35], v[144:147], v[208:211]\n"
    "v_mfma_f32_16x16x32_bf16 v[212:215], a[36:39], v[144:147], v[212:215]\n"
    "buffer_load_dwordx4 v[52:55], v0, s[8:11], s71 offen offset:1024 sc1\n"
    "v_mfma_f32_16x16x32_bf16 v[216:219], a[40:43], v[144:147], v[216:219]\n"
    "s_add_u32 s72, s88, 28852224\n"
    "v_mfma_f32_16x16x32_bf16 v[220:223], a[44:47], v[144:147], v[220:223]\n"
    "v_mfma_f32_16x16x32_bf16 v[44:47], a[44:47], v[148:151], v[44:47]\n"
    "v_mfma_f32_16x16x32_bf16 v[40:43], a[40:43], v[148:151], v[40:43]\n"
    "buffer_load_dwordx4 v[56:59], v0, s[8:11], s72 offen offset:0 sc1\n"
    "v_mfma_f32_16x16x32_bf16 v[36:39], a[36:39], v[148:151], v[36:39]\n"
    "v_mfma_f32_16x16x32_bf16 v[32:35], a[32:35], v[148:151], v[32:35]\n"
    "s_waitcnt vmcnt(16)\n"
    "v_mfma_f32_16x16x32_bf16 v[224:227], a[32:35], v[152:155], v[224:227]\n"
    "v_mfma_f32_16x16x32_bf16 v[228:231], a[36:39], v[152:155], v[228:231]\n"
    "v_mfma_f32_16x16x32_bf16 v[232:235], a[40:43], v[152:155], v[232:235]\n"
    "buffer_load_dwordx4 v[60:63], v0, s[8:11], s72 offen offset:1024 sc1\n"
    "v_mfma_f32_16x16x32_bf16 v[236:239], a[44:47], v[152:155], v[236:239]\n"
    "v_mfma_f32_16x16x32_bf16 v[252:255], a[44:47], v[156:159], v[252:255]\n"
    "v_mfma_f32_16x16x32_bf16 v[248:251], a[40:43], v[156:159], v[248:251]\n"
    "v_mfma_f32_16x16x32_bf16 v[244:247], a[36:39], v[156:159], v[244:247]\n"
    "buffer_load_dwordx4 v[112:115], v0, s[8:11], s71 offen offset:3072 sc1\n"
    "v_mfma_f32_16x16x32_bf16 v[240:243], a[32:35], v[156:159], v[240:243]\n"
    "s_waitcnt vmcnt(16)\n"
    "v_mfma_f32_16x16x32_bf16 v[208:211], a[48:51], v[160:163], v[208:211]\n"
    "v_mfma_f32_16x16x32_bf16 v[212:215], a[52:55], v[160:163], v[212:215]\n"
    "v_mfma_f32_16x16x32_bf16 v[216:219], a[56:59], v[160:163], v[216:219]\n"
    "v_mfma_f32_16x16x32_bf16 v[220:223], a[60:63], v[160:163], v[220:223]\n"
    "v_mfma_f32_16x16x32_bf16 v[44:47], a[60:63], v[164:167], v[44:47]\n"
    "v_mfma_f32_16x16x32_bf16 v[40:43], a[56:59], v[164:167], v[40:43]\n"
    "s_waitcnt vmcnt(12)\n"
    "v_max3_u32 v7, v176, v177, v178\n"
    "v_max_u32 v7, v7, v179\n"
    "v_mfma_f32_16x16x32_bf16 v[36:39], a[52:55], v[164:167], v[36:39]\n"
    "s_waitcnt vmcnt(11)\n"
    "v_max3_u32 v7, v7, v180, v181\n"
    "v_max3_u32 v7, v7, v182, v183\n"
    "v_mfma_f32_16x16x32_bf16 v[32:35], a[48:51], v[164:167], v[32:35]\n"
    "s_waitcnt vmcnt(10)\n"
    "v_max3_u32 v7, v7, v184, v185\n"
    "v_mfma_f32_16x16x32_bf16 v[224:227], a[48:51], v[168:171], v[224:227]\n"
    "v_max3_u32 v7, v7, v186, v187\n"
    "s_waitcnt vmcnt(9)\n"
    "v_max3_u32 v7, v7, v188, v189\n"
    "v_mfma_f32_16x16x32_bf16 v[228:231], a[52:55], v[168:171], v[228:231]\n"
    "s_add_u32 s71, s87, 28856320\n"
    "v_max3_u32 v7, v7, v190, v191\n"
    "v_mfma_f32_16x16x32_bf16 v[232:235], a[56:59], v[168:171], v[232:235]\n"
    "v_cmp_eq_u32 s[26:27], -1, v7\n"
    "v_mfma_f32_16x16x32_bf16 v[236:239], a[60:63], v[168:171], v[236:239]\n"
    "s_cmp_lg_u64 s[26:27], 0\n"
    "s_cbranch_scc1 Lgb63\n"
    "Lgo63:\n"
    "v_mfma_f32_16x16x32_bf16 v[252:255], a[60:63], v[172:175], v[252:255]\n"
    "buffer_load_dwordx4 v[88:91], v0, s[8:11], s71 offen offset:1024 sc1\n"
    "v_mfma_f32_16x16x32_bf16 v[248:251], a[56:59], v[172:175], v[248:251]\n"
    "v_mfma_f32_16x16x32_bf16 v[244:247], a[52:55], v[172:175], v[244:247]\n"
    "v_mfma_f32_16x16x32_bf16 v[240:243], a[48:51], v[172:175], v[240:243]\n"
    "v_cmp_u_f32 s[30:31], v208, v32\n"
    "v_mfma_f32_16x16x32_bf16 v[208:211], a[64:67], v[176:179], v[208:211]\n"
    "buffer_load_dwordx4 v[92:95], v0, s[8:11], s71 offen offset:3072 sc1\n"
    "v_mfma_f32_16x16x32_bf16 v[212:215], a[68:71], v[176:179], v[212:215]\n"
    "v_mfma_f32_16x16x32_bf16 v[216:219], a[72:75], v[176:179], v[216:219]\n"
    "v_mfma_f32_16x16x32_bf16 v[220:223], a[76:79], v[176:179], v[220:223]\n"
    "v_mfma_f32_16x16x32_bf16 v[44:47], a[76:79], v[180:183], v[44:47]\n"
    "buffer_load_dwordx4 v[96:99], v0, s[8:11], s72 offen offset:2048 sc1\n"
    "v_mfma_f32_16x16x32_bf16 v[40:43], a[72:75], v[180:183], v[40:43]\n"
    "s_waitcnt vmcnt(11)\n"
    "v_max3_u32 v7, v192, v193, v194\n"
    "v_max_u32 v7, v7, v195\n"
    "v_mfma_f32_16x16x32_bf16 v[36:39], a[68:71], v[180:183], v[36:39]\n"
    "s_add_u32 s72, s88, 28856320\n"
    "s_waitcnt vmcnt(10)\n"
    "v_max3_u32 v7, v7, v196, v197\n"
    "v_mfma_f32_16x16x32_bf16 v[32:35], a[64:67], v[180:183], v[32:35]\n"
    "v_max3_u32 v7, v7, v198, v199\n"
    "v_cmp_u_f32 s[24:25], v224, v240\n"
    "v_mfma_f32_16x16x32_bf16 v[224:227], a[64:67], v[184:187], v[224:227]\n"
    "s_waitcnt vmcnt(9)\n"
    "v_max3_u32 v7, v7, v200, v201\n"
    "v_max3_u32 v7, v7, v202, v203\n"
    "v_mfma_f32_16x16x32_bf16 v[228:231], a[68:71], v[184:187], v[228:231]\n"
    "buffer_load_dwordx4 v[100:103], v0, s[8:11], s72 offen offset:0 sc1\n"
    "v_mfma_f32_16x16x32_bf16 v[232:235], a[72:75], v[184:187], v[232:235]\n"
    "s_waitcnt vmcnt(9)\n"
    "v_max3_u32 v7, v7, v204, v205\n"
    "v_max3_u32 v7, v7, v206, v207\n"
    "v_mfma_f32_16x16x32_bf16 v[236:239], a[76:79], v[184:187], v[236:239]\n"
    "s_or_b64 s[30:31], s[30:31], s[24:25]\n"
    "s_cbranch_scc1 Lbad70\n"
    "Lok70:\n"
    "v_mfma_f32_16x16x32_bf16 v[252:255], a[76:79], v[188:191], v[252:255]\n"
    "v_cmp_eq_u32 s[26:27], -1, v7\n"
    "v_mfma_f32_16x16x32_bf16 v[248:251], a[72:75], v[188:191], v[248:251]\n"
    "buffer_load_dwordx4 v[108:111], v0, s[8:11], s72 offen offset:2048 sc1\n"
    "v_mfma_f32_16x16x32_bf16 v[244:247], a[68:71], v[188:191], v[244:247]\n"
    "s_add_u32 s72, s88, 28852224\n"
    "s_cmp_lg_u64 s[26:27], 0\n"
    "s_cbranch_scc1 Lgb64\n"
    "Lgo64:\n"
    "v_mfma_f32_16x16x32_bf16 v[240:243], a[64:67], v[188:191], v[240:243]\n"
    "v_mfma_f32_16x16x32_bf16 v[208:211], a[80:83], v[192:195], v[208:211]\n"
    "v_mfma_f32_16x16x32_bf16 v[212:215], a[84:87], v[192:195], v[212:215]\n"
    "buffer_load_dwordx4 v[116:119], v0, s[8:11], s72 offen offset:3072 sc1\n"
    "v_mfma_f32_16x16x32_bf16 v[216:219], a[88:91], v[192:195], v[216:219]\n"
    "v_mfma_f32_16x16x32_bf16 v[220:223], a[92:95], v[192:195], v[220:223]\n"
    "s_add_u32 s72, s88, 28856320\n"
    "v_mfma_f32_16x16x32_bf16 v[44:47], a[92:95], v[196:199], v[44:47]\n"
    "v_mfma_f32_16x16x32_bf16 v[40:43], a[88:91], v[196:199], v[40:43]\n"
    "s_waitcnt vmcnt(10)\n"
    "v_max3_u32 v7, v48, v49, v50\n"
    "v_max_u32 v7, v7, v51\n"
    "v_mfma_f32_16x16x32_bf16 v[36:39], a[84:87], v[196:199], v[36:39]\n"
    "buffer_load_dwordx4 v[120:123], v0, s[8:11], s72 offen offset:1024 sc1\n"
    "v_mfma_f32_16x16x32_bf16 v[32:35], a[80:83], v[196:199], v[32:35]\n"
    "s_waitcnt vmcnt(10)\n"
    "v_max3_u32 v7, v7, v52, v53\n"
    "v_max3_u32 v7, v7, v54, v55\n"
    "v_mfma_f32_16x16x32_bf16 v[224:227], a[80:83], v[200:203], v[224:227]\n"
    "s_waitcnt vmcnt(9)\n"
    "v_max3_u32 v7, v7, v56, v57\n"
    "v_max3_u32 v7, v7, v58, v59\n"
    "v_mfma_f32_16x16x32_bf16 v[228:231], a[84:87], v[200:203], v[228:231]\n"
    "s_waitcnt vmcnt(8)\n"
    "v_max3_u32 v7, v7, v60, v61\n"
    "v_max3_u32 v7, v7, v62, v63\n"
    "v_mfma_f32_16x16x32_bf16 v[232:235], a[88:91], v[200:203], v[232:235]\n"
    "buffer_load_dwordx4 v[124:127], v0, s[8:11], s72 offen offset:3072 sc1\n"
    "v_mfma_f32_16x16x32_bf16 v[236:239], a[92:95], v[200:203], v[236:239]\n"
    "v_cmp_eq_u32 s[26:27], -1, v7\n"
    "v_mfma_f32_16x16x32_bf16 v[252:255], a[92:95], v[204:207], v[252:255]\n"
    "s_add_u32 s71, s87, 28852224\n"
    "s_cmp_lg_u64 s[26:27], 0\n"
    "s_cbranch_scc1 Lgb65\n"
    "Lgo65:\n"
    "v_mfma_f32_16x16x32_bf16 v[248:251], a[88:91], v[204:207], v[248:251]\n"
    "v_mfma_f32_16x16x32_bf16 v[244:247], a[84:87], v[204:207], v[244:247]\n"
    "v_mfma_f32_16x16x32_bf16 v[240:243], a[80:83], v[204:207], v[240:243]\n"
    "buffer_load_dwordx4 v[80:83], v0, s[8:11], s71 offen offset:2048 sc1\n"
    "v_mfma_f32_16x16x32_bf16 v[208:211], a[96:99], v[48:51], v[208:211]\n"
    "s_add_u32 s71, s87, 28856320\n"
    "v_mfma_f32_16x16x32_bf16 v[212:215], a[100:103], v[48:51], v[212:215]\n"
    "v_mfma_f32_16x16x32_bf16 v[216:219], a[104:107], v[48:51], v[216:219]\n"
    "v_mfma_f32_16x16x32_bf16 v[220:223], a[108:111], v[48:51], v[220:223]\n"
    "buffer_load_dwordx4 v[84:87], v0, s[8:11], s71 offen offset:0 sc1\n"
    "v_mfma_f32_16x16x32_bf16 v[44:47], a[108:111], v[52:55], v[44:47]\n"
    "v_mfma_f32_16x16x32_bf16 v[40:43], a[104:107], v[52:55], v[40:43]\n"
    "v_mfma_f32_16x16x32_bf16 v[36:39], a[100:103], v[52:55], v[36:39]\n"
    "s_waitcnt vmcnt(10)\n"
    "v_max3_u32 v7, v112, v113, v114\n"
    "v_max_u32 v7, v7, v115\n"
    "v_mfma_f32_16x16x32_bf16 v[32:35], a[96:99], v[52:55], v[32:35]\n"
    "buffer_load_dwordx4 v[104:107], v0, s[8:11], s71 offen offset:2048 sc1\n"
    "v_mfma_f32_16x16x32_bf16 v[224:227], a[96:99], v[56:59], v[224:227]\n"
    "s_waitcnt vmcnt(10)\n"
    "v_max3_u32 v7, v7, v88, v89\n"
    "v_max3_u32 v7, v7, v90, v91\n"
    "v_mfma_f32_16x16x32_bf16 v[228:231], a[100:103], v[56:59], v[228:231]\n"
    "s_waitcnt vmcnt(9)\n"
    "v_max3_u32 v7, v7, v92, v93\n"
    "v_max3_u32 v7, v7, v94, v95\n"
    "v_mfma_f32_16x16x32_bf16 v[232:235], a[104:107], v[56:59], v[232:235]\n"
    "v_cmp_eq_u32 s[26:27], -1, v7\n"
    "v_mfma_f32_16x16x32_bf16 v[236:239], a[108:111], v[56:59], v[236:239]\n"
    "s_cmp_lg_u64 s[26:27], 0\n"
    "s_cbranch_scc1 Lgb66\n"
    "Lgo66:\n"
    "v_mfma_f32_16x16x32_bf16 v[252:255], a[108:111], v[60:63], v[252:255]\n"
    "buffer_load_dword a236, off, s[12:15], s91\n"
    "v_mfma_f32_16x16x32_bf16 v[248:251], a[104:107], v[60:63], v[248:251]\n"
    "v_mfma_f32_16x16x32_bf16 v[244:247], a[100:103], v[60:63], v[244:247]\n"
    "v_mfma_f32_16x16x32_bf16 v[240:243], a[96:99], v[60:63], v[240:243]\n"
    "v_mfma_f32_16x16x32_bf16 v[32:35], a[112:115], v[112:115], v[32:35]\n"
    "v_mfma_f32_16x16x32_bf16 v[36:39], a[116:119], v[112:115], v[36:39]\n"
    "v_mfma_f32_16x16x32_bf16 v[40:43], a[120:123], v[112:115], v[40:43]\n"
    "s_waitcnt vmcnt(9)\n"
    "v_max3_u32 v7, v96, v97, v98\n"
    "v_max_u32 v7, v7, v99\n"
    "v_mfma_f32_16x16x32_bf16 v[44:47], a[124:127], v[112:115], v[44:47]\n"
    "s_waitcnt vmcnt(8)\n"
    "v_max3_u32 v7, v7, v100, v101\n"
    "v_max3_u32 v7, v7, v102, v103\n"
    "v_mfma_f32_16x16x32_bf16 v[44:47], a[140:143], v[88:91], v[44:47]\n"
    "s_waitcnt vmcnt(7)\n"
    "v_max3_u32 v7, v7, v108, v109\n"
    "v_max3_u32 v7, v7, v110, v111\n"
    "v_mfma_f32_16x16x32_bf16 v[40:43], a[136:139], v[88:91], v[40:43]\n"
    "v_cmp_eq_u32 s[26:27], -1, v7\n"
    "v_mfma_f32_16x16x32_bf16 v[36:39], a[132:135], v[88:91], v[36:39]\n"
    "s_cmp_lg_u64 s[26:27], 0\n"
    "s_cbranch_scc1 Lgb67\n"
    "Lgo67:\n"
    "v_mfma_f32_16x16x32_bf16 v[32:35], a[128:131], v[88:91], v[32:35]\n"
    "v_mfma_f32_16x16x32_bf16 v[32:35], a[144:147], v[92:95], v[32:35]\n"
    "v_mfma_f32_16x16x32_bf16 v[36:39], a[148:151], v[92:95], v[36:39]\n"
    "v_mfma_f32_16x16x32_bf16 v[40:43], a[152:155], v[92:95], v[40:43]\n"
    "v_mfma_f32_16x16x32_bf16 v[44:47], a[156:159], v[92:95], v[44:47]\n"
    "v_mfma_f32_16x16x32_bf16 v[224:227], a[112:115], v[96:99], v[224:227]\n"
    "s_nop 3\n"
    "ds_write_b128 v11, v[32:35] offset:0\n"
    "v_mfma_f32_16x16x32_bf16 v[228:231], a[116:119], v[96:99], v[228:231]\n"
    "v_mfma_f32_16x16x32_bf16 v[232:235], a[120:123], v[96:99], v[232:235]\n"
    "s_waitcnt vmcnt(6)\n"
    "v_max3_u32 v7, v116, v117, v118\n"
    "v_max_u32 v7, v7, v119\n"
    "v_mfma_f32_16x16x32_bf16 v[236:239], a[124:127], v[96:99], v[236:239]\n"
    "ds_write_b128 v11, v[36:39] offset:1024\n"
    "v_mfma_f32_16x16x32_bf16 v[236:239], a[140:143], v[100:103], v[236:239]\n"
    "s_waitcnt vmcnt(5)\n"
    "v_max3_u32 v7, v7, v120, v121\n"
    "v_max3_u32 v7, v7, v122, v123\n"
    "v_mfma_f32_16x16x32_bf16 v[232:235], a[136:139], v[100:103], v[232:235]\n"
    "s_waitcnt vmcnt(4)\n"
    "v_max3_u32 v7, v7, v124, v125\n"
    "v_max3_u32 v7, v7, v126, v127\n"
    "v_mfma_f32_16x16x32_bf16 v[228:231], a[132:135], v[100:103], v[228:231]\n"
    "ds_write_b128 v11, v[40:43] offset:2048\n"
    "v_mfma_f32_16x16x32_bf16 v[224:227], a[128:131], v[100:103], v[224:227]\n"
    "v_cmp_eq_u32 s[26:27], -1, v7\n"
    "v_mfma_f32_16x16x32_bf16 v[224:227], a[144:147], v[108:111], v[224:227]\n"
    "s_cmp_lg_u64 s[26:27], 0\n"
    "s_cbranch_scc1 Lgb68\n"
    "Lgo68:\n"
    "v_mfma_f32_16x16x32_bf16 v[228:231], a[148:151], v[108:111], v[228:231]\n"
    "ds_write_b128 v11, v[44:47] offset:3072\n"
    "v_mfma_f32_16x16x32_bf16 v[232:235], a[152:155], v[108:111], v[232:235]\n"
    "v_mfma_f32_16x16x32_bf16 v[236:239], a[156:159], v[108:111], v[236:239]\n"
    "v_mfma_f32_16x16x32_bf16 v[240:243], a[112:115], v[116:119], v[240:243]\n"
    "s_nop 0\n"
    "ds_write_b128 v12, v[224:227] offset:0\n"
    "v_mfma_f32_16x16x32_bf16 v[244:247], a[116:119], v[116:119], v[244:247]\n"
    "v_mfma_f32_16x16x32_bf16 v[248:251], a[120:123], v[116:119], v[248:251]\n"
    "s_waitcnt vmcnt(3)\n"
    "v_max3_u32 v7, v80, v81, v82\n"
    "v_max_u32 v7, v7, v83\n"
    "v_mfma_f32_16x16x32_bf16 v[252:255], a[124:127], v[116:119], v[252:255]\n"
    "ds_write_b128 v12, v[228:231] offset:1024\n"
    "v_mfma_f32_16x16x32_bf16 v[252:255], a[140:143], v[120:123], v[252:255]\n"
    "s_waitcnt vmcnt(2)\n"
    "v_max3_u32 v7, v7, v84, v85\n"
    "v_max3_u32 v7, v7, v86, v87\n"
    "v_mfma_f32_16x16x32_bf16 v[248:251], a[136:139], v[120:123], v[248:251]\n"
    "s_waitcnt vmcnt(1)\n"
    "v_max3_u32 v7, v7, v104, v105\n"
    "v_max3_u32 v7, v7, v106, v107\n"
    "v_mfma_f32_16x16x32_bf16 v[244:247], a[132:135], v[120:123], v[244:247]\n"
    "ds_write_b128 v12, v[232:235] offset:2048\n"
    "v_mfma_f32_16x16x32_bf16 v[240:243], a[128:131], v[120:123], v[240:243]\n"
    "v_cmp_eq_u32 s[26:27], -1, v7\n"
    "v_mfma_f32_16x16x32_bf16 v[240:243], a[144:147], v[124:127], v[240:243]\n"
    "s_cmp_lg_u64 s[26:27], 0\n"
    "s_cbranch_scc1 Lgb69\n"
    "Lgo69:\n"
    "v_mfma_f32_16x16x32_bf16 v[244:247], a[148:151], v[124:127], v[244:247]\n"
    "ds_write_b128 v12, v[236:239] offset:3072\n"
    "v_mfma_f32_16x16x32_bf16 v[248:251], a[152:155], v[124:127], v[248:251]\n"
    "v_mfma_f32_16x16x32_bf16 v[252:255], a[156:159], v[124:127], v[252:255]\n"
    "v_mfma_f32_16x16x32_bf16 v[208:211], a[112:115], v[80:83], v[208:211]\n"
    "s_nop 0\n"
    "ds_write_b128 v13, v[240:243] offset:0\n"
    "v_mfma_f32_16x16x32_bf16 v[212:215], a[116:119], v[80:83], v[212:215]\n"
    "v_mfma_f32_16x16x32_bf16 v[216:219], a[120:123], v[80:83], v[216:219]\n"
    "v_mfma_f32_16x16x32_bf16 v[220:223], a[124:127], v[80:83], v[220:223]\n"
    "ds_write_b128 v13, v[244:247] offset:1024\n"
    "v_mfma_f32_16x16x32_bf16 v[220:223], a[140:143], v[84:87], v[220:223]\n"
    "v_mfma_f32_16x16x32_bf16 v[216:219], a[136:139], v[84:87], v[216:219]\n"
    "v_mfma_f32_16x16x32_bf16 v[212:215], a[132:135], v[84:87], v[212:215]\n"
    "ds_write_b128 v13, v[248:251] offset:2048\n"
    "v_mfma_f32_16x16x32_bf16 v[208:211], a[128:131], v[84:87], v[208:211]\n"
    "v_mfma_f32_16x16x32_bf16 v[208:211], a[144:147], v[104:107], v[208:211]\n"
    "v_mfma_f32_16x16x32_bf16 v[212:215], a[148:151], v[104:107], v[212:215]\n"
    "ds_write_b128 v13, v[252:255] offset:3072\n"
    "v_mfma_f32_16x16x32_bf16 v[216:219], a[152:155], v[104:107], v[216:219]\n"
    "v_mfma_f32_16x16x32_bf16 v[220:223], a[156:159], v[104:107], v[220:223]\n"
    "s_waitcnt lgkmcnt(0)\n"
    "s_nop 0\n"
    "s_barrier\n"
    "ds_read_b128 v[16:19], v10 offset:0\n"
    "ds_read_b128 v[20:23], v10 offset:1024\n"
    "ds_read_b128 v[24:27], v10 offset:2048\n"
    "ds_read_b128 v[28:31], v10 offset:3072\n"
    "ds_read_b128 v[64:67], v10 offset:4096\n"
    "ds_read_b128 v[68:71], v10 offset:5120\n"
    "ds_read_b128 v[72:75], v10 offset:6144\n"
    "ds_read_b128 v[76:79], v10 offset:7168\n"
    "ds_read_b128 v[48:51], v10 offset:8192\n"
    "ds_read_b128 v[52:55], v10 offset:9216\n"
    "ds_read_b128 v[56:59], v10 offset:10240\n"
    "ds_read_b128 v[60:63], v10 offset:11264\n"
    "s_waitcnt lgkmcnt(11)\n"
    "v_pk_add_f32 v[208:209], v[208:209], v[16:17] op_sel:[0,0] op_sel_hi:[1,1]\n"
    "v_pk_add_f32 v[210:211], v[210:211], v[18:19] op_sel:[0,0] op_sel_hi:[1,1]\n"
    "s_waitcnt lgkmcnt(10)\n"
    "v_pk_add_f32 v[212:213], v[212:213], v[20:21] op_sel:[0,0] op_sel_hi:[1,1]\n"
    "v_pk_add_f32 v[214:215], v[214:215], v[22:23] op_sel:[0,0] op_sel_hi:[1,1]\n"
    "s_waitcnt lgkmcnt(9)\n"
    "v_pk_add_f32 v[216:217], v[216:217], v[24:25] op_sel:[0,0] op_sel_hi:[1,1]\n"
    "v_pk_add_f32 v[218:219], v[218:219], v[26:27] op_sel:[0,0] op_sel_hi:[1,1]\n"
    "s_waitcnt lgkmcnt(8)\n"
    "v_pk_add_f32 v[220:221], v[220:221], v[28:29] op_sel:[0,0] op_sel_hi:[1,1]\n"
    "v_pk_add_f32 v[222:223], v[222:223], v[30:31] op_sel:[0,0] op_sel_hi:[1,1]\n"
    "s_waitcnt lgkmcnt(7)\n"
    "v_pk_add_f32 v[208:209], v[208:209], v[64:65] op_sel:[0,0] op_sel_hi:[1,1]\n"
    "v_pk_add_f32 v[210:211], v[210:211], v[66:67] op_sel:[0,0] op_sel_hi:[1,1]\n"
    "s_waitcnt lgkmcnt(6)\n"
    "v_pk_add_f32 v[212:213], v[212:213], v[68:69] op_sel:[0,0] op_sel_hi:[1,1]\n"
    "v_pk_add_f32 v[214:215], v[214:215], v[70:71] op_sel:[0,0] op_sel_hi:[1,1]\n"
    "s_waitcnt lgkmcnt(5)\n"
    "v_pk_add_f32 v[216:217], v[216:217], v[72:73] op_sel:[0,0] op_sel_hi:[1,1]\n"
    "v_pk_add_f32 v[218:219], v[218:219], v[74:75] op_sel:[0,0] op_sel_hi:[1,1]\n"
    "s_waitcnt lgkmcnt(4)\n"
    "v_pk_add_f32 v[220:221], v[220:221], v[76:77] op_sel:[0,0] op_sel_hi:[1,1]\n"
    "v_pk_add_f32 v[222:223], v[222:223], v[78:79] op_sel:[0,0] op_sel_hi:[1,1]\n"
    "s_waitcnt lgkmcnt(3)\n"
    "v_pk_add_f32 v[208:209], v[208:209], v[48:49] op_sel:[0,0] op_sel_hi:[1,1]\n"
    "v_pk_add_f32 v[210:211], v[210:211], v[50:51] op_sel:[0,0] op_sel_hi:[1,1]\n"
    "s_waitcnt lgkmcnt(2)\n"
    "v_pk_add_f32 v[212:213], v[212:213], v[52:53] op_sel:[0,0] op_sel_hi:[1,1]\n"
    "v_pk_add_f32 v[214:215], v[214:215], v[54:55] op_sel:[0,0] op_sel_hi:[1,1]\n"
    "s_waitcnt lgkmcnt(1)\n"
    "v_pk_add_f32 v[216:217], v[216:217], v[56:57] op_sel:[0,0] op_sel_hi:[1,1]\n"
    "v_pk_add_f32 v[218:219], v[218:219], v[58:59] op_sel:[0,0] op_sel_hi:[1,1]\n"
    "s_waitcnt lgkmcnt(0)\n"
    "v_pk_add_f32 v[220:221], v[220:221], v[60:61] op_sel:[0,0] op_sel_hi:[1,1]\n"
    "v_pk_add_f32 v[222:223], v[222:223], v[62:63] op_sel:[0,0] op_sel_hi:[1,1]\n"
    "v_cvt_pk_bf16_f32 v80, v208, v209\n"
    "v_cvt_pk_bf16_f32 v81, v210, v211\n"
    "v_cvt_pk_bf16_f32 v82, v212, v213\n"
    "v_cvt_pk_bf16_f32 v83, v214, v215\n"
    "v_cvt_pk_bf16_f32 v84, v216, v217\n"
    "v_cvt_pk_bf16_f32 v85, v218, v219\n"
    "v_cvt_pk_bf16_f32 v86, v220, v221\n"
    "v_cvt_pk_bf16_f32 v87, v222, v223\n"
    "ds_write_b64 v14, v[80:81] offset:0\n"
    "ds_write_b64 v14, v[82:83] offset:32\n"
    "ds_write_b64 v14, v[84:85] offset:64\n"
    "ds_write_b64 v14, v[86:87] offset:96\n"
    "ds_read_b128 v[88:91], v15 offset:0\n"
    "ds_read_b128 v[92:95], v15 offset:1152\n"
    "s_add_u32 s73, s91, 8192\n"
    "s_waitcnt lgkmcnt(1)\n"
    "buffer_store_dwordx4 v[88:91], v8, s[12:15], s91 offen sc1\n"
    "s_waitcnt lgkmcnt(0)\n"
    "buffer_store_dwordx4 v[92:95], v8, s[12:15], s73 offen sc1\n"
    "s_endpgm\n"
    "Lslow1:\n"
    "s_cmp_eq_u32 s61, 0\n"
    "s_cbranch_scc1 Lback1\n"
    "s_sub_u32 s61, s61, 1\n"
    "s_sleep 2\n"
    "s_add_u32 s29, s67, 589824\n"
    "buffer_load_dwordx2 v[8:9], v6, s[8:11], s29 offen sc1\n"
    "s_waitcnt vmcnt(0)\n"
    "v_cmp_ne_u64 vcc, s[34:35], v[8:9]\n"
    "s_nop 0\n"
    "s_and_b32 s29, vcc_lo, s95\n"
    "s_cbranch_scc1 Lslow1\n"
    "s_branch Lback1\n"
    "Lslow5:\n"
    "v_mov_b32 v8, 0\n"
    "buffer_atomic_add v8, v6, s[8:11], s68 offen sc0\n"
    "s_waitcnt vmcnt(6)\n"
    "v_readfirstlane_b32 s29, v9\n"
    "s_nop 0\n"
    "s_cmp_ge_u32 s29, 16\n"
    "s_cbranch_scc1 Lback5\n"
    "Lspin5:\n"
    "s_sleep 4\n"
    "v_mov_b32 v9, 0\n"
    "buffer_atomic_add v9, v6, s[8:11], s68 offen sc0\n"
    "s_waitcnt vmcnt(1)\n"
    "v_readfirstlane_b32 s29, v8\n"
    "s_nop 0\n"
    "s_cmp_ge_u32 s29, 16\n"
    "s_cbranch_scc1 Lback5\n"
    "s_sleep 4\n"
    "v_mov_b32 v8, 0\n"
    "buffer_atomic_add v8, v6, s[8:11], s68 offen sc0\n"
    "s_waitcnt vmcnt(1)\n"
    "v_readfirstlane_b32 s29, v9\n"
    "s_nop 0\n"
    "s_cmp_ge_u32 s29, 16\n"
    "s_cbranch_scc1 Lback5\n"
    "s_cmp_eq_u32 s61, 0\n"
    "s_cbranch_scc1 Lback5\n"
    "s_sub_u32 s61, s61, 1\n"
    "s_branch Lspin5\n"
    "Lslow10:\n"
    "v_mov_b32 v8, 0\n"
    "buffer_atomic_add v8, v6, s[8:11], s68 offen sc0\n"
    "s_waitcnt vmcnt(2)\n"
    "v_readfirstlane_b32 s29, v9\n"
    "s_nop 0\n"
    "s_cmp_ge_u32 s29, 16\n"
    "s_cbranch_scc1 Lback10\n"
    "Lspin10:\n"
    "s_sleep 4\n"
    "v_mov_b32 v9, 0\n"
    "buffer_atomic_add v9, v6, s[8:11], s68 offen sc0\n"
    "s_waitcnt vmcnt(1)\n"
    "v_readfirstlane_b32 s29, v8\n"
    "s_nop 0\n"
    "s_cmp_ge_u32 s29, 16\n"
    "s_cbranch_scc1 Lback10\n"
    "s_sleep 4\n"
    "v_mov_b32 v8, 0\n"
    "buffer_atomic_add v8, v6, s[8:11], s68 offen sc0\n"
    "s_waitcnt vmcnt(1)\n"
    "v_readfirstlane_b32 s29, v9\n"
    "s_nop 0\n"
    "s_cmp_ge_u32 s29, 16\n"
    "s_cbranch_scc1 Lback10\n"
    "s_cmp_eq_u32 s61, 0\n"
    "s_cbranch_scc1 Lback10\n"
    "s_sub_u32 s61, s61, 1\n"
    "s_branch Lspin10\n"
    "Lslow13:\n"
    "v_mov_b32 v8, 0\n"
    "buffer_atomic_add v8, v6, s[8:11], s68 offen sc0\n"
    "Lspin13:\n"
    "s_sleep 4\n"
    "v_mov_b32 v9, 0\n"
    "buffer_atomic_add v9, v6, s[8:11], s68 offen sc0\n"
    "s_waitcnt vmcnt(1)\n"
    "v_readfirstlane_b32 s29, v8\n"
    "s_nop 0\n"
    "s_cmp_ge_u32 s29, 16\n"
    "s_cbranch_scc1 Lback13\n"
    "s_sleep 4\n"
    "v_mov_b32 v8, 0\n"
    "buffer_atomic_add v8, v6, s[8:11], s68 offen sc0\n"
    "s_waitcnt vmcnt(1)\n"
    "v_readfirstlane_b32 s29, v9\n"
    "s_nop 0\n"
    "s_cmp_ge_u32 s29, 16\n"
    "s_cbranch_scc1 Lback13\n"
    "s_cmp_eq_u32 s61, 0\n"
    "s_cbranch_scc1 Lback13\n"
    "s_sub_u32 s61, s61, 1\n"
    "s_branch Lspin13\n"
    "Lslow23:\n"
    "s_cmp_eq_u32 s61, 0\n"
    "s_cbranch_scc1 Lback23\n"
    "s_sub_u32 s61, s61, 1\n"
    "s_sleep 2\n"
    "s_add_u32 s29, s67, 98304\n"
    "v_mov_b32 v8, 0\n"
    "buffer_atomic_add v8, v6, s[8:11], s29 offen sc0\n"
    "s_waitcnt vmcnt(0)\n"
    "v_readfirstlane_b32 s29, v8\n"
    "s_nop 0\n"
    "s_cmp_lt_u32 s29, 16\n"
    "s_cbranch_scc1 Lslow23\n"
    "s_branch Lback23\n"
    "Lslow34:\n"
    "v_mov_b32 v8, 0\n"
    "buffer_atomic_add v8, v6, s[8:11], s68 offen sc0\n"
    "Lspin34:\n"
    "s_sleep 4\n"
    "v_mov_b32 v9, 0\n"
    "buffer_atomic_add v9, v6, s[8:11], s68 offen sc0\n"
    "s_waitcnt vmcnt(1)\n"
    "v_readfirstlane_b32 s29, v8\n"
    "s_nop 0\n"
    "s_cmp_ge_u32 s29, 16\n"
    "s_cbranch_scc1 Lback34\n"
    "s_sleep 4\n"
    "v_mov_b32 v8, 0\n"
    "buffer_atomic_add v8, v6, s[8:11], s68 offen sc0\n"
    "s_waitcnt vmcnt(1)\n"
    "v_readfirstlane_b32 s29, v9\n"
    "s_nop 0\n"
    "s_cmp_ge_u32 s29, 16\n"
    "s_cbranch_scc1 Lback34\n"
    "s_cmp_eq_u32 s61, 0\n"
    "s_cbranch_scc1 Lback34\n"
    "s_sub_u32 s61, s61, 1\n"
    "s_branch Lspin34\n"
    "Lslow43:\n"
    "v_mov_b32 v8, 0\n"
    "buffer_atomic_add v8, v6, s[8:11], s68 offen sc0\n"
    "s_waitcnt vmcnt(4)\n"
    "v_readfirstlane_b32 s29, v9\n"
    "s_nop 0\n"
    "s_cmp_ge_u32 s29, 16\n"
    "s_cbranch_scc1 Lback43\n"
    "Lspin43:\n"
    "s_sleep 4\n"
    "v_mov_b32 v9, 0\n"
    "buffer_atomic_add v9, v6, s[8:11], s68 offen sc0\n"
    "s_waitcnt vmcnt(1)\n"
    "v_readfirstlane_b32 s29, v8\n"
    "s_nop 0\n"
    "s_cmp_ge_u32 s29, 16\n"
    "s_cbranch_scc1 Lback43\n"
    "s_sleep 4\n"
    "v_mov_b32 v8, 0\n"
    "buffer_atomic_add v8, v6, s[8:11], s68 offen sc0\n"
    "s_waitcnt vmcnt(1)\n"
    "v_readfirstlane_b32 s29, v9\n"
    "s_nop 0\n"
    "s_cmp_ge_u32 s29, 16\n"
    "s_cbranch_scc1 Lback43\n"
    "s_cmp_eq_u32 s61, 0\n"
    "s_cbranch_scc1 Lback43\n"
    "s_sub_u32 s61, s61, 1\n"
    "s_branch Lspin43\n"
    "Lslow48:\n"
    "v_mov_b32 v8, 0\n"
    "buffer_atomic_add v8, v6, s[8:11], s68 offen sc0\n"
    "s_waitcnt vmcnt(1)\n"
    "v_readfirstlane_b32 s29, v9\n"
    "s_nop 0\n"
    "s_cmp_ge_u32 s29, 16\n"
    "s_cbranch_scc1 Lback48\n"
    "Lspin48:\n"
    "s_sleep 4\n"
    "v_mov_b32 v9, 0\n"
    "buffer_atomic_add v9, v6, s[8:11], s68 offen sc0\n"
    "s_waitcnt vmcnt(1)\n"
    "v_readfirstlane_b32 s29, v8\n"
    "s_nop 0\n"
    "s_cmp_ge_u32 s29, 16\n"
    "s_cbranch_scc1 Lback48\n"
    "s_sleep 4\n"
    "v_mov_b32 v8, 0\n"
    "buffer_atomic_add v8, v6, s[8:11], s68 offen sc0\n"
    "s_waitcnt vmcnt(1)\n"
    "v_readfirstlane_b32 s29, v9\n"
    "s_nop 0\n"
    "s_cmp_ge_u32 s29, 16\n"
    "s_cbranch_scc1 Lback48\n"
    "s_cmp_eq_u32 s61, 0\n"
    "s_cbranch_scc1 Lback48\n"
    "s_sub_u32 s61, s61, 1\n"
    "s_branch Lspin48\n"
    "Lslow51:\n"
    "v_mov_b32 v8, 0\n"
    "buffer_atomic_add v8, v6, s[8:11], s68 offen sc0\n"
    "Lspin51:\n"
    "s_sleep 4\n"
    "v_mov_b32 v9, 0\n"
    "buffer_atomic_add v9, v6, s[8:11], s68 offen sc0\n"
    "s_waitcnt vmcnt(1)\n"
    "v_readfirstlane_b32 s29, v8\n"
    "s_nop 0\n"
    "s_cmp_ge_u32 s29, 16\n"
    "s_cbranch_scc1 Lback51\n"
    "s_sleep 4\n"
    "v_mov_b32 v8, 0\n"
    "buffer_atomic_add v8, v6, s[8:11], s68 offen sc0\n"
    "s_waitcnt vmcnt(1)\n"
    "v_readfirstlane_b32 s29, v9\n"
    "s_nop 0\n"
    "s_cmp_ge_u32 s29, 16\n"
    "s_cbranch_scc1 Lback51\n"
    "s_cmp_eq_u32 s61, 0\n"
    "s_cbranch_scc1 Lback51\n"
    "s_sub_u32 s61, s61, 1\n"
    "s_branch Lspin51\n"
    "Lslow61:\n"
    "s_cmp_eq_u32 s61, 0\n"
    "s_cbranch_scc1 Lback61\n"
    "s_sub_u32 s61, s61, 1\n"
    "s_sleep 2\n"
    "s_add_u32 s29, s67, 393216\n"
    "v_mov_b32 v8, 0\n"
    "buffer_atomic_add v8, v6, s[8:11], s29 offen sc0\n"
    "s_waitcnt vmcnt(0)\n"
    "v_readfirstlane_b32 s29, v8\n"
    "s_nop 0\n"
    "s_cmp_lt_u32 s29, 16\n"
    "s_cbranch_scc1 Lslow61\n"
    "s_branch Lback61\n"
    "Lbad8:\n"
    "s_cmp_eq_u32 s28, 15\n"
    "s_cbranch_scc1 Lok8\n"
    "s_waitcnt vmcnt(0) lgkmcnt(0)\n"
    "s_nop 15\n"
    "Lag7:\n"
    "s_cmp_eq_u32 s28, 14\n"
    "s_mov_b32 s28, 14\n"
    "s_cbranch_scc1 Lver7\n"
    "s_add_u32 s72, s64, 1708032\n"
    "buffer_load_dwordx4 v[160:163], v14, s[8:11], s72 offen sc1\n"
    "s_add_u32 s71, s90, 659456\n"
    "buffer_load_dwordx4 v[128:131], v0, s[8:11], s71 offen offset:0 sc1\n"
    "buffer_load_dwordx4 v[132:135], v0, s[8:11], s71 offen offset:1024 sc1\n"
    "buffer_load_dwordx4 v[136:139], v0, s[8:11], s71 offen offset:2048 sc1\n"
    "buffer_load_dwordx4 v[140:143], v0, s[8:11], s71 offen offset:3072 sc1\n"
    "s_add_u32 s71, s90, 663552\n"
    "buffer_load_dwordx4 v[144:147], v0, s[8:11], s71 offen offset:0 sc1\n"
    "buffer_load_dwordx4 v[148:151], v0, s[8:11], s71 offen offset:1024 sc1\n"
    "buffer_load_dwordx4 v[152:155], v0, s[8:11], s71 offen offset:2048 sc1\n"
    "buffer_load_dwordx4 v[156:159], v0, s[8:11], s71 offen offset:3072 sc1\n"
    "Lgq7:\n"
    "s_waitcnt vmcnt(0) lgkmcnt(0)\n"
    "s_add_u32 s72, s64, 1708032\n"
    "s_add_u32 m0, s66, 102400\n"
    "s_add_u32 s69, s76, 54272\n"
    "s_add_u32 s71, s90, 663552\n"
    "s_add_u32 s70, s36, 40960\n"
    "s_branch Lredo7\n"
    "Lver7:\n"
    "s_mov_b32 s28, 15\n"
    "s_add_u32 s72, s64, 1708032\n"
    "buffer_load_dwordx4 v[160:163], v14, s[8:11], s72 offen sc1\n"
    "s_add_u32 s71, s90, 659456\n"
    "buffer_load_dwordx4 v[128:131], v0, s[8:11], s71 offen offset:0 sc1\n"
    "buffer_load_dwordx4 v[132:135], v0, s[8:11], s71 offen offset:1024 sc1\n"
    "buffer_load_dwordx4 v[136:139], v0, s[8:11], s71 offen offset:2048 sc1\n"
    "buffer_load_dwordx4 v[140:143], v0, s[8:11], s71 offen offset:3072 sc1\n"
    "s_add_u32 s71, s90, 663552\n"
    "buffer_load_dwordx4 v[144:147], v0, s[8:11], s71 offen offset:0 sc1\n"
    "buffer_load_dwordx4 v[148:151], v0, s[8:11], s71 offen offset:1024 sc1\n"
    "buffer_load_dwordx4 v[152:155], v0, s[8:11], s71 offen offset:2048 sc1\n"
    "buffer_load_dwordx4 v[156:159], v0, s[8:11], s71 offen offset:3072 sc1\n"
    "s_waitcnt vmcnt(8)\n"
    "v_max3_u32 v7, v160, v161, v162\n"
    "v_max_u32 v7, v7, v163\n"
    "s_waitcnt vmcnt(7)\n"
    "v_max3_u32 v7, v7, v128, v129\n"
    "v_max3_u32 v7, v7, v130, v131\n"
    "s_waitcnt vmcnt(6)\n"
    "v_max3_u32 v7, v7, v132, v133\n"
    "v_max3_u32 v7, v7, v134, v135\n"
    "s_waitcnt vmcnt(5)\n"
    "v_max3_u32 v7, v7, v136, v137\n"
    "v_max3_u32 v7, v7, v138, v139\n"
    "s_waitcnt vmcnt(4)\n"
    "v_max3_u32 v7, v7, v140, v141\n"
    "v_max3_u32 v7, v7, v142, v143\n"
    "s_waitcnt vmcnt(3)\n"
    "v_max3_u32 v7, v7, v144, v145\n"
    "v_max3_u32 v7, v7, v146, v147\n"
    "s_waitcnt vmcnt(2)\n"
    "v_max3_u32 v7, v7, v148, v149\n"
    "v_max3_u32 v7, v7, v150, v151\n"
    "s_waitcnt vmcnt(1)\n"
    "v_max3_u32 v7, v7, v152, v153\n"
    "v_max3_u32 v7, v7, v154, v155\n"
    "s_waitcnt vmcnt(0)\n"
    "v_max3_u32 v7, v7, v156, v157\n"
    "v_max3_u32 v7, v7, v158, v159\n"
    "v_cmp_eq_u32 vcc, -1, v7\n"
    "s_nop 0\n"
    "s_cbranch_vccz Lgo7\n"
    "s_cmp_eq_u32 s61, 0\n"
    "s_cbranch_scc1 Lgo7\n"
    "s_sub_u32 s61, s61, 1\n"
    "s_branch Lver7\n"
    "Lgo7:\n"
    "s_branch Lgq7\n"
    "Lbad12:\n"
    "s_cmp_eq_u32 s28, 23\n"
    "s_cbranch_scc1 Lok12\n"
    "s_waitcnt vmcnt(0) lgkmcnt(0)\n"
    "s_nop 15\n"
    "Lag11:\n"
    "s_cmp_eq_u32 s28, 22\n"
    "s_mov_b32 s28, 22\n"
    "s_cbranch_scc1 Lver11\n"
    "s_add_u32 s71, s84, 18878464\n"
    "buffer_load_dwordx4 v[128:131], v0, s[8:11], s71 offen offset:0 sc1\n"
    "buffer_load_dwordx4 v[132:135], v0, s[8:11], s71 offen offset:1024 sc1\n"
    "buffer_load_dwordx4 v[136:139], v0, s[8:11], s71 offen offset:2048 sc1\n"
    "buffer_load_dwordx4 v[140:143], v0, s[8:11], s71 offen offset:3072 sc1\n"
    "s_add_u32 s71, s84, 18882560\n"
    "buffer_load_dwordx4 v[144:147], v0, s[8:11], s71 offen offset:0 sc1\n"
    "buffer_load_dwordx4 v[148:151], v0, s[8:11], s71 offen offset:1024 sc1\n"
    "buffer_load_dwordx4 v[152:155], v0, s[8:11], s71 offen offset:2048 sc1\n"
    "buffer_load_dwordx4 v[156:159], v0, s[8:11], s71 offen offset:3072 sc1\n"
    "s_add_u32 s71, s84, 18886656\n"
    "buffer_load_dwordx4 v[160:163], v0, s[8:11], s71 offen offset:0 sc1\n"
    "buffer_load_dwordx4 v[164:167], v0, s[8:11], s71 offen offset:1024 sc1\n"
    "Lgq11:\n"
    "ds_read_b64_tr_b16 v[48:49], v12 offset:0\n"
    "ds_read_b64_tr_b16 v[50:51], v12 offset:1024\n"
    "ds_read_b64_tr_b16 v[52:53], v13 offset:0\n"
    "ds_read_b64_tr_b16 v[54:55], v13 offset:1024\n"
    "ds_read_b64_tr_b16 v[56:57], v14 offset:0\n"
    "ds_read_b64_tr_b16 v[58:59], v14 offset:1024\n"
    "ds_read_b64_tr_b16 v[60:61], v15 offset:0\n"
    "ds_read_b64_tr_b16 v[62:63], v15 offset:1024\n"
    "ds_read_b64_tr_b16 v[64:65], v12 offset:4096\n"
    "ds_read_b64_tr_b16 v[66:67], v12 offset:5120\n"
    "ds_read_b64_tr_b16 v[68:69], v13 offset:4096\n"
    "ds_read_b64_tr_b16 v[70:71], v13 offset:5120\n"
    "ds_read_b64_tr_b16 v[72:73], v14 offset:4096\n"
    "ds_read_b64_tr_b16 v[74:75], v14 offset:5120\n"
    "ds_read_b64_tr_b16 v[76:77], v15 offset:4096\n"
    "ds_read_b64_tr_b16 v[78:79], v15 offset:5120\n"
    "ds_read_b64_tr_b16 v[80:81], v12 offset:8192\n"
    "ds_read_b64_tr_b16 v[82:83], v12 offset:9216\n"
    "ds_read_b64_tr_b16 v[84:85], v13 offset:8192\n"
    "ds_read_b64_tr_b16 v[86:87], v13 offset:9216\n"
    "ds_read_b64_tr_b16 v[88:89], v14 offset:8192\n"
    "ds_read_b64_tr_b16 v[90:91], v14 offset:9216\n"
    "ds_read_b64_tr_b16 v[92:93], v15 offset:8192\n"
    "ds_read_b64_tr_b16 v[94:95], v15 offset:9216\n"
    "ds_read_b64_tr_b16 v[96:97], v12 offset:12288\n"
    "ds_read_b64_tr_b16 v[98:99], v12 offset:13312\n"
    "ds_read_b64_tr_b16 v[100:101], v13 offset:12288\n"
    "ds_read_b64_tr_b16 v[102:103], v13 offset:13312\n"
    "ds_read_b64_tr_b16 v[104:105], v14 offset:12288\n"
    "ds_read_b64_tr_b16 v[106:107], v14 offset:13312\n"
    "ds_read_b64_tr_b16 v[108:109], v15 offset:12288\n"
    "ds_read_b64_tr_b16 v[110:111], v15 offset:13312\n"
    "ds_read_b64_tr_b16 v[112:113], v12 offset:16384\n"
    "ds_read_b64_tr_b16 v[114:115], v12 offset:17408\n"
    "ds_read_b64_tr_b16 v[116:117], v13 offset:16384\n"
    "ds_read_b64_tr_b16 v[118:119], v13 offset:17408\n"
    "ds_read_b64_tr_b16 v[120:121], v14 offset:16384\n"
    "ds_read_b64_tr_b16 v[122:123], v14 offset:17408\n"
    "ds_read_b64_tr_b16 v[124:125], v15 offset:16384\n"
    "ds_read_b64_tr_b16 v[126:127], v15 offset:17408\n"
    "ds_read_b64_tr_b16 v[208:209], v12 offset:20480\n"
    "ds_read_b64_tr_b16 v[210:211], v12 offset:21504\n"
    "ds_read_b64_tr_b16 v[212:213], v13 offset:20480\n"
    "ds_read_b64_tr_b16 v[214:215], v13 offset:21504\n"
    "ds_read_b64_tr_b16 v[216:217], v14 offset:20480\n"
    "ds_read_b64_tr_b16 v[218:219], v14 offset:21504\n"
    "ds_read_b64_tr_b16 v[220:221], v15 offset:20480\n"
    "ds_read_b64_tr_b16 v[222:223], v15 offset:21504\n"
    "ds_read_b64_tr_b16 v[224:225], v12 offset:24576\n"
    "ds_read_b64_tr_b16 v[226:227], v12 offset:25600\n"
    "ds_read_b64_tr_b16 v[228:229], v13 offset:24576\n"
    "ds_read_b64_tr_b16 v[230:231], v13 offset:25600\n"
    "ds_read_b64_tr_b16 v[232:233], v14 offset:24576\n"
    "ds_read_b64_tr_b16 v[234:235], v14 offset:25600\n"
    "ds_read_b64_tr_b16 v[236:237], v15 offset:24576\n"
    "ds_read_b64_tr_b16 v[238:239], v15 offset:25600\n"
    "ds_read_b64_tr_b16 v[240:241], v12 offset:28672\n"
    "ds_read_b64_tr_b16 v[242:243], v12 offset:29696\n"
    "ds_read_b64_tr_b16 v[244:245], v13 offset:28672\n"
    "ds_read_b64_tr_b16 v[246:247], v13 offset:29696\n"
    "ds_read_b64_tr_b16 v[248:249], v14 offset:28672\n"
    "ds_read_b64_tr_b16 v[250:251], v14 offset:29696\n"
    "ds_read_b64_tr_b16 v[252:253], v15 offset:28672\n"
    "ds_read_b64_tr_b16 v[254:255], v15 offset:29696\n"
    "ds_read_b64_tr_b16 v[32:33], v12 offset:32768\n"
    "ds_read_b64_tr_b16 v[34:35], v12 offset:33792\n"
    "ds_read_b64_tr_b16 v[36:37], v13 offset:32768\n"
    "ds_read_b64_tr_b16 v[38:39], v13 offset:33792\n"
    "ds_read_b64_tr_b16 v[40:41], v14 offset:32768\n"
    "ds_read_b64_tr_b16 v[42:43], v14 offset:33792\n"
    "v_accvgpr_read_b32 v16, a220\n"
    "v_accvgpr_read_b32 v17, a221\n"
    "v_accvgpr_read_b32 v18, a222\n"
    "v_accvgpr_read_b32 v19, a223\n"
    "v_accvgpr_read_b32 v20, a224\n"
    "v_accvgpr_read_b32 v21, a225\n"
    "v_accvgpr_read_b32 v22, a226\n"
    "v_accvgpr_read_b32 v23, a227\n"
    "v_accvgpr_read_b32 v24, a228\n"
    "v_accvgpr_read_b32 v25, a229\n"
    "v_accvgpr_read_b32 v26, a230\n"
    "v_accvgpr_read_b32 v27, a231\n"
    "v_accvgpr_read_b32 v28, a232\n"
    "v_accvgpr_read_b32 v29, a233\n"
    "v_accvgpr_read_b32 v30, a234\n"
    "v_accvgpr_read_b32 v31, a235\n"
    "s_waitcnt vmcnt(0) lgkmcnt(0)\n"
    "s_add_u32 s72, s64, 1708032\n"
    "s_add_u32 m0, s66, 36864\n"
    "s_add_u32 s69, s77, 4096\n"
    "s_add_u32 s71, s84, 18886656\n"
    "s_add_u32 s70, s79, 4096\n"
    "s_branch Lredo11\n"
    "Lver11:\n"
    "s_mov_b32 s28, 23\n"
    "s_add_u32 s71, s84, 18878464\n"
    "buffer_load_dwordx4 v[128:131], v0, s[8:11], s71 offen offset:0 sc1\n"
    "buffer_load_dwordx4 v[132:135], v0, s[8:11], s71 offen offset:1024 sc1\n"
    "buffer_load_dwordx4 v[136:139], v0, s[8:11], s71 offen offset:2048 sc1\n"
    "buffer_load_dwordx4 v[140:143], v0, s[8:11], s71 offen offset:3072 sc1\n"
    "s_add_u32 s71, s84, 18882560\n"
    "buffer_load_dwordx4 v[144:147], v0, s[8:11], s71 offen offset:0 sc1\n"
    "buffer_load_dwordx4 v[148:151], v0, s[8:11], s71 offen offset:1024 sc1\n"
    "buffer_load_dwordx4 v[152:155], v0, s[8:11], s71 offen offset:2048 sc1\n"
    "buffer_load_dwordx4 v[156:159], v0, s[8:11], s71 offen offset:3072 sc1\n"
    "s_add_u32 s71, s84, 18886656\n"
    "buffer_load_dwordx4 v[160:163], v0, s[8:11], s71 offen offset:0 sc1\n"
    "buffer_load_dwordx4 v[164:167], v0, s[8:11], s71 offen offset:1024 sc1\n"
    "buffer_load_dwordx4 v[168:171], v0, s[8:11], s71 offen offset:2048 sc1\n"
    "buffer_load_dwordx4 v[172:175], v0, s[8:11], s71 offen offset:3072 sc1\n"
    "s_add_u32 s71, s84, 18890752\n"
    "buffer_load_dwordx4 v[176:179], v0, s[8:11], s71 offen offset:0 sc1\n"
    "buffer_load_dwordx4 v[180:183], v0, s[8:11], s71 offen offset:1024 sc1\n"
    "buffer_load_dwordx4 v[184:187], v0, s[8:11], s71 offen offset:2048 sc1\n"
    "buffer_load_dwordx4 v[188:191], v0, s[8:11], s71 offen offset:3072 sc1\n"
    "s_waitcnt vmcnt(15)\n"
    "v_max3_u32 v7, v128, v129, v130\n"
    "v_max_u32 v7, v7, v131\n"
    "s_waitcnt vmcnt(14)\n"
    "v_max3_u32 v7, v7, v132, v133\n"
    "v_max3_u32 v7, v7, v134, v135\n"
    "s_waitcnt vmcnt(13)\n"
    "v_max3_u32 v7, v7, v136, v137\n"
    "v_max3_u32 v7, v7, v138, v139\n"
    "s_waitcnt vmcnt(12)\n"
    "v_max3_u32 v7, v7, v140, v141\n"
    "v_max3_u32 v7, v7, v142, v143\n"
    "s_waitcnt vmcnt(11)\n"
    "v_max3_u32 v7, v7, v144, v145\n"
    "v_max3_u32 v7, v7, v146, v147\n"
    "s_waitcnt vmcnt(10)\n"
    "v_max3_u32 v7, v7, v148, v149\n"
    "v_max3_u32 v7, v7, v150, v151\n"
    "s_waitcnt vmcnt(9)\n"
    "v_max3_u32 v7, v7, v152, v153\n"
    "v_max3_u32 v7, v7, v154, v155\n"
    "s_waitcnt vmcnt(8)\n"
    "v_max3_u32 v7, v7, v156, v157\n"
    "v_max3_u32 v7, v7, v158, v159\n"
    "s_waitcnt vmcnt(7)\n"
    "v_max3_u32 v7, v7, v160, v161\n"
    "v_max3_u32 v7, v7, v162, v163\n"
    "s_waitcnt vmcnt(6)\n"
    "v_max3_u32 v7, v7, v164, v165\n"
    "v_max3_u32 v7, v7, v166, v167\n"
    "s_waitcnt vmcnt(5)\n"
    "v_max3_u32 v7, v7, v168, v169\n"
    "v_max3_u32 v7, v7, v170, v171\n"
    "s_waitcnt vmcnt(4)\n"
    "v_max3_u32 v7, v7, v172, v173\n"
    "v_max3_u32 v7, v7, v174, v175\n"
    "s_waitcnt vmcnt(3)\n"
    "v_max3_u32 v7, v7, v176, v177\n"
    "v_max3_u32 v7, v7, v178, v179\n"
    "s_waitcnt vmcnt(2)\n"
    "v_max3_u32 v7, v7, v180, v181\n"
    "v_max3_u32 v7, v7, v182, v183\n"
    "s_waitcnt vmcnt(1)\n"
    "v_max3_u32 v7, v7, v184, v185\n"
    "v_max3_u32 v7, v7, v186, v187\n"
    "s_waitcnt vmcnt(0)\n"
    "v_max3_u32 v7, v7, v188, v189\n"
    "v_max3_u32 v7, v7, v190, v191\n"
    "v_cmp_eq_u32 vcc, -1, v7\n"
    "s_nop 0\n"
    "s_cbranch_vccz Lgo11\n"
    "s_cmp_eq_u32 s61, 0\n"
    "s_cbranch_scc1 Lgo11\n"
    "s_sub_u32 s61, s61, 1\n"
    "s_branch Lver11\n"
    "Lgo11:\n"
    "s_branch Lgq11\n"
    "Lbad17:\n"
    "s_cmp_eq_u32 s28, 33\n"
    "s_cbranch_scc1 Lok17\n"
    "s_waitcnt vmcnt(0) lgkmcnt(0)\n"
    "s_nop 15\n"
    "Lag16:\n"
    "s_cmp_eq_u32 s28, 32\n"
    "s_mov_b32 s28, 32\n"
    "s_cbranch_scc1 Lver16\n"
    "s_add_u32 s71, s80, 12193792\n"
    "buffer_load_dwordx4 v[128:131], v0, s[8:11], s71 offen offset:0 sc1\n"
    "buffer_load_dwordx4 v[132:135], v0, s[8:11], s71 offen offset:1024 sc1\n"
    "buffer_load_dwordx4 v[136:139], v0, s[8:11], s71 offen offset:2048 sc1\n"
    "buffer_load_dwordx4 v[140:143], v0, s[8:11], s71 offen offset:3072 sc1\n"
    "s_add_u32 s71, s80, 12197888\n"
    "buffer_load_dwordx4 v[144:147], v0, s[8:11], s71 offen offset:0 sc1\n"
    "buffer_load_dwordx4 v[148:151], v0, s[8:11], s71 offen offset:1024 sc1\n"
    "buffer_load_dwordx4 v[152:155], v0, s[8:11], s71 offen offset:2048 sc1\n"
    "buffer_load_dwordx4 v[156:159], v0, s[8:11], s71 offen offset:3072 sc1\n"
    "s_add_u32 s71, s80, 12201984\n"
    "buffer_load_dwordx4 v[160:163], v0, s[8:11], s71 offen offset:0 sc1\n"
    "buffer_load_dwordx4 v[164:167], v0, s[8:11], s71 offen offset:1024 sc1\n"
    "Lgq16:\n"
    "s_waitcnt vmcnt(0) lgkmcnt(0)\n"
    "s_add_u32 s72, s64, 1708032\n"
    "s_add_u32 m0, s66, 0\n"
    "s_add_u32 s69, s78, 0\n"
    "s_add_u32 s71, s80, 12201984\n"
    "s_add_u32 s70, s79, 12288\n"
    "s_branch Lredo16\n"
    "Lver16:\n"
    "s_mov_b32 s28, 33\n"
    "s_add_u32 s71, s80, 12193792\n"
    "buffer_load_dwordx4 v[128:131], v0, s[8:11], s71 offen offset:0 sc1\n"
    "buffer_load_dwordx4 v[132:135], v0, s[8:11], s71 offen offset:1024 sc1\n"
    "buffer_load_dwordx4 v[136:139], v0, s[8:11], s71 offen offset:2048 sc1\n"
    "buffer_load_dwordx4 v[140:143], v0, s[8:11], s71 offen offset:3072 sc1\n"
    "s_add_u32 s71, s80, 12197888\n"
    "buffer_load_dwordx4 v[144:147], v0, s[8:11], s71 offen offset:0 sc1\n"
    "buffer_load_dwordx4 v[148:151], v0, s[8:11], s71 offen offset:1024 sc1\n"
    "buffer_load_dwordx4 v[152:155], v0, s[8:11], s71 offen offset:2048 sc1\n"
    "buffer_load_dwordx4 v[156:159], v0, s[8:11], s71 offen offset:3072 sc1\n"
    "s_add_u32 s71, s80, 12201984\n"
    "buffer_load_dwordx4 v[160:163], v0, s[8:11], s71 offen offset:0 sc1\n"
    "buffer_load_dwordx4 v[164:167], v0, s[8:11], s71 offen offset:1024 sc1\n"
    "buffer_load_dwordx4 v[168:171], v0, s[8:11], s71 offen offset:2048 sc1\n"
    "buffer_load_dwordx4 v[172:175], v0, s[8:11], s71 offen offset:3072 sc1\n"
    "s_add_u32 s71, s80, 12206080\n"
    "buffer_load_dwordx4 v[176:179], v0, s[8:11], s71 offen offset:0 sc1\n"
    "buffer_load_dwordx4 v[180:183], v0, s[8:11], s71 offen offset:1024 sc1\n"
    "buffer_load_dwordx4 v[184:187], v0, s[8:11], s71 offen offset:2048 sc1\n"
    "buffer_load_dwordx4 v[188:191], v0, s[8:11], s71 offen offset:3072 sc1\n"
    "s_add_u32 s72, s80, 12226560\n"
    "buffer_load_dwordx4 v[100:103], v0, s[8:11], s72 offen offset:0 sc1\n"
    "s_add_u32 s71, s80, 12210176\n"
    "buffer_load_dwordx4 v[192:195], v0, s[8:11], s71 offen offset:0 sc1\n"
    "buffer_load_dwordx4 v[196:199], v0, s[8:11], s71 offen offset:1024 sc1\n"
    "buffer_load_dwordx4 v[104:107], v0, s[8:11], s72 offen offset:1024 sc1\n"
    "buffer_load_dwordx4 v[200:203], v0, s[8:11], s71 offen offset:2048 sc1\n"
    "buffer_load_dwordx4 v[204:207], v0, s[8:11], s71 offen offset:3072 sc1\n"
    "s_add_u32 s71, s80, 12214272\n"
    "buffer_load_dwordx4 v[208:211], v0, s[8:11], s71 offen offset:0 sc1\n"
    "buffer_load_dwordx4 v[212:215], v0, s[8:11], s71 offen offset:1024 sc1\n"
    "buffer_load_dwordx4 v[216:219], v0, s[8:11], s71 offen offset:2048 sc1\n"
    "buffer_load_dwordx4 v[220:223], v0, s[8:11], s71 offen offset:3072 sc1\n"
    "s_add_u32 s71, s80, 12218368\n"
    "buffer_load_dwordx4 v[224:227], v0, s[8:11], s71 offen offset:0 sc1\n"
    "buffer_load_dwordx4 v[228:231], v0, s[8:11], s71 offen offset:1024 sc1\n"
    "buffer_load_dwordx4 v[232:235], v0, s[8:11], s71 offen offset:2048 sc1\n"
    "buffer_load_dwordx4 v[236:239], v0, s[8:11], s71 offen offset:3072 sc1\n"
    "s_add_u32 s71, s80, 12222464\n"
    "buffer_load_dwordx4 v[240:243], v0, s[8:11], s71 offen offset:0 sc1\n"
    "buffer_load_dwordx4 v[244:247], v0, s[8:11], s71 offen offset:1024 sc1\n"
    "buffer_load_dwordx4 v[248:251], v0, s[8:11], s71 offen offset:2048 sc1\n"
    "buffer_load_dwordx4 v[252:255], v0, s[8:11], s71 offen offset:3072 sc1\n"
    "s_waitcnt vmcnt(33)\n"
    "v_max3_u32 v7, v128, v129, v130\n"
    "v_max_u32 v7, v7, v131\n"
    "s_waitcnt vmcnt(32)\n"
    "v_max3_u32 v7, v7, v132, v133\n"
    "v_max3_u32 v7, v7, v134, v135\n"
    "s_waitcnt vmcnt(31)\n"
    "v_max3_u32 v7, v7, v136, v137\n"
    "v_max3_u32 v7, v7, v138, v139\n"
    "s_waitcnt vmcnt(30)\n"
    "v_max3_u32 v7, v7, v140, v141\n"
    "v_max3_u32 v7, v7, v142, v143\n"
    "s_waitcnt vmcnt(29)\n"
    "v_max3_u32 v7, v7, v144, v145\n"
    "v_max3_u32 v7, v7, v146, v147\n"
    "s_waitcnt vmcnt(28)\n"
    "v_max3_u32 v7, v7, v148, v149\n"
    "v_max3_u32 v7, v7, v150, v151\n"
    "s_waitcnt vmcnt(27)\n"
    "v_max3_u32 v7, v7, v152, v153\n"
    "v_max3_u32 v7, v7, v154, v155\n"
    "s_waitcnt vmcnt(26)\n"
    "v_max3_u32 v7, v7, v156, v157\n"
    "v_max3_u32 v7, v7, v158, v159\n"
    "s_waitcnt vmcnt(25)\n"
    "v_max3_u32 v7, v7, v160, v161\n"
    "v_max3_u32 v7, v7, v162, v163\n"
    "s_waitcnt vmcnt(24)\n"
    "v_max3_u32 v7, v7, v164, v165\n"
    "v_max3_u32 v7, v7, v166, v167\n"
    "s_waitcnt vmcnt(23)\n"
    "v_max3_u32 v7, v7, v168, v169\n"
    "v_max3_u32 v7, v7, v170, v171\n"
    "s_waitcnt vmcnt(22)\n"
    "v_max3_u32 v7, v7, v172, v173\n"
    "v_max3_u32 v7, v7, v174, v175\n"
    "s_waitcnt vmcnt(21)\n"
    "v_max3_u32 v7, v7, v176, v177\n"
    "v_max3_u32 v7, v7, v178, v179\n"
    "s_waitcnt vmcnt(20)\n"
    "v_max3_u32 v7, v7, v180, v181\n"
    "v_max3_u32 v7, v7, v182, v183\n"
    "s_waitcnt vmcnt(19)\n"
    "v_max3_u32 v7, v7, v184, v185\n"
    "v_max3_u32 v7, v7, v186, v187\n"
    "s_waitcnt vmcnt(18)\n"
    "v_max3_u32 v7, v7, v188, v189\n"
    "v_max3_u32 v7, v7, v190, v191\n"
    "s_waitcnt vmcnt(17)\n"
    "v_max3_u32 v7, v7, v100, v101\n"
    "v_max3_u32 v7, v7, v102, v103\n"
    "s_waitcnt vmcnt(16)\n"
    "v_max3_u32 v7, v7, v192, v193\n"
    "v_max3_u32 v7, v7, v194, v195\n"
    "s_waitcnt vmcnt(15)\n"
    "v_max3_u32 v7, v7, v196, v197\n"
    "v_max3_u32 v7, v7, v198, v199\n"
    "s_waitcnt vmcnt(14)\n"
    "v_max3_u32 v7, v7, v104, v105\n"
    "v_max3_u32 v7, v7, v106, v107\n"
    "s_waitcnt vmcnt(13)\n"
    "v_max3_u32 v7, v7, v200, v201\n"
    "v_max3_u32 v7, v7, v202, v203\n"
    "s_waitcnt vmcnt(12)\n"
    "v_max3_u32 v7, v7, v204, v205\n"
    "v_max3_u32 v7, v7, v206, v207\n"
    "s_waitcnt vmcnt(11)\n"
    "v_max3_u32 v7, v7, v208, v209\n"
    "v_max3_u32 v7, v7, v210, v211\n"
    "s_waitcnt vmcnt(10)\n"
    "v_max3_u32 v7, v7, v212, v213\n"
    "v_max3_u32 v7, v7, v214, v215\n"
    "s_waitcnt vmcnt(9)\n"
    "v_max3_u32 v7, v7, v216, v217\n"
    "v_max3_u32 v7, v7, v218, v219\n"
    "s_waitcnt vmcnt(8)\n"
    "v_max3_u32 v7, v7, v220, v221\n"
    "v_max3_u32 v7, v7, v222, v223\n"
    "s_waitcnt vmcnt(7)\n"
    "v_max3_u32 v7, v7, v224, v225\n"
    "v_max3_u32 v7, v7, v226, v227\n"
    "s_waitcnt vmcnt(6)\n"
    "v_max3_u32 v7, v7, v228, v229\n"
    "v_max3_u32 v7, v7, v230, v231\n"
    "s_waitcnt vmcnt(5)\n"
    "v_max3_u32 v7, v7, v232, v233\n"
    "v_max3_u32 v7, v7, v234, v235\n"
    "s_waitcnt vmcnt(4)\n"
    "v_max3_u32 v7, v7, v236, v237\n"
    "v_max3_u32 v7, v7, v238, v239\n"
    "s_waitcnt vmcnt(3)\n"
    "v_max3_u32 v7, v7, v240, v241\n"
    "v_max3_u32 v7, v7, v242, v243\n"
    "s_waitcnt vmcnt(2)\n"
    "v_max3_u32 v7, v7, v244, v245\n"
    "v_max3_u32 v7, v7, v246, v247\n"
    "s_waitcnt vmcnt(1)\n"
    "v_max3_u32 v7, v7, v248, v249\n"
    "v_max3_u32 v7, v7, v250, v251\n"
    "s_waitcnt vmcnt(0)\n"
    "v_max3_u32 v7, v7, v252, v253\n"
    "v_max3_u32 v7, v7, v254, v255\n"
    "v_cmp_eq_u32 vcc, -1, v7\n"
    "s_nop 0\n"
    "s_cbranch_vccz Lgo16\n"
    "s_cmp_eq_u32 s61, 0\n"
    "s_cbranch_scc1 Lgo16\n"
    "s_sub_u32 s61, s61, 1\n"
    "s_branch Lver16\n"
    "Lgo16:\n"
    "s_branch Lgq16\n"
    "Lgb18:\n"
    "s_cmp_eq_u32 s61, 0\n"
    "s_cbranch_scc1 Lgo18\n"
    "s_sub_u32 s61, s61, 1\n"
    "s_add_u32 s29, s80, 12193792\n"
    "buffer_load_dwordx4 v[128:131], v0, s[8:11], s29 offen offset:0 sc1\n"
    "s_add_u32 s29, s80, 12193792\n"
    "buffer_load_dwordx4 v[132:135], v0, s[8:11], s29 offen offset:1024 sc1\n"
    "s_waitcnt vmcnt(1)\n"
    "v_max3_u32 v7, v128, v129, v130\n"
    "v_max_u32 v7, v7, v131\n"
    "s_waitcnt vmcnt(0)\n"
    "v_max3_u32 v7, v7, v132, v133\n"
    "v_max3_u32 v7, v7, v134, v135\n"
    "v_cmp_eq_u32 vcc, -1, v7\n"
    "s_nop 0\n"
    "s_cbranch_vccnz Lgb18\n"
    "s_branch Lgo18\n"
    "Lgb19:\n"
    "s_cmp_eq_u32 s61, 0\n"
    "s_cbranch_scc1 Lgo19\n"
    "s_sub_u32 s61, s61, 1\n"
    "s_add_u32 s29, s80, 12193792\n"
    "buffer_load_dwordx4 v[136:139], v0, s[8:11], s29 offen offset:2048 sc1\n"
    "s_add_u32 s29, s80, 12193792\n"
    "buffer_load_dwordx4 v[140:143], v0, s[8:11], s29 offen offset:3072 sc1\n"
    "s_waitcnt vmcnt(1)\n"
    "v_max3_u32 v7, v136, v137, v138\n"
    "v_max_u32 v7, v7, v139\n"
    "s_waitcnt vmcnt(0)\n"
    "v_max3_u32 v7, v7, v140, v141\n"
    "v_max3_u32 v7, v7, v142, v143\n"
    "v_cmp_eq_u32 vcc, -1, v7\n"
    "s_nop 0\n"
    "s_cbranch_vccnz Lgb19\n"
    "s_branch Lgo19\n"
    "Lgb20:\n"
    "s_cmp_eq_u32 s61, 0\n"
    "s_cbranch_scc1 Lgo20\n"
    "s_sub_u32 s61, s61, 1\n"
    "s_add_u32 s29, s80, 12226560\n"
    "buffer_load_dwordx4 v[100:103], v0, s[8:11], s29 offen offset:0 sc1\n"
    "s_add_u32 s29, s80, 12226560\n"
    "buffer_load_dwordx4 v[104:107], v0, s[8:11], s29 offen offset:1024 sc1\n"
    "s_waitcnt vmcnt(1)\n"
    "v_max3_u32 v110, v100, v101, v102\n"
    "v_max_u32 v110, v110, v103\n"
    "s_waitcnt vmcnt(0)\n"
    "v_max3_u32 v110, v110, v104, v105\n"
    "v_max3_u32 v110, v110, v106, v107\n"
    "v_cmp_eq_u32 vcc, -1, v110\n"
    "s_nop 0\n"
    "s_cbranch_vccnz Lgb20\n"
    "s_branch Lgo20\n"
    "Lbad21:\n"
    "s_cmp_eq_u32 s28, 33\n"
    "s_cbranch_scc1 Lok21\n"
    "s_waitcnt vmcnt(0) lgkmcnt(0)\n"
    "s_nop 15\n"
    "s_branch Lag16\n"
    "Lgb25:\n"
    "s_cmp_eq_u32 s61, 0\n"
    "s_cbranch_scc1 Lgo25\n"
    "s_sub_u32 s61, s61, 1\n"
    "s_add_u32 s29, s87, 23080960\n"
    "buffer_load_dwordx4 v[176:179], v0, s[8:11], s29 offen offset:0 sc1\n"
    "s_add_u32 s29, s87, 23080960\n"
    "buffer_load_dwordx4 v[180:183], v0, s[8:11], s29 offen offset:1024 sc1\n"
    "s_add_u32 s29, s88, 23080960\n"
    "buffer_load_dwordx4 v[184:187], v0, s[8:11], s29 offen offset:0 sc1\n"
    "s_add_u32 s29, s88, 23080960\n"
    "buffer_load_dwordx4 v[188:191], v0, s[8:11], s29 offen offset:1024 sc1\n"
    "s_waitcnt vmcnt(3)\n"
    "v_max3_u32 v7, v176, v177, v178\n"
    "v_max_u32 v7, v7, v179\n"
    "s_waitcnt vmcnt(2)\n"
    "v_max3_u32 v7, v7, v180, v181\n"
    "v_max3_u32 v7, v7, v182, v183\n"
    "s_waitcnt vmcnt(1)\n"
    "v_max3_u32 v7, v7, v184, v185\n"
    "v_max3_u32 v7, v7, v186, v187\n"
    "s_waitcnt vmcnt(0)\n"
    "v_max3_u32 v7, v7, v188, v189\n"
    "v_max3_u32 v7, v7, v190, v191\n"
    "v_cmp_eq_u32 vcc, -1, v7\n"
    "s_nop 0\n"
    "s_cbranch_vccnz Lgb25\n"
    "s_branch Lgo25\n"
    "Lgb26:\n"
    "s_cmp_eq_u32 s61, 0\n"
    "s_cbranch_scc1 Lgo26\n"
    "s_sub_u32 s61, s61, 1\n"
    "s_add_u32 s29, s87, 23080960\n"
    "buffer_load_dwordx4 v[192:195], v0, s[8:11], s29 offen offset:2048 sc1\n"
    "s_add_u32 s29, s87, 23080960\n"
    "buffer_load_dwordx4 v[196:199], v0, s[8:11], s29 offen offset:3072 sc1\n"
    "s_add_u32 s29, s88, 23080960\n"
    "buffer_load_dwordx4 v[200:203], v0, s[8:11], s29 offen offset:2048 sc1\n"
    "s_add_u32 s29, s88, 23080960\n"
    "buffer_load_dwordx4 v[204:207], v0, s[8:11], s29 offen offset:3072 sc1\n"
    "s_waitcnt vmcnt(3)\n"
    "v_max3_u32 v7, v192, v193, v194\n"
    "v_max_u32 v7, v7, v195\n"
    "s_waitcnt vmcnt(2)\n"
    "v_max3_u32 v7, v7, v196, v197\n"
    "v_max3_u32 v7, v7, v198, v199\n"
    "s_waitcnt vmcnt(1)\n"
    "v_max3_u32 v7, v7, v200, v201\n"
    "v_max3_u32 v7, v7, v202, v203\n"
    "s_waitcnt vmcnt(0)\n"
    "v_max3_u32 v7, v7, v204, v205\n"
    "v_max3_u32 v7, v7, v206, v207\n"
    "v_cmp_eq_u32 vcc, -1, v7\n"
    "s_nop 0\n"
    "s_cbranch_vccnz Lgb26\n"
    "s_branch Lgo26\n"
    "Lgb27:\n"
    "s_cmp_eq_u32 s61, 0\n"
    "s_cbranch_scc1 Lgo27\n"
    "s_sub_u32 s61, s61, 1\n"
    "s_add_u32 s29, s87, 23085056\n"
    "buffer_load_dwordx4 v[48:51], v0, s[8:11], s29 offen offset:0 sc1\n"
    "s_add_u32 s29, s87, 23085056\n"
    "buffer_load_dwordx4 v[52:55], v0, s[8:11], s29 offen offset:1024 sc1\n"
    "s_add_u32 s29, s88, 23085056\n"
    "buffer_load_dwordx4 v[56:59], v0, s[8:11], s29 offen offset:0 sc1\n"
    "s_add_u32 s29, s88, 23085056\n"
    "buffer_load_dwordx4 v[60:63], v0, s[8:11], s29 offen offset:1024 sc1\n"
    "s_waitcnt vmcnt(3)\n"
    "v_max3_u32 v7, v48, v49, v50\n"
    "v_max_u32 v7, v7, v51\n"
    "s_waitcnt vmcnt(2)\n"
    "v_max3_u32 v7, v7, v52, v53\n"
    "v_max3_u32 v7, v7, v54, v55\n"
    "s_waitcnt vmcnt(1)\n"
    "v_max3_u32 v7, v7, v56, v57\n"
    "v_max3_u32 v7, v7, v58, v59\n"
    "s_waitcnt vmcnt(0)\n"
    "v_max3_u32 v7, v7, v60, v61\n"
    "v_max3_u32 v7, v7, v62, v63\n"
    "v_cmp_eq_u32 vcc, -1, v7\n"
    "s_nop 0\n"
    "s_cbranch_vccnz Lgb27\n"
    "s_branch Lgo27\n"
    "Lgb28:\n"
    "s_cmp_eq_u32 s61, 0\n"
    "s_cbranch_scc1 Lgo28\n"
    "s_sub_u32 s61, s61, 1\n"
    "s_add_u32 s29, s87, 23085056\n"
    "buffer_load_dwordx4 v[112:115], v0, s[8:11], s29 offen offset:3072 sc1\n"
    "s_add_u32 s29, s87, 23089152\n"
    "buffer_load_dwordx4 v[88:91], v0, s[8:11], s29 offen offset:1024 sc1\n"
    "s_add_u32 s29, s87, 23089152\n"
    "buffer_load_dwordx4 v[92:95], v0, s[8:11], s29 offen offset:3072 sc1\n"
    "s_waitcnt vmcnt(2)\n"
    "v_max3_u32 v7, v112, v113, v114\n"
    "v_max_u32 v7, v7, v115\n"
    "s_waitcnt vmcnt(1)\n"
    "v_max3_u32 v7, v7, v88, v89\n"
    "v_max3_u32 v7, v7, v90, v91\n"
    "s_waitcnt vmcnt(0)\n"
    "v_max3_u32 v7, v7, v92, v93\n"
    "v_max3_u32 v7, v7, v94, v95\n"
    "v_cmp_eq_u32 vcc, -1, v7\n"
    "s_nop 0\n"
    "s_cbranch_vccnz Lgb28\n"
    "s_branch Lgo28\n"
    "Lgb29:\n"
    "s_cmp_eq_u32 s61, 0\n"
    "s_cbranch_scc1 Lgo29\n"
    "s_sub_u32 s61, s61, 1\n"
    "s_add_u32 s29, s88, 23085056\n"
    "buffer_load_dwordx4 v[96:99], v0, s[8:11], s29 offen offset:2048 sc1\n"
    "s_add_u32 s29, s88, 23089152\n"
    "buffer_load_dwordx4 v[100:103], v0, s[8:11], s29 offen offset:0 sc1\n"
    "s_add_u32 s29, s88, 23089152\n"
    "buffer_load_dwordx4 v[108:111], v0, s[8:11], s29 offen offset:2048 sc1\n"
    "s_waitcnt vmcnt(2)\n"
    "v_max3_u32 v7, v96, v97, v98\n"
    "v_max_u32 v7, v7, v99\n"
    "s_waitcnt vmcnt(1)\n"
    "v_max3_u32 v7, v7, v100, v101\n"
    "v_max3_u32 v7, v7, v102, v103\n"
    "s_waitcnt vmcnt(0)\n"
    "v_max3_u32 v7, v7, v108, v109\n"
    "v_max3_u32 v7, v7, v110, v111\n"
    "v_cmp_eq_u32 vcc, -1, v7\n"
    "s_nop 0\n"
    "s_cbranch_vccnz Lgb29\n"
    "s_branch Lgo29\n"
    "Lgb30:\n"
    "s_cmp_eq_u32 s61, 0\n"
    "s_cbranch_scc1 Lgo30\n"
    "s_sub_u32 s61, s61, 1\n"
    "s_add_u32 s29, s88, 23085056\n"
    "buffer_load_dwordx4 v[116:119], v0, s[8:11], s29 offen offset:3072 sc1\n"
    "s_add_u32 s29, s88, 23089152\n"
    "buffer_load_dwordx4 v[120:123], v0, s[8:11], s29 offen offset:1024 sc1\n"
    "s_add_u32 s29, s88, 23089152\n"
    "buffer_load_dwordx4 v[124:127], v0, s[8:11], s29 offen offset:3072 sc1\n"
    "s_waitcnt vmcnt(2)\n"
    "v_max3_u32 v7, v116, v117, v118\n"
    "v_max_u32 v7, v7, v119\n"
    "s_waitcnt vmcnt(1)\n"
    "v_max3_u32 v7, v7, v120, v121\n"
    "v_max3_u32 v7, v7, v122, v123\n"
    "s_waitcnt vmcnt(0)\n"
    "v_max3_u32 v7, v7, v124, v125\n"
    "v_max3_u32 v7, v7, v126, v127\n"
    "v_cmp_eq_u32 vcc, -1, v7\n"
    "s_nop 0\n"
    "s_cbranch_vccnz Lgb30\n"
    "s_branch Lgo30\n"
    "Lgb31:\n"
    "s_cmp_eq_u32 s61, 0\n"
    "s_cbranch_scc1 Lgo31\n"
    "s_sub_u32 s61, s61, 1\n"
    "s_add_u32 s29, s87, 23085056\n"
    "buffer_load_dwordx4 v[80:83], v0, s[8:11], s29 offen offset:2048 sc1\n"
    "s_add_u32 s29, s87, 23089152\n"
    "buffer_load_dwordx4 v[84:87], v0, s[8:11], s29 offen offset:0 sc1\n"
    "s_add_u32 s29, s87, 23089152\n"
    "buffer_load_dwordx4 v[104:107], v0, s[8:11], s29 offen offset:2048 sc1\n"
    "s_waitcnt vmcnt(2)\n"
    "v_max3_u32 v7, v80, v81, v82\n"
    "v_max_u32 v7, v7, v83\n"
    "s_waitcnt vmcnt(1)\n"
    "v_max3_u32 v7, v7, v84, v85\n"
    "v_max3_u32 v7, v7, v86, v87\n"
    "s_waitcnt vmcnt(0)\n"
    "v_max3_u32 v7, v7, v104, v105\n"
    "v_max3_u32 v7, v7, v106, v107\n"
    "v_cmp_eq_u32 vcc, -1, v7\n"
    "s_nop 0\n"
    "s_cbranch_vccnz Lgb31\n"
    "s_branch Lgo31\n"
    "Lbad32:\n"
    "s_cmp_eq_u32 s28, 49\n"
    "s_cbranch_scc1 Lok32\n"
    "s_waitcnt vmcnt(0) lgkmcnt(0)\n"
    "s_nop 15\n"
    "Lag24:\n"
    "s_cmp_eq_u32 s28, 48\n"
    "s_mov_b32 s28, 48\n"
    "s_cbranch_scc1 Lver24\n"
    "s_add_u32 s71, s87, 23093248\n"
    "s_add_u32 s71, s71, s89\n"
    "buffer_load_dwordx4 v[16:19], v0, s[8:11], s71 offen offset:0 sc1\n"
    "buffer_load_dwordx4 v[20:23], v0, s[8:11], s71 offen offset:1024 sc1\n"
    "s_add_u32 s72, s88, 23093248\n"
    "s_add_u32 s72, s72, s89\n"
    "buffer_load_dwordx4 v[24:27], v0, s[8:11], s72 offen offset:0 sc1\n"
    "buffer_load_dwordx4 v[28:31], v0, s[8:11], s72 offen offset:1024 sc1\n"
    "s_add_u32 s71, s87, 23072768\n"
    "buffer_load_dwordx4 v[128:131], v0, s[8:11], s71 offen offset:0 sc1\n"
    "buffer_load_dwordx4 v[132:135], v0, s[8:11], s71 offen offset:1024 sc1\n"
    "s_add_u32 s72, s88, 23072768\n"
    "buffer_load_dwordx4 v[136:139], v0, s[8:11], s72 offen offset:0 sc1\n"
    "buffer_load_dwordx4 v[140:143], v0, s[8:11], s72 offen offset:1024 sc1\n"
    "buffer_load_dwordx4 v[64:67], v0, s[8:11], s71 offen offset:2048 sc1\n"
    "buffer_load_dwordx4 v[68:71], v0, s[8:11], s71 offen offset:3072 sc1\n"
    "buffer_load_dwordx4 v[72:75], v0, s[8:11], s72 offen offset:2048 sc1\n"
    "buffer_load_dwordx4 v[76:79], v0, s[8:11], s72 offen offset:3072 sc1\n"
    "s_add_u32 s71, s87, 23076864\n"
    "buffer_load_dwordx4 v[144:147], v0, s[8:11], s71 offen offset:0 sc1\n"
    "buffer_load_dwordx4 v[148:151], v0, s[8:11], s71 offen offset:1024 sc1\n"
    "s_add_u32 s72, s88, 23076864\n"
    "buffer_load_dwordx4 v[152:155], v0, s[8:11], s72 offen offset:0 sc1\n"
    "buffer_load_dwordx4 v[156:159], v0, s[8:11], s72 offen offset:1024 sc1\n"
    "buffer_load_dwordx4 v[160:163], v0, s[8:11], s71 offen offset:2048 sc1\n"
    "buffer_load_dwordx4 v[164:167], v0, s[8:11], s71 offen offset:3072 sc1\n"
    "Lgq24:\n"
    "s_waitcnt vmcnt(0) lgkmcnt(0)\n"
    "s_add_u32 s72, s88, 23076864\n"
    "s_add_u32 m0, s66, 36864\n"
    "s_add_u32 s69, s77, 135168\n"
    "s_add_u32 s71, s87, 23076864\n"
    "s_add_u32 s70, s79, 32768\n"
    "s_branch Lredo24\n"
    "Lver24:\n"
    "s_mov_b32 s28, 49\n"
    "s_add_u32 s71, s87, 23093248\n"
    "s_add_u32 s71, s71, s89\n"
    "buffer_load_dwordx4 v[16:19], v0, s[8:11], s71 offen offset:0 sc1\n"
    "buffer_load_dwordx4 v[20:23], v0, s[8:11], s71 offen offset:1024 sc1\n"
    "s_add_u32 s72, s88, 23093248\n"
    "s_add_u32 s72, s72, s89\n"
    "buffer_load_dwordx4 v[24:27], v0, s[8:11], s72 offen offset:0 sc1\n"
    "buffer_load_dwordx4 v[28:31], v0, s[8:11], s72 offen offset:1024 sc1\n"
    "s_add_u32 s71, s87, 23072768\n"
    "buffer_load_dwordx4 v[128:131], v0, s[8:11], s71 offen offset:0 sc1\n"
    "buffer_load_dwordx4 v[132:135], v0, s[8:11], s71 offen offset:1024 sc1\n"
    "s_add_u32 s72, s88, 23072768\n"
    "buffer_load_dwordx4 v[136:139], v0, s[8:11], s72 offen offset:0 sc1\n"
    "buffer_load_dwordx4 v[140:143], v0, s[8:11], s72 offen offset:1024 sc1\n"
    "buffer_load_dwordx4 v[64:67], v0, s[8:11], s71 offen offset:2048 sc1\n"
    "buffer_load_dwordx4 v[68:71], v0, s[8:11], s71 offen offset:3072 sc1\n"
    "buffer_load_dwordx4 v[72:75], v0, s[8:11], s72 offen offset:2048 sc1\n"
    "buffer_load_dwordx4 v[76:79], v0, s[8:11], s72 offen offset:3072 sc1\n"
    "s_add_u32 s71, s87, 23076864\n"
    "buffer_load_dwordx4 v[144:147], v0, s[8:11], s71 offen offset:0 sc1\n"
    "buffer_load_dwordx4 v[148:151], v0, s[8:11], s71 offen offset:1024 sc1\n"
    "s_add_u32 s72, s88, 23076864\n"
    "buffer_load_dwordx4 v[152:155], v0, s[8:11], s72 offen offset:0 sc1\n"
    "buffer_load_dwordx4 v[156:159], v0, s[8:11], s72 offen offset:1024 sc1\n"
    "buffer_load_dwordx4 v[160:163], v0, s[8:11], s71 offen offset:2048 sc1\n"
    "buffer_load_dwordx4 v[164:167], v0, s[8:11], s71 offen offset:3072 sc1\n"
    "buffer_load_dwordx4 v[168:171], v0, s[8:11], s72 offen offset:2048 sc1\n"
    "buffer_load_dwordx4 v[172:175], v0, s[8:11], s72 offen offset:3072 sc1\n"
    "s_add_u32 s71, s87, 23080960\n"
    "buffer_load_dwordx4 v[176:179], v0, s[8:11], s71 offen offset:0 sc1\n"
    "buffer_load_dwordx4 v[180:183], v0, s[8:11], s71 offen offset:1024 sc1\n"
    "s_add_u32 s72, s88, 23080960\n"
    "buffer_load_dwordx4 v[184:187], v0, s[8:11], s72 offen offset:0 sc1\n"
    "buffer_load_dwordx4 v[188:191], v0, s[8:11], s72 offen offset:1024 sc1\n"
    "buffer_load_dwordx4 v[192:195], v0, s[8:11], s71 offen offset:2048 sc1\n"
    "buffer_load_dwordx4 v[196:199], v0, s[8:11], s71 offen offset:3072 sc1\n"
    "buffer_load_dwordx4 v[200:203], v0, s[8:11], s72 offen offset:2048 sc1\n"
    "buffer_load_dwordx4 v[204:207], v0, s[8:11], s72 offen offset:3072 sc1\n"
    "s_add_u32 s71, s87, 23085056\n"
    "buffer_load_dwordx4 v[48:51], v0, s[8:11], s71 offen offset:0 sc1\n"
    "buffer_load_dwordx4 v[52:55], v0, s[8:11], s71 offen offset:1024 sc1\n"
    "s_add_u32 s72, s88, 23085056\n"
    "buffer_load_dwordx4 v[56:59], v0, s[8:11], s72 offen offset:0 sc1\n"
    "buffer_load_dwordx4 v[60:63], v0, s[8:11], s72 offen offset:1024 sc1\n"
    "buffer_load_dwordx4 v[112:115], v0, s[8:11], s71 offen offset:3072 sc1\n"
    "s_add_u32 s71, s87, 23089152\n"
    "buffer_load_dwordx4 v[88:91], v0, s[8:11], s71 offen offset:1024 sc1\n"
    "buffer_load_dwordx4 v[92:95], v0, s[8:11], s71 offen offset:3072 sc1\n"
    "buffer_load_dwordx4 v[96:99], v0, s[8:11], s72 offen offset:2048 sc1\n"
    "s_add_u32 s72, s88, 23089152\n"
    "buffer_load_dwordx4 v[100:103], v0, s[8:11], s72 offen offset:0 sc1\n"
    "buffer_load_dwordx4 v[108:111], v0, s[8:11], s72 offen offset:2048 sc1\n"
    "s_add_u32 s72, s88, 23085056\n"
    "buffer_load_dwordx4 v[116:119], v0, s[8:11], s72 offen offset:3072 sc1\n"
    "s_add_u32 s72, s88, 23089152\n"
    "buffer_load_dwordx4 v[120:123], v0, s[8:11], s72 offen offset:1024 sc1\n"
    "buffer_load_dwordx4 v[124:127], v0, s[8:11], s72 offen offset:3072 sc1\n"
    "s_add_u32 s71, s87, 23085056\n"
    "buffer_load_dwordx4 v[80:83], v0, s[8:11], s71 offen offset:2048 sc1\n"
    "s_add_u32 s71, s87, 23089152\n"
    "buffer_load_dwordx4 v[84:87], v0, s[8:11], s71 offen offset:0 sc1\n"
    "buffer_load_dwordx4 v[104:107], v0, s[8:11], s71 offen offset:2048 sc1\n"
    "s_waitcnt vmcnt(43)\n"
    "v_max3_u32 v7, v16, v17, v18\n"
    "v_max_u32 v7, v7, v19\n"
    "s_waitcnt vmcnt(42)\n"
    "v_max3_u32 v7, v7, v20, v21\n"
    "v_max3_u32 v7, v7, v22, v23\n"
    "s_waitcnt vmcnt(41)\n"
    "v_max3_u32 v7, v7, v24, v25\n"
    "v_max3_u32 v7, v7, v26, v27\n"
    "s_waitcnt vmcnt(40)\n"
    "v_max3_u32 v7, v7, v28, v29\n"
    "v_max3_u32 v7, v7, v30, v31\n"
    "s_waitcnt vmcnt(39)\n"
    "v_max3_u32 v7, v7, v128, v129\n"
    "v_max3_u32 v7, v7, v130, v131\n"
    "s_waitcnt vmcnt(38)\n"
    "v_max3_u32 v7, v7, v132, v133\n"
    "v_max3_u32 v7, v7, v134, v135\n"
    "s_waitcnt vmcnt(37)\n"
    "v_max3_u32 v7, v7, v136, v137\n"
    "v_max3_u32 v7, v7, v138, v139\n"
    "s_waitcnt vmcnt(36)\n"
    "v_max3_u32 v7, v7, v140, v141\n"
    "v_max3_u32 v7, v7, v142, v143\n"
    "s_waitcnt vmcnt(35)\n"
    "v_max3_u32 v7, v7, v64, v65\n"
    "v_max3_u32 v7, v7, v66, v67\n"
    "s_waitcnt vmcnt(34)\n"
    "v_max3_u32 v7, v7, v68, v69\n"
    "v_max3_u32 v7, v7, v70, v71\n"
    "s_waitcnt vmcnt(33)\n"
    "v_max3_u32 v7, v7, v72, v73\n"
    "v_max3_u32 v7, v7, v74, v75\n"
    "s_waitcnt vmcnt(32)\n"
    "v_max3_u32 v7, v7, v76, v77\n"
    "v_max3_u32 v7, v7, v78, v79\n"
    "s_waitcnt vmcnt(31)\n"
    "v_max3_u32 v7, v7, v144, v145\n"
    "v_max3_u32 v7, v7, v146, v147\n"
    "s_waitcnt vmcnt(30)\n"
    "v_max3_u32 v7, v7, v148, v149\n"
    "v_max3_u32 v7, v7, v150, v151\n"
    "s_waitcnt vmcnt(29)\n"
    "v_max3_u32 v7, v7, v152, v153\n"
    "v_max3_u32 v7, v7, v154, v155\n"
    "s_waitcnt vmcnt(28)\n"
    "v_max3_u32 v7, v7, v156, v157\n"
    "v_max3_u32 v7, v7, v158, v159\n"
    "s_waitcnt vmcnt(27)\n"
    "v_max3_u32 v7, v7, v160, v161\n"
    "v_max3_u32 v7, v7, v162, v163\n"
    "s_waitcnt vmcnt(26)\n"
    "v_max3_u32 v7, v7, v164, v165\n"
    "v_max3_u32 v7, v7, v166, v167\n"
    "s_waitcnt vmcnt(25)\n"
    "v_max3_u32 v7, v7, v168, v169\n"
    "v_max3_u32 v7, v7, v170, v171\n"
    "s_waitcnt vmcnt(24)\n"
    "v_max3_u32 v7, v7, v172, v173\n"
    "v_max3_u32 v7, v7, v174, v175\n"
    "s_waitcnt vmcnt(23)\n"
    "v_max3_u32 v7, v7, v176, v177\n"
    "v_max3_u32 v7, v7, v178, v179\n"
    "s_waitcnt vmcnt(22)\n"
    "v_max3_u32 v7, v7, v180, v181\n"
    "v_max3_u32 v7, v7, v182, v183\n"
    "s_waitcnt vmcnt(21)\n"
    "v_max3_u32 v7, v7, v184, v185\n"
    "v_max3_u32 v7, v7, v186, v187\n"
    "s_waitcnt vmcnt(20)\n"
    "v_max3_u32 v7, v7, v188, v189\n"
    "v_max3_u32 v7, v7, v190, v191\n"
    "s_waitcnt vmcnt(19)\n"
    "v_max3_u32 v7, v7, v192, v193\n"
    "v_max3_u32 v7, v7, v194, v195\n"
    "s_waitcnt vmcnt(18)\n"
    "v_max3_u32 v7, v7, v196, v197\n"
    "v_max3_u32 v7, v7, v198, v199\n"
    "s_waitcnt vmcnt(17)\n"
    "v_max3_u32 v7, v7, v200, v201\n"
    "v_max3_u32 v7, v7, v202, v203\n"
    "s_waitcnt vmcnt(16)\n"
    "v_max3_u32 v7, v7, v204, v205\n"
    "v_max3_u32 v7, v7, v206, v207\n"
    "s_waitcnt vmcnt(15)\n"
    "v_max3_u32 v7, v7, v48, v49\n"
    "v_max3_u32 v7, v7, v50, v51\n"
    "s_waitcnt vmcnt(14)\n"
    "v_max3_u32 v7, v7, v52, v53\n"
    "v_max3_u32 v7, v7, v54, v55\n"
    "s_waitcnt vmcnt(13)\n"
    "v_max3_u32 v7, v7, v56, v57\n"
    "v_max3_u32 v7, v7, v58, v59\n"
    "s_waitcnt vmcnt(12)\n"
    "v_max3_u32 v7, v7, v60, v61\n"
    "v_max3_u32 v7, v7, v62, v63\n"
    "s_waitcnt vmcnt(11)\n"
    "v_max3_u32 v7, v7, v112, v113\n"
    "v_max3_u32 v7, v7, v114, v115\n"
    "s_waitcnt vmcnt(10)\n"
    "v_max3_u32 v7, v7, v88, v89\n"
    "v_max3_u32 v7, v7, v90, v91\n"
    "s_waitcnt vmcnt(9)\n"
    "v_max3_u32 v7, v7, v92, v93\n"
    "v_max3_u32 v7, v7, v94, v95\n"
    "s_waitcnt vmcnt(8)\n"
    "v_max3_u32 v7, v7, v96, v97\n"
    "v_max3_u32 v7, v7, v98, v99\n"
    "s_waitcnt vmcnt(7)\n"
    "v_max3_u32 v7, v7, v100, v101\n"
    "v_max3_u32 v7, v7, v102, v103\n"
    "s_waitcnt vmcnt(6)\n"
    "v_max3_u32 v7, v7, v108, v109\n"
    "v_max3_u32 v7, v7, v110, v111\n"
    "s_waitcnt vmcnt(5)\n"
    "v_max3_u32 v7, v7, v116, v117\n"
    "v_max3_u32 v7, v7, v118, v119\n"
    "s_waitcnt vmcnt(4)\n"
    "v_max3_u32 v7, v7, v120, v121\n"
    "v_max3_u32 v7, v7, v122, v123\n"
    "s_waitcnt vmcnt(3)\n"
    "v_max3_u32 v7, v7, v124, v125\n"
    "v_max3_u32 v7, v7, v126, v127\n"
    "s_waitcnt vmcnt(2)\n"
    "v_max3_u32 v7, v7, v80, v81\n"
    "v_max3_u32 v7, v7, v82, v83\n"
    "s_waitcnt vmcnt(1)\n"
    "v_max3_u32 v7, v7, v84, v85\n"
    "v_max3_u32 v7, v7, v86, v87\n"
    "s_waitcnt vmcnt(0)\n"
    "v_max3_u32 v7, v7, v104, v105\n"
    "v_max3_u32 v7, v7, v106, v107\n"
    "v_cmp_eq_u32 vcc, -1, v7\n"
    "s_nop 0\n"
    "s_cbranch_vccz Lgo24\n"
    "s_cmp_eq_u32 s61, 0\n"
    "s_cbranch_scc1 Lgo24\n"
    "s_sub_u32 s61, s61, 1\n"
    "s_branch Lver24\n"
    "Lgo24:\n"
    "s_branch Lgq24\n"
    "Lbad38:\n"
    "s_cmp_eq_u32 s28, 75\n"
    "s_cbranch_scc1 Lok38\n"
    "s_waitcnt vmcnt(0) lgkmcnt(0)\n"
    "s_nop 15\n"
    "Lag37:\n"
    "s_cmp_eq_u32 s28, 74\n"
    "s_mov_b32 s28, 74\n"
    "s_cbranch_scc1 Lver37\n"
    "s_add_u32 s71, s80, 14422016\n"
    "buffer_load_dwordx4 v[128:131], v0, s[8:11], s71 offen offset:0 sc1\n"
    "buffer_load_dwordx4 v[132:135], v0, s[8:11], s71 offen offset:1024 sc1\n"
    "buffer_load_dwordx4 v[136:139], v0, s[8:11], s71 offen offset:2048 sc1\n"
    "buffer_load_dwordx4 v[140:143], v0, s[8:11], s71 offen offset:3072 sc1\n"
    "s_add_u32 s71, s80, 14426112\n"
    "buffer_load_dwordx4 v[144:147], v0, s[8:11], s71 offen offset:0 sc1\n"
    "buffer_load_dwordx4 v[148:151], v0, s[8:11], s71 offen offset:1024 sc1\n"
    "buffer_load_dwordx4 v[152:155], v0, s[8:11], s71 offen offset:2048 sc1\n"
    "buffer_load_dwordx4 v[156:159], v0, s[8:11], s71 offen offset:3072 sc1\n"
    "s_add_u32 s71, s80, 14430208\n"
    "buffer_load_dwordx4 v[160:163], v0, s[8:11], s71 offen offset:0 sc1\n"
    "buffer_load_dwordx4 v[164:167], v0, s[8:11], s71 offen offset:1024 sc1\n"
    "Lgq37:\n"
    "s_waitcnt vmcnt(0) lgkmcnt(0)\n"
    "s_add_u32 s72, s88, 23089152\n"
    "s_add_u32 m0, s66, 36864\n"
    "s_add_u32 s69, s75, 36864\n"
    "s_add_u32 s71, s80, 14430208\n"
    "s_add_u32 s70, s79, 36864\n"
    "s_branch Lredo37\n"
    "Lver37:\n"
    "s_mov_b32 s28, 75\n"
    "s_add_u32 s71, s80, 14422016\n"
    "buffer_load_dwordx4 v[128:131], v0, s[8:11], s71 offen offset:0 sc1\n"
    "buffer_load_dwordx4 v[132:135], v0, s[8:11], s71 offen offset:1024 sc1\n"
    "buffer_load_dwordx4 v[136:139], v0, s[8:11], s71 offen offset:2048 sc1\n"
    "buffer_load_dwordx4 v[140:143], v0, s[8:11], s71 offen offset:3072 sc1\n"
    "s_add_u32 s71, s80, 14426112\n"
    "buffer_load_dwordx4 v[144:147], v0, s[8:11], s71 offen offset:0 sc1\n"
    "buffer_load_dwordx4 v[148:151], v0, s[8:11], s71 offen offset:1024 sc1\n"
    "buffer_load_dwordx4 v[152:155], v0, s[8:11], s71 offen offset:2048 sc1\n"
    "buffer_load_dwordx4 v[156:159], v0, s[8:11], s71 offen offset:3072 sc1\n"
    "s_add_u32 s71, s80, 14430208\n"
    "buffer_load_dwordx4 v[160:163], v0, s[8:11], s71 offen offset:0 sc1\n"
    "buffer_load_dwordx4 v[164:167], v0, s[8:11], s71 offen offset:1024 sc1\n"
    "buffer_load_dwordx4 v[168:171], v0, s[8:11], s71 offen offset:2048 sc1\n"
    "buffer_load_dwordx4 v[172:175], v0, s[8:11], s71 offen offset:3072 sc1\n"
    "s_add_u32 s71, s80, 14434304\n"
    "buffer_load_dwordx4 v[176:179], v0, s[8:11], s71 offen offset:0 sc1\n"
    "buffer_load_dwordx4 v[180:183], v0, s[8:11], s71 offen offset:1024 sc1\n"
    "buffer_load_dwordx4 v[184:187], v0, s[8:11], s71 offen offset:2048 sc1\n"
    "buffer_load_dwordx4 v[188:191], v0, s[8:11], s71 offen offset:3072 sc1\n"
    "s_add_u32 s72, s80, 14454784\n"
    "buffer_load_dwordx4 v[100:103], v0, s[8:11], s72 offen offset:0 sc1\n"
    "s_add_u32 s71, s80, 14438400\n"
    "buffer_load_dwordx4 v[192:195], v0, s[8:11], s71 offen offset:0 sc1\n"
    "buffer_load_dwordx4 v[196:199], v0, s[8:11], s71 offen offset:1024 sc1\n"
    "buffer_load_dwordx4 v[104:107], v0, s[8:11], s72 offen offset:1024 sc1\n"
    "buffer_load_dwordx4 v[200:203], v0, s[8:11], s71 offen offset:2048 sc1\n"
    "buffer_load_dwordx4 v[204:207], v0, s[8:11], s71 offen offset:3072 sc1\n"
    "s_add_u32 s71, s80, 14442496\n"
    "buffer_load_dwordx4 v[208:211], v0, s[8:11], s71 offen offset:0 sc1\n"
    "buffer_load_dwordx4 v[212:215], v0, s[8:11], s71 offen offset:1024 sc1\n"
    "buffer_load_dwordx4 v[216:219], v0, s[8:11], s71 offen offset:2048 sc1\n"
    "buffer_load_dwordx4 v[220:223], v0, s[8:11], s71 offen offset:3072 sc1\n"
    "s_add_u32 s71, s80, 14446592\n"
    "buffer_load_dwordx4 v[224:227], v0, s[8:11], s71 offen offset:0 sc1\n"
    "buffer_load_dwordx4 v[228:231], v0, s[8:11], s71 offen offset:1024 sc1\n"
    "buffer_load_dwordx4 v[232:235], v0, s[8:11], s71 offen offset:2048 sc1\n"
    "buffer_load_dwordx4 v[236:239], v0, s[8:11], s71 offen offset:3072 sc1\n"
    "s_add_u32 s71, s80, 14450688\n"
    "buffer_load_dwordx4 v[240:243], v0, s[8:11], s71 offen offset:0 sc1\n"
    "buffer_load_dwordx4 v[244:247], v0, s[8:11], s71 offen offset:1024 sc1\n"
    "buffer_load_dwordx4 v[248:251], v0, s[8:11], s71 offen offset:2048 sc1\n"
    "buffer_load_dwordx4 v[252:255], v0, s[8:11], s71 offen offset:3072 sc1\n"
    "s_waitcnt vmcnt(33)\n"
    "v_max3_u32 v7, v128, v129, v130\n"
    "v_max_u32 v7, v7, v131\n"
    "s_waitcnt vmcnt(32)\n"
    "v_max3_u32 v7, v7, v132, v133\n"
    "v_max3_u32 v7, v7, v134, v135\n"
    "s_waitcnt vmcnt(31)\n"
    "v_max3_u32 v7, v7, v136, v137\n"
    "v_max3_u32 v7, v7, v138, v139\n"
    "s_waitcnt vmcnt(30)\n"
    "v_max3_u32 v7, v7, v140, v141\n"
    "v_max3_u32 v7, v7, v142, v143\n"
    "s_waitcnt vmcnt(29)\n"
    "v_max3_u32 v7, v7, v144, v145\n"
    "v_max3_u32 v7, v7, v146, v147\n"
    "s_waitcnt vmcnt(28)\n"
    "v_max3_u32 v7, v7, v148, v149\n"
    "v_max3_u32 v7, v7, v150, v151\n"
    "s_waitcnt vmcnt(27)\n"
    "v_max3_u32 v7, v7, v152, v153\n"
    "v_max3_u32 v7, v7, v154, v155\n"
    "s_waitcnt vmcnt(26)\n"
    "v_max3_u32 v7, v7, v156, v157\n"
    "v_max3_u32 v7, v7, v158, v159\n"
    "s_waitcnt vmcnt(25)\n"
    "v_max3_u32 v7, v7, v160, v161\n"
    "v_max3_u32 v7, v7, v162, v163\n"
    "s_waitcnt vmcnt(24)\n"
    "v_max3_u32 v7, v7, v164, v165\n"
    "v_max3_u32 v7, v7, v166, v167\n"
    "s_waitcnt vmcnt(23)\n"
    "v_max3_u32 v7, v7, v168, v169\n"
    "v_max3_u32 v7, v7, v170, v171\n"
    "s_waitcnt vmcnt(22)\n"
    "v_max3_u32 v7, v7, v172, v173\n"
    "v_max3_u32 v7, v7, v174, v175\n"
    "s_waitcnt vmcnt(21)\n"
    "v_max3_u32 v7, v7, v176, v177\n"
    "v_max3_u32 v7, v7, v178, v179\n"
    "s_waitcnt vmcnt(20)\n"
    "v_max3_u32 v7, v7, v180, v181\n"
    "v_max3_u32 v7, v7, v182, v183\n"
    "s_waitcnt vmcnt(19)\n"
    "v_max3_u32 v7, v7, v184, v185\n"
    "v_max3_u32 v7, v7, v186, v187\n"
    "s_waitcnt vmcnt(18)\n"
    "v_max3_u32 v7, v7, v188, v189\n"
    "v_max3_u32 v7, v7, v190, v191\n"
    "s_waitcnt vmcnt(17)\n"
    "v_max3_u32 v7, v7, v100, v101\n"
    "v_max3_u32 v7, v7, v102, v103\n"
    "s_waitcnt vmcnt(16)\n"
    "v_max3_u32 v7, v7, v192, v193\n"
    "v_max3_u32 v7, v7, v194, v195\n"
    "s_waitcnt vmcnt(15)\n"
    "v_max3_u32 v7, v7, v196, v197\n"
    "v_max3_u32 v7, v7, v198, v199\n"
    "s_waitcnt vmcnt(14)\n"
    "v_max3_u32 v7, v7, v104, v105\n"
    "v_max3_u32 v7, v7, v106, v107\n"
    "s_waitcnt vmcnt(13)\n"
    "v_max3_u32 v7, v7, v200, v201\n"
    "v_max3_u32 v7, v7, v202, v203\n"
    "s_waitcnt vmcnt(12)\n"
    "v_max3_u32 v7, v7, v204, v205\n"
    "v_max3_u32 v7, v7, v206, v207\n"
    "s_waitcnt vmcnt(11)\n"
    "v_max3_u32 v7, v7, v208, v209\n"
    "v_max3_u32 v7, v7, v210, v211\n"
    "s_waitcnt vmcnt(10)\n"
    "v_max3_u32 v7, v7, v212, v213\n"
    "v_max3_u32 v7, v7, v214, v215\n"
    "s_waitcnt vmcnt(9)\n"
    "v_max3_u32 v7, v7, v216, v217\n"
    "v_max3_u32 v7, v7, v218, v219\n"
    "s_waitcnt vmcnt(8)\n"
    "v_max3_u32 v7, v7, v220, v221\n"
    "v_max3_u32 v7, v7, v222, v223\n"
    "s_waitcnt vmcnt(7)\n"
    "v_max3_u32 v7, v7, v224, v225\n"
    "v_max3_u32 v7, v7, v226, v227\n"
    "s_waitcnt vmcnt(6)\n"
    "v_max3_u32 v7, v7, v228, v229\n"
    "v_max3_u32 v7, v7, v230, v231\n"
    "s_waitcnt vmcnt(5)\n"
    "v_max3_u32 v7, v7, v232, v233\n"
    "v_max3_u32 v7, v7, v234, v235\n"
    "s_waitcnt vmcnt(4)\n"
    "v_max3_u32 v7, v7, v236, v237\n"
    "v_max3_u32 v7, v7, v238, v239\n"
    "s_waitcnt vmcnt(3)\n"
    "v_max3_u32 v7, v7, v240, v241\n"
    "v_max3_u32 v7, v7, v242, v243\n"
    "s_waitcnt vmcnt(2)\n"
    "v_max3_u32 v7, v7, v244, v245\n"
    "v_max3_u32 v7, v7, v246, v247\n"
    "s_waitcnt vmcnt(1)\n"
    "v_max3_u32 v7, v7, v248, v249\n"
    "v_max3_u32 v7, v7, v250, v251\n"
    "s_waitcnt vmcnt(0)\n"
    "v_max3_u32 v7, v7, v252, v253\n"
    "v_max3_u32 v7, v7, v254, v255\n"
    "v_cmp_eq_u32 vcc, -1, v7\n"
    "s_nop 0\n"
    "s_cbranch_vccz Lgo37\n"
    "s_cmp_eq_u32 s61, 0\n"
    "s_cbranch_scc1 Lgo37\n"
    "s_sub_u32 s61, s61, 1\n"
    "s_branch Lver37\n"
    "Lgo37:\n"
    "s_branch Lgq37\n"
    "Lgb39:\n"
    "s_cmp_eq_u32 s61, 0\n"
    "s_cbranch_scc1 Lgo39\n"
    "s_sub_u32 s61, s61, 1\n"
    "s_add_u32 s29, s80, 14422016\n"
    "buffer_load_dwordx4 v[128:131], v0, s[8:11], s29 offen offset:0 sc1\n"
    "s_add_u32 s29, s80, 14422016\n"
    "buffer_load_dwordx4 v[132:135], v0, s[8:11], s29 offen offset:1024 sc1\n"
    "s_waitcnt vmcnt(1)\n"
    "v_max3_u32 v7, v128, v129, v130\n"
    "v_max_u32 v7, v7, v131\n"
    "s_waitcnt vmcnt(0)\n"
    "v_max3_u32 v7, v7, v132, v133\n"
    "v_max3_u32 v7, v7, v134, v135\n"
    "v_cmp_eq_u32 vcc, -1, v7\n"
    "s_nop 0\n"
    "s_cbranch_vccnz Lgb39\n"
    "s_branch Lgo39\n"
    "Lgb40:\n"
    "s_cmp_eq_u32 s61, 0\n"
    "s_cbranch_scc1 Lgo40\n"
    "s_sub_u32 s61, s61, 1\n"
    "s_add_u32 s29, s80, 14422016\n"
    "buffer_load_dwordx4 v[136:139], v0, s[8:11], s29 offen offset:2048 sc1\n"
    "s_add_u32 s29, s80, 14422016\n"
    "buffer_load_dwordx4 v[140:143], v0, s[8:11], s29 offen offset:3072 sc1\n"
    "s_waitcnt vmcnt(1)\n"
    "v_max3_u32 v7, v136, v137, v138\n"
    "v_max_u32 v7, v7, v139\n"
    "s_waitcnt vmcnt(0)\n"
    "v_max3_u32 v7, v7, v140, v141\n"
    "v_max3_u32 v7, v7, v142, v143\n"
    "v_cmp_eq_u32 vcc, -1, v7\n"
    "s_nop 0\n"
    "s_cbranch_vccnz Lgb40\n"
    "s_branch Lgo40\n"
    "Lgb41:\n"
    "s_cmp_eq_u32 s61, 0\n"
    "s_cbranch_scc1 Lgo41\n"
    "s_sub_u32 s61, s61, 1\n"
    "s_add_u32 s29, s80, 14454784\n"
    "buffer_load_dwordx4 v[100:103], v0, s[8:11], s29 offen offset:0 sc1\n"
    "s_add_u32 s29, s80, 14454784\n"
    "buffer_load_dwordx4 v[104:107], v0, s[8:11], s29 offen offset:1024 sc1\n"
    "s_waitcnt vmcnt(1)\n"
    "v_max3_u32 v110, v100, v101, v102\n"
    "v_max_u32 v110, v110, v103\n"
    "s_waitcnt vmcnt(0)\n"
    "v_max3_u32 v110, v110, v104, v105\n"
    "v_max3_u32 v110, v110, v106, v107\n"
    "v_cmp_eq_u32 vcc, -1, v110\n"
    "s_nop 0\n"
    "s_cbranch_vccnz Lgb41\n"
    "s_branch Lgo41\n"
    "Lbad42:\n"
    "s_cmp_eq_u32 s28, 75\n"
    "s_cbranch_scc1 Lok42\n"
    "s_waitcnt vmcnt(0) lgkmcnt(0)\n"
    "s_nop 15\n"
    "s_branch Lag37\n"
    "Lbad46:\n"
    "s_cmp_eq_u32 s28, 91\n"
    "s_cbranch_scc1 Lok46\n"
    "s_waitcnt vmcnt(0) lgkmcnt(0)\n"
    "s_nop 15\n"
    "Lag45:\n"
    "s_cmp_eq_u32 s28, 90\n"
    "s_mov_b32 s28, 90\n"
    "s_cbranch_scc1 Lver45\n"
    "s_add_u32 s72, s64, 1839104\n"
    "buffer_load_dwordx4 v[160:163], v14, s[8:11], s72 offen sc1\n"
    "s_add_u32 s71, s90, 1183744\n"
    "buffer_load_dwordx4 v[128:131], v0, s[8:11], s71 offen offset:0 sc1\n"
    "buffer_load_dwordx4 v[132:135], v0, s[8:11], s71 offen offset:1024 sc1\n"
    "buffer_load_dwordx4 v[136:139], v0, s[8:11], s71 offen offset:2048 sc1\n"
    "buffer_load_dwordx4 v[140:143], v0, s[8:11], s71 offen offset:3072 sc1\n"
    "s_add_u32 s71, s90, 1187840\n"
    "buffer_load_dwordx4 v[144:147], v0, s[8:11], s71 offen offset:0 sc1\n"
    "buffer_load_dwordx4 v[148:151], v0, s[8:11], s71 offen offset:1024 sc1\n"
    "buffer_load_dwordx4 v[152:155], v0, s[8:11], s71 offen offset:2048 sc1\n"
    "buffer_load_dwordx4 v[156:159], v0, s[8:11], s71 offen offset:3072 sc1\n"
    "Lgq45:\n"
    "s_waitcnt vmcnt(0) lgkmcnt(0)\n"
    "s_add_u32 s72, s64, 1839104\n"
    "s_add_u32 m0, s66, 36864\n"
    "s_add_u32 s69, s77, 4096\n"
    "s_add_u32 s71, s90, 1187840\n"
    "s_add_u32 s70, s79, 36864\n"
    "s_branch Lredo45\n"
    "Lver45:\n"
    "s_mov_b32 s28, 91\n"
    "s_add_u32 s72, s64, 1839104\n"
    "buffer_load_dwordx4 v[160:163], v14, s[8:11], s72 offen sc1\n"
    "s_add_u32 s71, s90, 1183744\n"
    "buffer_load_dwordx4 v[128:131], v0, s[8:11], s71 offen offset:0 sc1\n"
    "buffer_load_dwordx4 v[132:135], v0, s[8:11], s71 offen offset:1024 sc1\n"
    "buffer_load_dwordx4 v[136:139], v0, s[8:11], s71 offen offset:2048 sc1\n"
    "buffer_load_dwordx4 v[140:143], v0, s[8:11], s71 offen offset:3072 sc1\n"
    "s_add_u32 s71, s90, 1187840\n"
    "buffer_load_dwordx4 v[144:147], v0, s[8:11], s71 offen offset:0 sc1\n"
    "buffer_load_dwordx4 v[148:151], v0, s[8:11], s71 offen offset:1024 sc1\n"
    "buffer_load_dwordx4 v[152:155], v0, s[8:11], s71 offen offset:2048 sc1\n"
    "buffer_load_dwordx4 v[156:159], v0, s[8:11], s71 offen offset:3072 sc1\n"
    "s_waitcnt vmcnt(8)\n"
    "v_max3_u32 v7, v160, v161, v162\n"
    "v_max_u32 v7, v7, v163\n"
    "s_waitcnt vmcnt(7)\n"
    "v_max3_u32 v7, v7, v128, v129\n"
    "v_max3_u32 v7, v7, v130, v131\n"
    "s_waitcnt vmcnt(6)\n"
    "v_max3_u32 v7, v7, v132, v133\n"
    "v_max3_u32 v7, v7, v134, v135\n"
    "s_waitcnt vmcnt(5)\n"
    "v_max3_u32 v7, v7, v136, v137\n"
    "v_max3_u32 v7, v7, v138, v139\n"
    "s_waitcnt vmcnt(4)\n"
    "v_max3_u32 v7, v7, v140, v141\n"
    "v_max3_u32 v7, v7, v142, v143\n"
    "s_waitcnt vmcnt(3)\n"
    "v_max3_u32 v7, v7, v144, v145\n"
    "v_max3_u32 v7, v7, v146, v147\n"
    "s_waitcnt vmcnt(2)\n"
    "v_max3_u32 v7, v7, v148, v149\n"
    "v_max3_u32 v7, v7, v150, v151\n"
    "s_waitcnt vmcnt(1)\n"
    "v_max3_u32 v7, v7, v152, v153\n"
    "v_max3_u32 v7, v7, v154, v155\n"
    "s_waitcnt vmcnt(0)\n"
    "v_max3_u32 v7, v7, v156, v157\n"
    "v_max3_u32 v7, v7, v158, v159\n"
    "v_cmp_eq_u32 vcc, -1, v7\n"
    "s_nop 0\n"
    "s_cbranch_vccz Lgo45\n"
    "s_cmp_eq_u32 s61, 0\n"
    "s_cbranch_scc1 Lgo45\n"
    "s_sub_u32 s61, s61, 1\n"
    "s_branch Lver45\n"
    "Lgo45:\n"
    "s_branch Lgq45\n"
    "Lbad50:\n"
    "s_cmp_eq_u32 s28, 99\n"
    "s_cbranch_scc1 Lok50\n"
    "s_waitcnt vmcnt(0) lgkmcnt(0)\n"
    "s_nop 15\n"
    "Lag49:\n"
    "s_cmp_eq_u32 s28, 98\n"
    "s_mov_b32 s28, 98\n"
    "s_cbranch_scc1 Lver49\n"
    "s_add_u32 s71, s84, 20975616\n"
    "buffer_load_dwordx4 v[128:131], v0, s[8:11], s71 offen offset:0 sc1\n"
    "buffer_load_dwordx4 v[132:135], v0, s[8:11], s71 offen offset:1024 sc1\n"
    "buffer_load_dwordx4 v[136:139], v0, s[8:11], s71 offen offset:2048 sc1\n"
    "buffer_load_dwordx4 v[140:143], v0, s[8:11], s71 offen offset:3072 sc1\n"
    "s_add_u32 s71, s84, 20979712\n"
    "buffer_load_dwordx4 v[144:147], v0, s[8:11], s71 offen offset:0 sc1\n"
    "buffer_load_dwordx4 v[148:151], v0, s[8:11], s71 offen offset:1024 sc1\n"
    "buffer_load_dwordx4 v[152:155], v0, s[8:11], s71 offen offset:2048 sc1\n"
    "buffer_load_dwordx4 v[156:159], v0, s[8:11], s71 offen offset:3072 sc1\n"
    "s_add_u32 s71, s84, 20983808\n"
    "buffer_load_dwordx4 v[160:163], v0, s[8:11], s71 offen offset:0 sc1\n"
    "buffer_load_dwordx4 v[164:167], v0, s[8:11], s71 offen offset:1024 sc1\n"
    "Lgq49:\n"
    "ds_read_b64_tr_b16 v[48:49], v12 offset:0\n"
    "ds_read_b64_tr_b16 v[50:51], v12 offset:1024\n"
    "ds_read_b64_tr_b16 v[52:53], v13 offset:0\n"
    "ds_read_b64_tr_b16 v[54:55], v13 offset:1024\n"
    "ds_read_b64_tr_b16 v[56:57], v14 offset:0\n"
    "ds_read_b64_tr_b16 v[58:59], v14 offset:1024\n"
    "ds_read_b64_tr_b16 v[60:61], v15 offset:0\n"
    "ds_read_b64_tr_b16 v[62:63], v15 offset:1024\n"
    "ds_read_b64_tr_b16 v[64:65], v12 offset:4096\n"
    "ds_read_b64_tr_b16 v[66:67], v12 offset:5120\n"
    "ds_read_b64_tr_b16 v[68:69], v13 offset:4096\n"
    "ds_read_b64_tr_b16 v[70:71], v13 offset:5120\n"
    "ds_read_b64_tr_b16 v[72:73], v14 offset:4096\n"
    "ds_read_b64_tr_b16 v[74:75], v14 offset:5120\n"
    "ds_read_b64_tr_b16 v[76:77], v15 offset:4096\n"
    "ds_read_b64_tr_b16 v[78:79], v15 offset:5120\n"
    "ds_read_b64_tr_b16 v[80:81], v12 offset:8192\n"
    "ds_read_b64_tr_b16 v[82:83], v12 offset:9216\n"
    "ds_read_b64_tr_b16 v[84:85], v13 offset:8192\n"
    "ds_read_b64_tr_b16 v[86:87], v13 offset:9216\n"
    "ds_read_b64_tr_b16 v[88:89], v14 offset:8192\n"
    "ds_read_b64_tr_b16 v[90:91], v14 offset:9216\n"
    "ds_read_b64_tr_b16 v[92:93], v15 offset:8192\n"
    "ds_read_b64_tr_b16 v[94:95], v15 offset:9216\n"
    "ds_read_b64_tr_b16 v[96:97], v12 offset:12288\n"
    "ds_read_b64_tr_b16 v[98:99], v12 offset:13312\n"
    "ds_read_b64_tr_b16 v[100:101], v13 offset:12288\n"
    "ds_read_b64_tr_b16 v[102:103], v13 offset:13312\n"
    "ds_read_b64_tr_b16 v[104:105], v14 offset:12288\n"
    "ds_read_b64_tr_b16 v[106:107], v14 offset:13312\n"
    "ds_read_b64_tr_b16 v[108:109], v15 offset:12288\n"
    "ds_read_b64_tr_b16 v[110:111], v15 offset:13312\n"
    "ds_read_b64_tr_b16 v[112:113], v12 offset:16384\n"
    "ds_read_b64_tr_b16 v[114:115], v12 offset:17408\n"
    "ds_read_b64_tr_b16 v[116:117], v13 offset:16384\n"
    "ds_read_b64_tr_b16 v[118:119], v13 offset:17408\n"
    "ds_read_b64_tr_b16 v[120:121], v14 offset:16384\n"
    "ds_read_b64_tr_b16 v[122:123], v14 offset:17408\n"
    "ds_read_b64_tr_b16 v[124:125], v15 offset:16384\n"
    "ds_read_b64_tr_b16 v[126:127], v15 offset:17408\n"
    "ds_read_b64_tr_b16 v[192:193], v12 offset:20480\n"
    "ds_read_b64_tr_b16 v[194:195], v12 offset:21504\n"
    "ds_read_b64_tr_b16 v[196:197], v13 offset:20480\n"
    "ds_read_b64_tr_b16 v[198:199], v13 offset:21504\n"
    "ds_read_b64_tr_b16 v[200:201], v14 offset:20480\n"
    "ds_read_b64_tr_b16 v[202:203], v14 offset:21504\n"
    "ds_read_b64_tr_b16 v[204:205], v15 offset:20480\n"
    "ds_read_b64_tr_b16 v[206:207], v15 offset:21504\n"
    "ds_read_b64_tr_b16 v[208:209], v12 offset:24576\n"
    "ds_read_b64_tr_b16 v[210:211], v12 offset:25600\n"
    "ds_read_b64_tr_b16 v[212:213], v13 offset:24576\n"
    "ds_read_b64_tr_b16 v[214:215], v13 offset:25600\n"
    "ds_read_b64_tr_b16 v[216:217], v14 offset:24576\n"
    "ds_read_b64_tr_b16 v[218:219], v14 offset:25600\n"
    "ds_read_b64_tr_b16 v[220:221], v15 offset:24576\n"
    "ds_read_b64_tr_b16 v[222:223], v15 offset:25600\n"
    "ds_read_b64_tr_b16 v[224:225], v12 offset:28672\n"
    "ds_read_b64_tr_b16 v[226:227], v12 offset:29696\n"
    "ds_read_b64_tr_b16 v[228:229], v13 offset:28672\n"
    "ds_read_b64_tr_b16 v[230:231], v13 offset:29696\n"
    "ds_read_b64_tr_b16 v[232:233], v14 offset:28672\n"
    "ds_read_b64_tr_b16 v[234:235], v14 offset:29696\n"
    "ds_read_b64_tr_b16 v[236:237], v15 offset:28672\n"
    "ds_read_b64_tr_b16 v[238:239], v15 offset:29696\n"
    "ds_read_b64_tr_b16 v[240:241], v12 offset:32768\n"
    "ds_read_b64_tr_b16 v[242:243], v12 offset:33792\n"
    "ds_read_b64_tr_b16 v[244:245], v13 offset:32768\n"
    "ds_read_b64_tr_b16 v[246:247], v13 offset:33792\n"
    "ds_read_b64_tr_b16 v[248:249], v14 offset:32768\n"
    "ds_read_b64_tr_b16 v[250:251], v14 offset:33792\n"
    "ds_read_b64_tr_b16 v[252:253], v15 offset:32768\n"
    "ds_read_b64_tr_b16 v[254:255], v15 offset:33792\n"
    "ds_read_b64_tr_b16 v[32:33], v12 offset:36864\n"
    "ds_read_b64_tr_b16 v[34:35], v12 offset:37888\n"
    "ds_read_b64_tr_b16 v[36:37], v13 offset:36864\n"
    "ds_read_b64_tr_b16 v[38:39], v13 offset:37888\n"
    "ds_read_b64_tr_b16 v[40:41], v14 offset:36864\n"
    "ds_read_b64_tr_b16 v[42:43], v14 offset:37888\n"
    "v_accvgpr_read_b32 v16, a220\n"
    "v_accvgpr_read_b32 v17, a221\n"
    "v_accvgpr_read_b32 v18, a222\n"
    "v_accvgpr_read_b32 v19, a223\n"
    "v_accvgpr_read_b32 v20, a224\n"
    "v_accvgpr_read_b32 v21, a225\n"
    "v_accvgpr_read_b32 v22, a226\n"
    "v_accvgpr_read_b32 v23, a227\n"
    "v_accvgpr_read_b32 v24, a228\n"
    "v_accvgpr_read_b32 v25, a229\n"
    "v_accvgpr_read_b32 v26, a230\n"
    "v_accvgpr_read_b32 v27, a231\n"
    "v_accvgpr_read_b32 v28, a232\n"
    "v_accvgpr_read_b32 v29, a233\n"
    "v_accvgpr_read_b32 v30, a234\n"
    "v_accvgpr_read_b32 v31, a235\n"
    "s_waitcnt vmcnt(0) lgkmcnt(0)\n"
    "s_add_u32 s72, s64, 1839104\n"
    "s_add_u32 m0, s66, 36864\n"
    "s_add_u32 s69, s77, 4096\n"
    "s_add_u32 s71, s84, 20983808\n"
    "s_add_u32 s70, s79, 36864\n"
    "s_branch Lredo49\n"
    "Lver49:\n"
    "s_mov_b32 s28, 99\n"
    "s_add_u32 s71, s84, 20975616\n"
    "buffer_load_dwordx4 v[128:131], v0, s[8:11], s71 offen offset:0 sc1\n"
    "buffer_load_dwordx4 v[132:135], v0, s[8:11], s71 offen offset:1024 sc1\n"
    "buffer_load_dwordx4 v[136:139], v0, s[8:11], s71 offen offset:2048 sc1\n"
    "buffer_load_dwordx4 v[140:143], v0, s[8:11], s71 offen offset:3072 sc1\n"
    "s_add_u32 s71, s84, 20979712\n"
    "buffer_load_dwordx4 v[144:147], v0, s[8:11], s71 offen offset:0 sc1\n"
    "buffer_load_dwordx4 v[148:151], v0, s[8:11], s71 offen offset:1024 sc1\n"
    "buffer_load_dwordx4 v[152:155], v0, s[8:11], s71 offen offset:2048 sc1\n"
    "buffer_load_dwordx4 v[156:159], v0, s[8:11], s71 offen offset:3072 sc1\n"
    "s_add_u32 s71, s84, 20983808\n"
    "buffer_load_dwordx4 v[160:163], v0, s[8:11], s71 offen offset:0 sc1\n"
    "buffer_load_dwordx4 v[164:167], v0, s[8:11], s71 offen offset:1024 sc1\n"
    "buffer_load_dwordx4 v[168:171], v0, s[8:11], s71 offen offset:2048 sc1\n"
    "buffer_load_dwordx4 v[172:175], v0, s[8:11], s71 offen offset:3072 sc1\n"
    "s_add_u32 s71, s84, 20987904\n"
    "buffer_load_dwordx4 v[176:179], v0, s[8:11], s71 offen offset:0 sc1\n"
    "buffer_load_dwordx4 v[180:183], v0, s[8:11], s71 offen offset:1024 sc1\n"
    "buffer_load_dwordx4 v[184:187], v0, s[8:11], s71 offen offset:2048 sc1\n"
    "buffer_load_dwordx4 v[188:191], v0, s[8:11], s71 offen offset:3072 sc1\n"
    "s_waitcnt vmcnt(15)\n"
    "v_max3_u32 v7, v128, v129, v130\n"
    "v_max_u32 v7, v7, v131\n"
    "s_waitcnt vmcnt(14)\n"
    "v_max3_u32 v7, v7, v132, v133\n"
    "v_max3_u32 v7, v7, v134, v135\n"
    "s_waitcnt vmcnt(13)\n"
    "v_max3_u32 v7, v7, v136, v137\n"
    "v_max3_u32 v7, v7, v138, v139\n"
    "s_waitcnt vmcnt(12)\n"
    "v_max3_u32 v7, v7, v140, v141\n"
    "v_max3_u32 v7, v7, v142, v143\n"
    "s_waitcnt vmcnt(11)\n"
    "v_max3_u32 v7, v7, v144, v145\n"
    "v_max3_u32 v7, v7, v146, v147\n"
    "s_waitcnt vmcnt(10)\n"
    "v_max3_u32 v7, v7, v148, v149\n"
    "v_max3_u32 v7, v7, v150, v151\n"
    "s_waitcnt vmcnt(9)\n"
    "v_max3_u32 v7, v7, v152, v153\n"
    "v_max3_u32 v7, v7, v154, v155\n"
    "s_waitcnt vmcnt(8)\n"
    "v_max3_u32 v7, v7, v156, v157\n"
    "v_max3_u32 v7, v7, v158, v159\n"
    "s_waitcnt vmcnt(7)\n"
    "v_max3_u32 v7, v7, v160, v161\n"
    "v_max3_u32 v7, v7, v162, v163\n"
    "s_waitcnt vmcnt(6)\n"
    "v_max3_u32 v7, v7, v164, v165\n"
    "v_max3_u32 v7, v7, v166, v167\n"
    "s_waitcnt vmcnt(5)\n"
    "v_max3_u32 v7, v7, v168, v169\n"
    "v_max3_u32 v7, v7, v170, v171\n"
    "s_waitcnt vmcnt(4)\n"
    "v_max3_u32 v7, v7, v172, v173\n"
    "v_max3_u32 v7, v7, v174, v175\n"
    "s_waitcnt vmcnt(3)\n"
    "v_max3_u32 v7, v7, v176, v177\n"
    "v_max3_u32 v7, v7, v178, v179\n"
    "s_waitcnt vmcnt(2)\n"
    "v_max3_u32 v7, v7, v180, v181\n"
    "v_max3_u32 v7, v7, v182, v183\n"
    "s_waitcnt vmcnt(1)\n"
    "v_max3_u32 v7, v7, v184, v185\n"
    "v_max3_u32 v7, v7, v186, v187\n"
    "s_waitcnt vmcnt(0)\n"
    "v_max3_u32 v7, v7, v188, v189\n"
    "v_max3_u32 v7, v7, v190, v191\n"
    "v_cmp_eq_u32 vcc, -1, v7\n"
    "s_nop 0\n"
    "s_cbranch_vccz Lgo49\n"
    "s_cmp_eq_u32 s61, 0\n"
    "s_cbranch_scc1 Lgo49\n"
    "s_sub_u32 s61, s61, 1\n"
    "s_branch Lver49\n"
    "Lgo49:\n"
    "s_branch Lgq49\n"
    "Lbad55:\n"
    "s_cmp_eq_u32 s28, 109\n"
    "s_cbranch_scc1 Lok55\n"
    "s_waitcnt vmcnt(0) lgkmcnt(0)\n"
    "s_nop 15\n"
    "Lag54:\n"
    "s_cmp_eq_u32 s28, 108\n"
    "s_mov_b32 s28, 108\n"
    "s_cbranch_scc1 Lver54\n"
    "s_add_u32 s71, s80, 16650240\n"
    "buffer_load_dwordx4 v[128:131], v0, s[8:11], s71 offen offset:0 sc1\n"
    "buffer_load_dwordx4 v[132:135], v0, s[8:11], s71 offen offset:1024 sc1\n"
    "buffer_load_dwordx4 v[136:139], v0, s[8:11], s71 offen offset:2048 sc1\n"
    "buffer_load_dwordx4 v[140:143], v0, s[8:11], s71 offen offset:3072 sc1\n"
    "s_add_u32 s71, s80, 16654336\n"
    "buffer_load_dwordx4 v[144:147], v0, s[8:11], s71 offen offset:0 sc1\n"
    "buffer_load_dwordx4 v[148:151], v0, s[8:11], s71 offen offset:1024 sc1\n"
    "buffer_load_dwordx4 v[152:155], v0, s[8:11], s71 offen offset:2048 sc1\n"
    "buffer_load_dwordx4 v[156:159], v0, s[8:11], s71 offen offset:3072 sc1\n"
    "s_add_u32 s71, s80, 16658432\n"
    "buffer_load_dwordx4 v[160:163], v0, s[8:11], s71 offen offset:0 sc1\n"
    "buffer_load_dwordx4 v[164:167], v0, s[8:11], s71 offen offset:1024 sc1\n"
    "Lgq54:\n"
    "s_waitcnt vmcnt(0) lgkmcnt(0)\n"
    "s_add_u32 s72, s64, 1839104\n"
    "s_add_u32 m0, s66, 69632\n"
    "s_add_u32 s69, s77, 36864\n"
    "s_add_u32 s71, s80, 16658432\n"
    "s_add_u32 s70, s79, 36864\n"
    "s_branch Lredo54\n"
    "Lver54:\n"
    "s_mov_b32 s28, 109\n"
    "s_add_u32 s71, s80, 16650240\n"
    "buffer_load_dwordx4 v[128:131], v0, s[8:11], s71 offen offset:0 sc1\n"
    "buffer_load_dwordx4 v[132:135], v0, s[8:11], s71 offen offset:1024 sc1\n"
    "buffer_load_dwordx4 v[136:139], v0, s[8:11], s71 offen offset:2048 sc1\n"
    "buffer_load_dwordx4 v[140:143], v0, s[8:11], s71 offen offset:3072 sc1\n"
    "s_add_u32 s71, s80, 16654336\n"
    "buffer_load_dwordx4 v[144:147], v0, s[8:11], s71 offen offset:0 sc1\n"
    "buffer_load_dwordx4 v[148:151], v0, s[8:11], s71 offen offset:1024 sc1\n"
    "buffer_load_dwordx4 v[152:155], v0, s[8:11], s71 offen offset:2048 sc1\n"
    "buffer_load_dwordx4 v[156:159], v0, s[8:11], s71 offen offset:3072 sc1\n"
    "s_add_u32 s71, s80, 16658432\n"
    "buffer_load_dwordx4 v[160:163], v0, s[8:11], s71 offen offset:0 sc1\n"
    "buffer_load_dwordx4 v[164:167], v0, s[8:11], s71 offen offset:1024 sc1\n"
    "buffer_load_dwordx4 v[168:171], v0, s[8:11], s71 offen offset:2048 sc1\n"
    "buffer_load_dwordx4 v[172:175], v0, s[8:11], s71 offen offset:3072 sc1\n"
    "s_add_u32 s71, s80, 16662528\n"
    "buffer_load_dwordx4 v[176:179], v0, s[8:11], s71 offen offset:0 sc1\n"
    "buffer_load_dwordx4 v[180:183], v0, s[8:11], s71 offen offset:1024 sc1\n"
    "buffer_load_dwordx4 v[184:187], v0, s[8:11], s71 offen offset:2048 sc1\n"
    "buffer_load_dwordx4 v[188:191], v0, s[8:11], s71 offen offset:3072 sc1\n"
    "s_add_u32 s72, s80, 16683008\n"
    "buffer_load_dwordx4 v[100:103], v0, s[8:11], s72 offen offset:0 sc1\n"
    "s_add_u32 s71, s80, 16666624\n"
    "buffer_load_dwordx4 v[192:195], v0, s[8:11], s71 offen offset:0 sc1\n"
    "buffer_load_dwordx4 v[196:199], v0, s[8:11], s71 offen offset:1024 sc1\n"
    "buffer_load_dwordx4 v[104:107], v0, s[8:11], s72 offen offset:1024 sc1\n"
    "buffer_load_dwordx4 v[200:203], v0, s[8:11], s71 offen offset:2048 sc1\n"
    "buffer_load_dwordx4 v[204:207], v0, s[8:11], s71 offen offset:3072 sc1\n"
    "s_add_u32 s71, s80, 16670720\n"
    "buffer_load_dwordx4 v[208:211], v0, s[8:11], s71 offen offset:0 sc1\n"
    "buffer_load_dwordx4 v[212:215], v0, s[8:11], s71 offen offset:1024 sc1\n"
    "buffer_load_dwordx4 v[216:219], v0, s[8:11], s71 offen offset:2048 sc1\n"
    "buffer_load_dwordx4 v[220:223], v0, s[8:11], s71 offen offset:3072 sc1\n"
    "s_add_u32 s71, s80, 16674816\n"
    "buffer_load_dwordx4 v[224:227], v0, s[8:11], s71 offen offset:0 sc1\n"
    "buffer_load_dwordx4 v[228:231], v0, s[8:11], s71 offen offset:1024 sc1\n"
    "buffer_load_dwordx4 v[232:235], v0, s[8:11], s71 offen offset:2048 sc1\n"
    "buffer_load_dwordx4 v[236:239], v0, s[8:11], s71 offen offset:3072 sc1\n"
    "s_add_u32 s71, s80, 16678912\n"
    "buffer_load_dwordx4 v[240:243], v0, s[8:11], s71 offen offset:0 sc1\n"
    "buffer_load_dwordx4 v[244:247], v0, s[8:11], s71 offen offset:1024 sc1\n"
    "buffer_load_dwordx4 v[248:251], v0, s[8:11], s71 offen offset:2048 sc1\n"
    "buffer_load_dwordx4 v[252:255], v0, s[8:11], s71 offen offset:3072 sc1\n"
    "s_waitcnt vmcnt(33)\n"
    "v_max3_u32 v7, v128, v129, v130\n"
    "v_max_u32 v7, v7, v131\n"
    "s_waitcnt vmcnt(32)\n"
    "v_max3_u32 v7, v7, v132, v133\n"
    "v_max3_u32 v7, v7, v134, v135\n"
    "s_waitcnt vmcnt(31)\n"
    "v_max3_u32 v7, v7, v136, v137\n"
    "v_max3_u32 v7, v7, v138, v139\n"
    "s_waitcnt vmcnt(30)\n"
    "v_max3_u32 v7, v7, v140, v141\n"
    "v_max3_u32 v7, v7, v142, v143\n"
    "s_waitcnt vmcnt(29)\n"
    "v_max3_u32 v7, v7, v144, v145\n"
    "v_max3_u32 v7, v7, v146, v147\n"
    "s_waitcnt vmcnt(28)\n"
    "v_max3_u32 v7, v7, v148, v149\n"
    "v_max3_u32 v7, v7, v150, v151\n"
    "s_waitcnt vmcnt(27)\n"
    "v_max3_u32 v7, v7, v152, v153\n"
    "v_max3_u32 v7, v7, v154, v155\n"
    "s_waitcnt vmcnt(26)\n"
    "v_max3_u32 v7, v7, v156, v157\n"
    "v_max3_u32 v7, v7, v158, v159\n"
    "s_waitcnt vmcnt(25)\n"
    "v_max3_u32 v7, v7, v160, v161\n"
    "v_max3_u32 v7, v7, v162, v163\n"
    "s_waitcnt vmcnt(24)\n"
    "v_max3_u32 v7, v7, v164, v165\n"
    "v_max3_u32 v7, v7, v166, v167\n"
    "s_waitcnt vmcnt(23)\n"
    "v_max3_u32 v7, v7, v168, v169\n"
    "v_max3_u32 v7, v7, v170, v171\n"
    "s_waitcnt vmcnt(22)\n"
    "v_max3_u32 v7, v7, v172, v173\n"
    "v_max3_u32 v7, v7, v174, v175\n"
    "s_waitcnt vmcnt(21)\n"
    "v_max3_u32 v7, v7, v176, v177\n"
    "v_max3_u32 v7, v7, v178, v179\n"
    "s_waitcnt vmcnt(20)\n"
    "v_max3_u32 v7, v7, v180, v181\n"
    "v_max3_u32 v7, v7, v182, v183\n"
    "s_waitcnt vmcnt(19)\n"
    "v_max3_u32 v7, v7, v184, v185\n"
    "v_max3_u32 v7, v7, v186, v187\n"
    "s_waitcnt vmcnt(18)\n"
    "v_max3_u32 v7, v7, v188, v189\n"
    "v_max3_u32 v7, v7, v190, v191\n"
    "s_waitcnt vmcnt(17)\n"
    "v_max3_u32 v7, v7, v100, v101\n"
    "v_max3_u32 v7, v7, v102, v103\n"
    "s_waitcnt vmcnt(16)\n"
    "v_max3_u32 v7, v7, v192, v193\n"
    "v_max3_u32 v7, v7, v194, v195\n"
    "s_waitcnt vmcnt(15)\n"
    "v_max3_u32 v7, v7, v196, v197\n"
    "v_max3_u32 v7, v7, v198, v199\n"
    "s_waitcnt vmcnt(14)\n"
    "v_max3_u32 v7, v7, v104, v105\n"
    "v_max3_u32 v7, v7, v106, v107\n"
    "s_waitcnt vmcnt(13)\n"
    "v_max3_u32 v7, v7, v200, v201\n"
    "v_max3_u32 v7, v7, v202, v203\n"
    "s_waitcnt vmcnt(12)\n"
    "v_max3_u32 v7, v7, v204, v205\n"
    "v_max3_u32 v7, v7, v206, v207\n"
    "s_waitcnt vmcnt(11)\n"
    "v_max3_u32 v7, v7, v208, v209\n"
    "v_max3_u32 v7, v7, v210, v211\n"
    "s_waitcnt vmcnt(10)\n"
    "v_max3_u32 v7, v7, v212, v213\n"
    "v_max3_u32 v7, v7, v214, v215\n"
    "s_waitcnt vmcnt(9)\n"
    "v_max3_u32 v7, v7, v216, v217\n"
    "v_max3_u32 v7, v7, v218, v219\n"
    "s_waitcnt vmcnt(8)\n"
    "v_max3_u32 v7, v7, v220, v221\n"
    "v_max3_u32 v7, v7, v222, v223\n"
    "s_waitcnt vmcnt(7)\n"
    "v_max3_u32 v7, v7, v224, v225\n"
    "v_max3_u32 v7, v7, v226, v227\n"
    "s_waitcnt vmcnt(6)\n"
    "v_max3_u32 v7, v7, v228, v229\n"
    "v_max3_u32 v7, v7, v230, v231\n"
    "s_waitcnt vmcnt(5)\n"
    "v_max3_u32 v7, v7, v232, v233\n"
    "v_max3_u32 v7, v7, v234, v235\n"
    "s_waitcnt vmcnt(4)\n"
    "v_max3_u32 v7, v7, v236, v237\n"
    "v_max3_u32 v7, v7, v238, v239\n"
    "s_waitcnt vmcnt(3)\n"
    "v_max3_u32 v7, v7, v240, v241\n"
    "v_max3_u32 v7, v7, v242, v243\n"
    "s_waitcnt vmcnt(2)\n"
    "v_max3_u32 v7, v7, v244, v245\n"
    "v_max3_u32 v7, v7, v246, v247\n"
    "s_waitcnt vmcnt(1)\n"
    "v_max3_u32 v7, v7, v248, v249\n"
    "v_max3_u32 v7, v7, v250, v251\n"
    "s_waitcnt vmcnt(0)\n"
    "v_max3_u32 v7, v7, v252, v253\n"
    "v_max3_u32 v7, v7, v254, v255\n"
    "v_cmp_eq_u32 vcc, -1, v7\n"
    "s_nop 0\n"
    "s_cbranch_vccz Lgo54\n"
    "s_cmp_eq_u32 s61, 0\n"
    "s_cbranch_scc1 Lgo54\n"
    "s_sub_u32 s61, s61, 1\n"
    "s_branch Lver54\n"
    "Lgo54:\n"
    "s_branch Lgq54\n"
    "Lgb56:\n"
    "s_cmp_eq_u32 s61, 0\n"
    "s_cbranch_scc1 Lgo56\n"
    "s_sub_u32 s61, s61, 1\n"
    "s_add_u32 s29, s80, 16650240\n"
    "buffer_load_dwordx4 v[128:131], v0, s[8:11], s29 offen offset:0 sc1\n"
    "s_add_u32 s29, s80, 16650240\n"
    "buffer_load_dwordx4 v[132:135], v0, s[8:11], s29 offen offset:1024 sc1\n"
    "s_waitcnt vmcnt(1)\n"
    "v_max3_u32 v7, v128, v129, v130\n"
    "v_max_u32 v7, v7, v131\n"
    "s_waitcnt vmcnt(0)\n"
    "v_max3_u32 v7, v7, v132, v133\n"
    "v_max3_u32 v7, v7, v134, v135\n"
    "v_cmp_eq_u32 vcc, -1, v7\n"
    "s_nop 0\n"
    "s_cbranch_vccnz Lgb56\n"
    "s_branch Lgo56\n"
    "Lgb57:\n"
    "s_cmp_eq_u32 s61, 0\n"
    "s_cbranch_scc1 Lgo57\n"
    "s_sub_u32 s61, s61, 1\n"
    "s_add_u32 s29, s80, 16650240\n"
    "buffer_load_dwordx4 v[136:139], v0, s[8:11], s29 offen offset:2048 sc1\n"
    "s_add_u32 s29, s80, 16650240\n"
    "buffer_load_dwordx4 v[140:143], v0, s[8:11], s29 offen offset:3072 sc1\n"
    "s_waitcnt vmcnt(1)\n"
    "v_max3_u32 v7, v136, v137, v138\n"
    "v_max_u32 v7, v7, v139\n"
    "s_waitcnt vmcnt(0)\n"
    "v_max3_u32 v7, v7, v140, v141\n"
    "v_max3_u32 v7, v7, v142, v143\n"
    "v_cmp_eq_u32 vcc, -1, v7\n"
    "s_nop 0\n"
    "s_cbranch_vccnz Lgb57\n"
    "s_branch Lgo57\n"
    "Lgb58:\n"
    "s_cmp_eq_u32 s61, 0\n"
    "s_cbranch_scc1 Lgo58\n"
    "s_sub_u32 s61, s61, 1\n"
    "s_add_u32 s29, s80, 16683008\n"
    "buffer_load_dwordx4 v[100:103], v0, s[8:11], s29 offen offset:0 sc1\n"
    "s_add_u32 s29, s80, 16683008\n"
    "buffer_load_dwordx4 v[104:107], v0, s[8:11], s29 offen offset:1024 sc1\n"
    "s_waitcnt vmcnt(1)\n"
    "v_max3_u32 v110, v100, v101, v102\n"
    "v_max_u32 v110, v110, v103\n"
    "s_waitcnt vmcnt(0)\n"
    "v_max3_u32 v110, v110, v104, v105\n"
    "v_max3_u32 v110, v110, v106, v107\n"
    "v_cmp_eq_u32 vcc, -1, v110\n"
    "s_nop 0\n"
    "s_cbranch_vccnz Lgb58\n"
    "s_branch Lgo58\n"
    "Lbad59:\n"
    "s_cmp_eq_u32 s28, 109\n"
    "s_cbranch_scc1 Lok59\n"
    "s_waitcnt vmcnt(0) lgkmcnt(0)\n"
    "s_nop 15\n"
    "s_branch Lag54\n"
    "Lgb63:\n"
    "s_cmp_eq_u32 s61, 0\n"
    "s_cbranch_scc1 Lgo63\n"
    "s_sub_u32 s61, s61, 1\n"
    "s_add_u32 s29, s87, 28848128\n"
    "buffer_load_dwordx4 v[176:179], v0, s[8:11], s29 offen offset:0 sc1\n"
    "s_add_u32 s29, s87, 28848128\n"
    "buffer_load_dwordx4 v[180:183], v0, s[8:11], s29 offen offset:1024 sc1\n"
    "s_add_u32 s29, s88, 28848128\n"
    "buffer_load_dwordx4 v[184:187], v0, s[8:11], s29 offen offset:0 sc1\n"
    "s_add_u32 s29, s88, 28848128\n"
    "buffer_load_dwordx4 v[188:191], v0, s[8:11], s29 offen offset:1024 sc1\n"
    "s_waitcnt vmcnt(3)\n"
    "v_max3_u32 v7, v176, v177, v178\n"
    "v_max_u32 v7, v7, v179\n"
    "s_waitcnt vmcnt(2)\n"
    "v_max3_u32 v7, v7, v180, v181\n"
    "v_max3_u32 v7, v7, v182, v183\n"
    "s_waitcnt vmcnt(1)\n"
    "v_max3_u32 v7, v7, v184, v185\n"
    "v_max3_u32 v7, v7, v186, v187\n"
    "s_waitcnt vmcnt(0)\n"
    "v_max3_u32 v7, v7, v188, v189\n"
    "v_max3_u32 v7, v7, v190, v191\n"
    "v_cmp_eq_u32 vcc, -1, v7\n"
    "s_nop 0\n"
    "s_cbranch_vccnz Lgb63\n"
    "s_branch Lgo63\n"
    "Lgb64:\n"
    "s_cmp_eq_u32 s61, 0\n"
    "s_cbranch_scc1 Lgo64\n"
    "s_sub_u32 s61, s61, 1\n"
    "s_add_u32 s29, s87, 28848128\n"
    "buffer_load_dwordx4 v[192:195], v0, s[8:11], s29 offen offset:2048 sc1\n"
    "s_add_u32 s29, s87, 28848128\n"
    "buffer_load_dwordx4 v[196:199], v0, s[8:11], s29 offen offset:3072 sc1\n"
    "s_add_u32 s29, s88, 28848128\n"
    "buffer_load_dwordx4 v[200:203], v0, s[8:11], s29 offen offset:2048 sc1\n"
    "s_add_u32 s29, s88, 28848128\n"
    "buffer_load_dwordx4 v[204:207], v0, s[8:11], s29 offen offset:3072 sc1\n"
    "s_waitcnt vmcnt(3)\n"
    "v_max3_u32 v7, v192, v193, v194\n"
    "v_max_u32 v7, v7, v195\n"
    "s_waitcnt vmcnt(2)\n"
    "v_max3_u32 v7, v7, v196, v197\n"
    "v_max3_u32 v7, v7, v198, v199\n"
    "s_waitcnt vmcnt(1)\n"
    "v_max3_u32 v7, v7, v200, v201\n"
    "v_max3_u32 v7, v7, v202, v203\n"
    "s_waitcnt vmcnt(0)\n"
    "v_max3_u32 v7, v7, v204, v205\n"
    "v_max3_u32 v7, v7, v206, v207\n"
    "v_cmp_eq_u32 vcc, -1, v7\n"
    "s_nop 0\n"
    "s_cbranch_vccnz Lgb64\n"
    "s_branch Lgo64\n"
    "Lgb65:\n"
    "s_cmp_eq_u32 s61, 0\n"
    "s_cbranch_scc1 Lgo65\n"
    "s_sub_u32 s61, s61, 1\n"
    "s_add_u32 s29, s87, 28852224\n"
    "buffer_load_dwordx4 v[48:51], v0, s[8:11], s29 offen offset:0 sc1\n"
    "s_add_u32 s29, s87, 28852224\n"
    "buffer_load_dwordx4 v[52:55], v0, s[8:11], s29 offen offset:1024 sc1\n"
    "s_add_u32 s29, s88, 28852224\n"
    "buffer_load_dwordx4 v[56:59], v0, s[8:11], s29 offen offset:0 sc1\n"
    "s_add_u32 s29, s88, 28852224\n"
    "buffer_load_dwordx4 v[60:63], v0, s[8:11], s29 offen offset:1024 sc1\n"
    "s_waitcnt vmcnt(3)\n"
    "v_max3_u32 v7, v48, v49, v50\n"
    "v_max_u32 v7, v7, v51\n"
    "s_waitcnt vmcnt(2)\n"
    "v_max3_u32 v7, v7, v52, v53\n"
    "v_max3_u32 v7, v7, v54, v55\n"
    "s_waitcnt vmcnt(1)\n"
    "v_max3_u32 v7, v7, v56, v57\n"
    "v_max3_u32 v7, v7, v58, v59\n"
    "s_waitcnt vmcnt(0)\n"
    "v_max3_u32 v7, v7, v60, v61\n"
    "v_max3_u32 v7, v7, v62, v63\n"
    "v_cmp_eq_u32 vcc, -1, v7\n"
    "s_nop 0\n"
    "s_cbranch_vccnz Lgb65\n"
    "s_branch Lgo65\n"
    "Lgb66:\n"
    "s_cmp_eq_u32 s61, 0\n"
    "s_cbranch_scc1 Lgo66\n"
    "s_sub_u32 s61, s61, 1\n"
    "s_add_u32 s29, s87, 28852224\n"
    "buffer_load_dwordx4 v[112:115], v0, s[8:11], s29 offen offset:3072 sc1\n"
    "s_add_u32 s29, s87, 28856320\n"
    "buffer_load_dwordx4 v[88:91], v0, s[8:11], s29 offen offset:1024 sc1\n"
    "s_add_u32 s29, s87, 28856320\n"
    "buffer_load_dwordx4 v[92:95], v0, s[8:11], s29 offen offset:3072 sc1\n"
    "s_waitcnt vmcnt(2)\n"
    "v_max3_u32 v7, v112, v113, v114\n"
    "v_max_u32 v7, v7, v115\n"
    "s_waitcnt vmcnt(1)\n"
    "v_max3_u32 v7, v7, v88, v89\n"
    "v_max3_u32 v7, v7, v90, v91\n"
    "s_waitcnt vmcnt(0)\n"
    "v_max3_u32 v7, v7, v92, v93\n"
    "v_max3_u32 v7, v7, v94, v95\n"
    "v_cmp_eq_u32 vcc, -1, v7\n"
    "s_nop 0\n"
    "s_cbranch_vccnz Lgb66\n"
    "s_branch Lgo66\n"
    "Lgb67:\n"
    "s_cmp_eq_u32 s61, 0\n"
    "s_cbranch_scc1 Lgo67\n"
    "s_sub_u32 s61, s61, 1\n"
    "s_add_u32 s29, s88, 28852224\n"
    "buffer_load_dwordx4 v[96:99], v0, s[8:11], s29 offen offset:2048 sc1\n"
    "s_add_u32 s29, s88, 28856320\n"
    "buffer_load_dwordx4 v[100:103], v0, s[8:11], s29 offen offset:0 sc1\n"
    "s_add_u32 s29, s88, 28856320\n"
    "buffer_load_dwordx4 v[108:111], v0, s[8:11], s29 offen offset:2048 sc1\n"
    "s_waitcnt vmcnt(2)\n"
    "v_max3_u32 v7, v96, v97, v98\n"
    "v_max_u32 v7, v7, v99\n"
    "s_waitcnt vmcnt(1)\n"
    "v_max3_u32 v7, v7, v100, v101\n"
    "v_max3_u32 v7, v7, v102, v103\n"
    "s_waitcnt vmcnt(0)\n"
    "v_max3_u32 v7, v7, v108, v109\n"
    "v_max3_u32 v7, v7, v110, v111\n"
    "v_cmp_eq_u32 vcc, -1, v7\n"
    "s_nop 0\n"
    "s_cbranch_vccnz Lgb67\n"
    "s_branch Lgo67\n"
    "Lgb68:\n"
    "s_cmp_eq_u32 s61, 0\n"
    "s_cbranch_scc1 Lgo68\n"
    "s_sub_u32 s61, s61, 1\n"
    "s_add_u32 s29, s88, 28852224\n"
    "buffer_load_dwordx4 v[116:119], v0, s[8:11], s29 offen offset:3072 sc1\n"
    "s_add_u32 s29, s88, 28856320\n"
    "buffer_load_dwordx4 v[120:123], v0, s[8:11], s29 offen offset:1024 sc1\n"
    "s_add_u32 s29, s88, 28856320\n"
    "buffer_load_dwordx4 v[124:127], v0, s[8:11], s29 offen offset:3072 sc1\n"
    "s_waitcnt vmcnt(2)\n"
    "v_max3_u32 v7, v116, v117, v118\n"
    "v_max_u32 v7, v7, v119\n"
    "s_waitcnt vmcnt(1)\n"
    "v_max3_u32 v7, v7, v120, v121\n"
    "v_max3_u32 v7, v7, v122, v123\n"
    "s_waitcnt vmcnt(0)\n"
    "v_max3_u32 v7, v7, v124, v125\n"
    "v_max3_u32 v7, v7, v126, v127\n"
    "v_cmp_eq_u32 vcc, -1, v7\n"
    "s_nop 0\n"
    "s_cbranch_vccnz Lgb68\n"
    "s_branch Lgo68\n"
    "Lgb69:\n"
    "s_cmp_eq_u32 s61, 0\n"
    "s_cbranch_scc1 Lgo69\n"
    "s_sub_u32 s61, s61, 1\n"
    "s_add_u32 s29, s87, 28852224\n"
    "buffer_load_dwordx4 v[80:83], v0, s[8:11], s29 offen offset:2048 sc1\n"
    "s_add_u32 s29, s87, 28856320\n"
    "buffer_load_dwordx4 v[84:87], v0, s[8:11], s29 offen offset:0 sc1\n"
    "s_add_u32 s29, s87, 28856320\n"
    "buffer_load_dwordx4 v[104:107], v0, s[8:11], s29 offen offset:2048 sc1\n"
    "s_waitcnt vmcnt(2)\n"
    "v_max3_u32 v7, v80, v81, v82\n"
    "v_max_u32 v7, v7, v83\n"
    "s_waitcnt vmcnt(1)\n"
    "v_max3_u32 v7, v7, v84, v85\n"
    "v_max3_u32 v7, v7, v86, v87\n"
    "s_waitcnt vmcnt(0)\n"
    "v_max3_u32 v7, v7, v104, v105\n"
    "v_max3_u32 v7, v7, v106, v107\n"
    "v_cmp_eq_u32 vcc, -1, v7\n"
    "s_nop 0\n"
    "s_cbranch_vccnz Lgb69\n"
    "s_branch Lgo69\n"
    "Lbad70:\n"
    "s_cmp_eq_u32 s28, 125\n"
    "s_cbranch_scc1 Lok70\n"
    "s_waitcnt vmcnt(0) lgkmcnt(0)\n"
    "s_nop 15\n"
    "Lag62:\n"
    "s_cmp_eq_u32 s28, 124\n"
    "s_mov_b32 s28, 124\n"
    "s_cbranch_scc1 Lver62\n"
    "s_add_u32 s71, s87, 28860416\n"
    "s_add_u32 s71, s71, s89\n"
    "buffer_load_dwordx4 v[16:19], v0, s[8:11], s71 offen offset:0 sc1\n"
    "buffer_load_dwordx4 v[20:23], v0, s[8:11], s71 offen offset:1024 sc1\n"
    "s_add_u32 s72, s88, 28860416\n"
    "s_add_u32 s72, s72, s89\n"
    "buffer_load_dwordx4 v[24:27], v0, s[8:11], s72 offen offset:0 sc1\n"
    "buffer_load_dwordx4 v[28:31], v0, s[8:11], s72 offen offset:1024 sc1\n"
    "s_add_u32 s71, s87, 28839936\n"
    "buffer_load_dwordx4 v[128:131], v0, s[8:11], s71 offen offset:0 sc1\n"
    "buffer_load_dwordx4 v[132:135], v0, s[8:11], s71 offen offset:1024 sc1\n"
    "s_add_u32 s72, s88, 28839936\n"
    "buffer_load_dwordx4 v[136:139], v0, s[8:11], s72 offen offset:0 sc1\n"
    "buffer_load_dwordx4 v[140:143], v0, s[8:11], s72 offen offset:1024 sc1\n"
    "buffer_load_dwordx4 v[64:67], v0, s[8:11], s71 offen offset:2048 sc1\n"
    "buffer_load_dwordx4 v[68:71], v0, s[8:11], s71 offen offset:3072 sc1\n"
    "buffer_load_dwordx4 v[72:75], v0, s[8:11], s72 offen offset:2048 sc1\n"
    "buffer_load_dwordx4 v[76:79], v0, s[8:11], s72 offen offset:3072 sc1\n"
    "s_add_u32 s71, s87, 28844032\n"
    "buffer_load_dwordx4 v[144:147], v0, s[8:11], s71 offen offset:0 sc1\n"
    "buffer_load_dwordx4 v[148:151], v0, s[8:11], s71 offen offset:1024 sc1\n"
    "s_add_u32 s72, s88, 28844032\n"
    "buffer_load_dwordx4 v[152:155], v0, s[8:11], s72 offen offset:0 sc1\n"
    "buffer_load_dwordx4 v[156:159], v0, s[8:11], s72 offen offset:1024 sc1\n"
    "buffer_load_dwordx4 v[160:163], v0, s[8:11], s71 offen offset:2048 sc1\n"
    "buffer_load_dwordx4 v[164:167], v0, s[8:11], s71 offen offset:3072 sc1\n"
    "Lgq62:\n"
    "s_waitcnt vmcnt(0) lgkmcnt(0)\n"
    "s_add_u32 s72, s88, 28844032\n"
    "s_add_u32 m0, s66, 36864\n"
    "s_add_u32 s69, s77, 135168\n"
    "s_add_u32 s71, s87, 28844032\n"
    "s_add_u32 s70, s79, 36864\n"
    "s_branch Lredo62\n"
    "Lver62:\n"
    "s_mov_b32 s28, 125\n"
    "s_add_u32 s71, s87, 28860416\n"
    "s_add_u32 s71, s71, s89\n"
    "buffer_load_dwordx4 v[16:19], v0, s[8:11], s71 offen offset:0 sc1\n"
    "buffer_load_dwordx4 v[20:23], v0, s[8:11], s71 offen offset:1024 sc1\n"
    "s_add_u32 s72, s88, 28860416\n"
    "s_add_u32 s72, s72, s89\n"
    "buffer_load_dwordx4 v[24:27], v0, s[8:11], s72 offen offset:0 sc1\n"
    "buffer_load_dwordx4 v[28:31], v0, s[8:11], s72 offen offset:1024 sc1\n"
    "s_add_u32 s71, s87, 28839936\n"
    "buffer_load_dwordx4 v[128:131], v0, s[8:11], s71 offen offset:0 sc1\n"
    "buffer_load_dwordx4 v[132:135], v0, s[8:11], s71 offen offset:1024 sc1\n"
    "s_add_u32 s72, s88, 28839936\n"
    "buffer_load_dwordx4 v[136:139], v0, s[8:11], s72 offen offset:0 sc1\n"
    "buffer_load_dwordx4 v[140:143], v0, s[8:11], s72 offen offset:1024 sc1\n"
    "buffer_load_dwordx4 v[64:67], v0, s[8:11], s71 offen offset:2048 sc1\n"
    "buffer_load_dwordx4 v[68:71], v0, s[8:11], s71 offen offset:3072 sc1\n"
    "buffer_load_dwordx4 v[72:75], v0, s[8:11], s72 offen offset:2048 sc1\n"
    "buffer_load_dwordx4 v[76:79], v0, s[8:11], s72 offen offset:3072 sc1\n"
    "s_add_u32 s71, s87, 28844032\n"
    "buffer_load_dwordx4 v[144:147], v0, s[8:11], s71 offen offset:0 sc1\n"
    "buffer_load_dwordx4 v[148:151], v0, s[8:11], s71 offen offset:1024 sc1\n"
    "s_add_u32 s72, s88, 28844032\n"
    "buffer_load_dwordx4 v[152:155], v0, s[8:11], s72 offen offset:0 sc1\n"
    "buffer_load_dwordx4 v[156:159], v0, s[8:11], s72 offen offset:1024 sc1\n"
    "buffer_load_dwordx4 v[160:163], v0, s[8:11], s71 offen offset:2048 sc1\n"
    "buffer_load_dwordx4 v[164:167], v0, s[8:11], s71 offen offset:3072 sc1\n"
    "buffer_load_dwordx4 v[168:171], v0, s[8:11], s72 offen offset:2048 sc1\n"
    "buffer_load_dwordx4 v[172:175], v0, s[8:11], s72 offen offset:3072 sc1\n"
    "s_add_u32 s71, s87, 28848128\n"
    "buffer_load_dwordx4 v[176:179], v0, s[8:11], s71 offen offset:0 sc1\n"
    "buffer_load_dwordx4 v[180:183], v0, s[8:11], s71 offen offset:1024 sc1\n"
    "s_add_u32 s72, s88, 28848128\n"
    "buffer_load_dwordx4 v[184:187], v0, s[8:11], s72 offen offset:0 sc1\n"
    "buffer_load_dwordx4 v[188:191], v0, s[8:11], s72 offen offset:1024 sc1\n"
    "buffer_load_dwordx4 v[192:195], v0, s[8:11], s71 offen offset:2048 sc1\n"
    "buffer_load_dwordx4 v[196:199], v0, s[8:11], s71 offen offset:3072 sc1\n"
    "buffer_load_dwordx4 v[200:203], v0, s[8:11], s72 offen offset:2048 sc1\n"
    "buffer_load_dwordx4 v[204:207], v0, s[8:11], s72 offen offset:3072 sc1\n"
    "s_add_u32 s71, s87, 28852224\n"
    "buffer_load_dwordx4 v[48:51], v0, s[8:11], s71 offen offset:0 sc1\n"
    "buffer_load_dwordx4 v[52:55], v0, s[8:11], s71 offen offset:1024 sc1\n"
    "s_add_u32 s72, s88, 28852224\n"
    "buffer_load_dwordx4 v[56:59], v0, s[8:11], s72 offen offset:0 sc1\n"
    "buffer_load_dwordx4 v[60:63], v0, s[8:11], s72 offen offset:1024 sc1\n"
    "buffer_load_dwordx4 v[112:115], v0, s[8:11], s71 offen offset:3072 sc1\n"
    "s_add_u32 s71, s87, 28856320\n"
    "buffer_load_dwordx4 v[88:91], v0, s[8:11], s71 offen offset:1024 sc1\n"
    "buffer_load_dwordx4 v[92:95], v0, s[8:11], s71 offen offset:3072 sc1\n"
    "buffer_load_dwordx4 v[96:99], v0, s[8:11], s72 offen offset:2048 sc1\n"
    "s_add_u32 s72, s88, 28856320\n"
    "buffer_load_dwordx4 v[100:103], v0, s[8:11], s72 offen offset:0 sc1\n"
    "buffer_load_dwordx4 v[108:111], v0, s[8:11], s72 offen offset:2048 sc1\n"
    "s_add_u32 s72, s88, 28852224\n"
    "buffer_load_dwordx4 v[116:119], v0, s[8:11], s72 offen offset:3072 sc1\n"
    "s_add_u32 s72, s88, 28856320\n"
    "buffer_load_dwordx4 v[120:123], v0, s[8:11], s72 offen offset:1024 sc1\n"
    "buffer_load_dwordx4 v[124:127], v0, s[8:11], s72 offen offset:3072 sc1\n"
    "s_add_u32 s71, s87, 28852224\n"
    "buffer_load_dwordx4 v[80:83], v0, s[8:11], s71 offen offset:2048 sc1\n"
    "s_add_u32 s71, s87, 28856320\n"
    "buffer_load_dwordx4 v[84:87], v0, s[8:11], s71 offen offset:0 sc1\n"
    "buffer_load_dwordx4 v[104:107], v0, s[8:11], s71 offen offset:2048 sc1\n"
    "s_waitcnt vmcnt(43)\n"
    "v_max3_u32 v7, v16, v17, v18\n"
    "v_max_u32 v7, v7, v19\n"
    "s_waitcnt vmcnt(42)\n"
    "v_max3_u32 v7, v7, v20, v21\n"
    "v_max3_u32 v7, v7, v22, v23\n"
    "s_waitcnt vmcnt(41)\n"
    "v_max3_u32 v7, v7, v24, v25\n"
    "v_max3_u32 v7, v7, v26, v27\n"
    "s_waitcnt vmcnt(40)\n"
    "v_max3_u32 v7, v7, v28, v29\n"
    "v_max3_u32 v7, v7, v30, v31\n"
    "s_waitcnt vmcnt(39)\n"
    "v_max3_u32 v7, v7, v128, v129\n"
    "v_max3_u32 v7, v7, v130, v131\n"
    "s_waitcnt vmcnt(38)\n"
    "v_max3_u32 v7, v7, v132, v133\n"
    "v_max3_u32 v7, v7, v134, v135\n"
    "s_waitcnt vmcnt(37)\n"
    "v_max3_u32 v7, v7, v136, v137\n"
    "v_max3_u32 v7, v7, v138, v139\n"
    "s_waitcnt vmcnt(36)\n"
    "v_max3_u32 v7, v7, v140, v141\n"
    "v_max3_u32 v7, v7, v142, v143\n"
    "s_waitcnt vmcnt(35)\n"
    "v_max3_u32 v7, v7, v64, v65\n"
    "v_max3_u32 v7, v7, v66, v67\n"
    "s_waitcnt vmcnt(34)\n"
    "v_max3_u32 v7, v7, v68, v69\n"
    "v_max3_u32 v7, v7, v70, v71\n"
    "s_waitcnt vmcnt(33)\n"
    "v_max3_u32 v7, v7, v72, v73\n"
    "v_max3_u32 v7, v7, v74, v75\n"
    "s_waitcnt vmcnt(32)\n"
    "v_max3_u32 v7, v7, v76, v77\n"
    "v_max3_u32 v7, v7, v78, v79\n"
    "s_waitcnt vmcnt(31)\n"
    "v_max3_u32 v7, v7, v144, v145\n"
    "v_max3_u32 v7, v7, v146, v147\n"
    "s_waitcnt vmcnt(30)\n"
    "v_max3_u32 v7, v7, v148, v149\n"
    "v_max3_u32 v7, v7, v150, v151\n"
    "s_waitcnt vmcnt(29)\n"
    "v_max3_u32 v7, v7, v152, v153\n"
    "v_max3_u32 v7, v7, v154, v155\n"
    "s_waitcnt vmcnt(28)\n"
    "v_max3_u32 v7, v7, v156, v157\n"
    "v_max3_u32 v7, v7, v158, v159\n"
    "s_waitcnt vmcnt(27)\n"
    "v_max3_u32 v7, v7, v160, v161\n"
    "v_max3_u32 v7, v7, v162, v163\n"
    "s_waitcnt vmcnt(26)\n"
    "v_max3_u32 v7, v7, v164, v165\n"
    "v_max3_u32 v7, v7, v166, v167\n"
    "s_waitcnt vmcnt(25)\n"
    "v_max3_u32 v7, v7, v168, v169\n"
    "v_max3_u32 v7, v7, v170, v171\n"
    "s_waitcnt vmcnt(24)\n"
    "v_max3_u32 v7, v7, v172, v173\n"
    "v_max3_u32 v7, v7, v174, v175\n"
    "s_waitcnt vmcnt(23)\n"
    "v_max3_u32 v7, v7, v176, v177\n"
    "v_max3_u32 v7, v7, v178, v179\n"
    "s_waitcnt vmcnt(22)\n"
    "v_max3_u32 v7, v7, v180, v181\n"
    "v_max3_u32 v7, v7, v182, v183\n"
    "s_waitcnt vmcnt(21)\n"
    "v_max3_u32 v7, v7, v184, v185\n"
    "v_max3_u32 v7, v7, v186, v187\n"
    "s_waitcnt vmcnt(20)\n"
    "v_max3_u32 v7, v7, v188, v189\n"
    "v_max3_u32 v7, v7, v190, v191\n"
    "s_waitcnt vmcnt(19)\n"
    "v_max3_u32 v7, v7, v192, v193\n"
    "v_max3_u32 v7, v7, v194, v195\n"
    "s_waitcnt vmcnt(18)\n"
    "v_max3_u32 v7, v7, v196, v197\n"
    "v_max3_u32 v7, v7, v198, v199\n"
    "s_waitcnt vmcnt(17)\n"
    "v_max3_u32 v7, v7, v200, v201\n"
    "v_max3_u32 v7, v7, v202, v203\n"
    "s_waitcnt vmcnt(16)\n"
    "v_max3_u32 v7, v7, v204, v205\n"
    "v_max3_u32 v7, v7, v206, v207\n"
    "s_waitcnt vmcnt(15)\n"
    "v_max3_u32 v7, v7, v48, v49\n"
    "v_max3_u32 v7, v7, v50, v51\n"
    "s_waitcnt vmcnt(14)\n"
    "v_max3_u32 v7, v7, v52, v53\n"
    "v_max3_u32 v7, v7, v54, v55\n"
    "s_waitcnt vmcnt(13)\n"
    "v_max3_u32 v7, v7, v56, v57\n"
    "v_max3_u32 v7, v7, v58, v59\n"
    "s_waitcnt vmcnt(12)\n"
    "v_max3_u32 v7, v7, v60, v61\n"
    "v_max3_u32 v7, v7, v62, v63\n"
    "s_waitcnt vmcnt(11)\n"
    "v_max3_u32 v7, v7, v112, v113\n"
    "v_max3_u32 v7, v7, v114, v115\n"
    "s_waitcnt vmcnt(10)\n"
    "v_max3_u32 v7, v7, v88, v89\n"
    "v_max3_u32 v7, v7, v90, v91\n"
    "s_waitcnt vmcnt(9)\n"
    "v_max3_u32 v7, v7, v92, v93\n"
    "v_max3_u32 v7, v7, v94, v95\n"
    "s_waitcnt vmcnt(8)\n"
    "v_max3_u32 v7, v7, v96, v97\n"
    "v_max3_u32 v7, v7, v98, v99\n"
    "s_waitcnt vmcnt(7)\n"
    "v_max3_u32 v7, v7, v100, v101\n"
    "v_max3_u32 v7, v7, v102, v103\n"
    "s_waitcnt vmcnt(6)\n"
    "v_max3_u32 v7, v7, v108, v109\n"
    "v_max3_u32 v7, v7, v110, v111\n"
    "s_waitcnt vmcnt(5)\n"
    "v_max3_u32 v7, v7, v116, v117\n"
    "v_max3_u32 v7, v7, v118, v119\n"
    "s_waitcnt vmcnt(4)\n"
    "v_max3_u32 v7, v7, v120, v121\n"
    "v_max3_u32 v7, v7, v122, v123\n"
    "s_waitcnt vmcnt(3)\n"
    "v_max3_u32 v7, v7, v124, v125\n"
    "v_max3_u32 v7, v7, v126, v127\n"
    "s_waitcnt vmcnt(2)\n"
    "v_max3_u32 v7, v7, v80, v81\n"
    "v_max3_u32 v7, v7, v82, v83\n"
    "s_waitcnt vmcnt(1)\n"
    "v_max3_u32 v7, v7, v84, v85\n"
    "v_max3_u32 v7, v7, v86, v87\n"
    "s_waitcnt vmcnt(0)\n"
    "v_max3_u32 v7, v7, v104, v105\n"
    "v_max3_u32 v7, v7, v106, v107\n"
    "v_cmp_eq_u32 vcc, -1, v7\n"
    "s_nop 0\n"
    "s_cbranch_vccz Lgo62\n"
    "s_cmp_eq_u32 s61, 0\n"
    "s_cbranch_scc1 Lgo62\n"
    "s_sub_u32 s61, s61, 1\n"
    "s_branch Lver62\n"
    "Lgo62:\n"
    "s_branch Lgq62\n"
    "Lend:\n"
    : "+v"(tid) : "s"(__builtin_amdgcn_kernarg_segment_ptr()), "s"(blockIdx.x), "s"((unsigned)(size_t)lds) : "v1", "v2", "v3", "v4", "v5", "v6", "v7", "v8", "v9", "v10", "v11", "v12", "v13", "v14", "v15", "v16", "v17", "v18", "v19", "v20", "v21", "v22", "v23", "v24", "v25", "v26", "v27", "v28", "v29", "v30", "v31", "v32", "v33", "v34", "v35", "v36", "v37", "v38", "v39", "v40", "v41", "v42", "v43", "v44", "v45", "v46", "v47", "v48", "v49", "v50", "v51", "v52", "v53", "v54", "v55", "v56", "v57", "v58", "v59", "v60", "v61", "v62", "v63", "v64", "v65", "v66", "v67", "v68", "v69", "v70", "v71", "v72", "v73", "v74", "v75", "v76", "v77", "v78", "v79", "v80", "v81", "v82", "v83", "v84", "v85", "v86", "v87", "v88", "v89", "v90", "v91", "v92", "v93", "v94", "v95", "v96", "v97", "v98", "v99", "v100", "v101", "v102", "v103", "v104", "v105", "v106", "v107", "v108", "v109", "v110", "v111", "v112", "v113", "v114", "v115", "v116", "v117", "v118", "v119", "v120", "v121", "v122", "v123", "v124", "v125", "v126", "v127", "v128", "v129", "v130", "v131", "v132", "v133", "v134", "v135", "v136", "v137", "v138", "v139", "v140", "v141", "v142", "v143", "v144", "v145", "v146", "v147", "v148", "v149", "v150", "v151", "v152", "v153", "v154", "v155", "v156", "v157", "v158", "v159", "v160", "v161", "v162", "v163", "v164", "v165", "v166", "v167", "v168", "v169", "v170", "v171", "v172", "v173", "v174", "v175", "v176", "v177", "v178", "v179", "v180", "v181", "v182", "v183", "v184", "v185", "v186", "v187", "v188", "v189", "v190", "v191", "v192", "v193", "v194", "v195", "v196", "v197", "v198", "v199", "v200", "v201", "v202", "v203", "v204", "v205", "v206", "v207", "v208", "v209", "v210", "v211", "v212", "v213", "v214", "v215", "v216", "v217", "v218", "v219", "v220", "v221", "v222", "v223", "v224", "v225", "v226", "v227", "v228", "v229", "v230", "v231", "v232", "v233", "v234", "v235", "v236", "v237", "v238", "v239", "v240", "v241", "v242", "v243", "v244", "v245", "v246", "v247", "v248", "v249", "v250", "v251", "v252", "v253", "v254", "v255", "a0", "a1", "a2", "a3", "a4", "a5", "a6", "a7", "a8", "a9", "a10", "a11", "a12", "a13", "a14", "a15", "a16", "a17", "a18", "a19", "a20", "a21", "a22", "a23", "a24", "a25", "a26", "a27", "a28", "a29", "a30", "a31", "a32", "a33", "a34", "a35", "a36", "a37", "a38", "a39", "a40", "a41", "a42", "a43", "a44", "a45", "a46", "a47", "a48", "a49", "a50", "a51", "a52", "a53", "a54", "a55", "a56", "a57", "a58", "a59", "a60", "a61", "a62", "a63", "a64", "a65", "a66", "a67", "a68", "a69", "a70", "a71", "a72", "a73", "a74", "a75", "a76", "a77", "a78", "a79", "a80", "a81", "a82", "a83", "a84", "a85", "a86", "a87", "a88", "a89", "a90", "a91", "a92", "a93", "a94", "a95", "a96", "a97", "a98", "a99", "a100", "a101", "a102", "a103", "a104", "a105", "a106", "a107", "a108", "a109", "a110", "a111", "a112", "a113", "a114", "a115", "a116", "a117", "a118", "a119", "a120", "a121", "a122", "a123", "a124", "a125", "a126", "a127", "a128", "a129", "a130", "a131", "a132", "a133", "a134", "a135", "a136", "a137", "a138", "a139", "a140", "a141", "a142", "a143", "a144", "a145", "a146", "a147", "a148", "a149", "a150", "a151", "a152", "a153", "a154", "a155", "a156", "a157", "a158", "a159", "a160", "a161", "a162", "a163", "a164", "a165", "a166", "a167", "a168", "a169", "a170", "a171", "a172", "a173", "a174", "a175", "a176", "a177", "a178", "a179", "a180", "a181", "a182", "a183", "a184", "a185", "a186", "a187", "a188", "a189", "a190", "a191", "a192", "a193", "a194", "a195", "a196", "a197", "a198", "a199", "a200", "a201", "a202", "a203", "a204", "a205", "a206", "a207", "a208", "a209", "a210", "a211", "a212", "a213", "a214", "a215", "a216", "a217", "a218", "a219", "a220", "a221", "a222", "a223", "a224", "a225", "a226", "a227", "a228", "a229", "a230", "a231", "a232", "a233", "a234", "a235", "a236", "a237", "a238", "a239", "a240", "a241", "a242", "a243", "a244", "a245", "a246", "a247", "a248", "a249", "a250", "a251", "a252", "a253", "a254", "a255", "s4", "s5", "s6", "s7", "s8", "s9", "s10", "s11", "s12", "s13", "s14", "s15", "s16", "s17", "s18", "s19", "s20", "s21", "s22", "s23", "s24", "s25", "s26", "s27", "s28", "s29", "s30", "s31", "s34", "s35", "s36", "s37", "s38", "s39", "s40", "s41", "s42", "s43", "s44", "s45", "s46", "s47", "s48", "s49", "s50", "s51", "s52", "s53", "s54", "s55", "s56", "s57", "s58", "s59", "s60", "s61", "s62", "s63", "s64", "s65", "s66", "s67", "s68", "s69", "s70", "s71", "s72", "s73", "s74", "s75", "s76", "s77", "s78", "s79", "s80", "s81", "s82", "s83", "s84", "s85", "s86", "s87", "s88", "s89", "s90", "s91", "s92", "s93", "s94", "s95", "s96", "s97", "s98", "s99", "vcc", "memory");
}

extern "C" void kernel_launch(void* const* d_in, const int* in_sizes, int n_in, void* d_out, int out_size, void* d_ws, size_t ws_size,
                              hipStream_t stream) {
  k_main<<<256, 256, 0, stream>>>(d_in[0], d_in[1], d_in[2], d_in[3], d_in[5], d_in[6], d_in[7], d_out, d_ws);
}
```
